```python
import torch

def _softmax(x, axis=-1):
    return torch.softmax(x, dim=axis)
def _gelu(x):
    return torch.nn.functional.gelu(x, approximate="tanh")
def _silu(x):
    return torch.nn.functional.silu(x)
def _sigmoid(x):
    return torch.sigmoid(x)
def _layernorm(x, axis=-1, eps=1e-5):
    m = x.mean(dim=axis, keepdim=True)
    v = x.var(dim=axis, keepdim=True, unbiased=False)
    return (x - m) / torch.sqrt(v + eps)
def _scatter_add(tgt, idx, src, axis=0):
    return tgt.clone().index_add_(axis, idx, src)
def _segment_sum(src, seg, n):
    out = torch.zeros((n,) + src.shape[1:], dtype=src.dtype)
    return out.index_add_(0, seg, src)
def _pad(x, pw):
    flat = tuple(v for pair in reversed(pw) for v in pair)
    return torch.nn.functional.pad(x, flat)

kernel_name = 'gated_mix__softmax_full_mha__linear_kernel_hedgehog__prompt'

def setup_inputs():

    _w = (lambda a: (lambda t: t.to(torch.bfloat16) if t.is_floating_point() else t)(torch.as_tensor(a)))
    def _A_setup():
        B, S, D = 1, 2048, 512
        return {
            "x": _w((lambda *s: torch.randn(*s, dtype=torch.bfloat16) * 0.05)(B, S, D)),
            "wqkv": _w((lambda *s: torch.randn(*s, dtype=torch.bfloat16) * 0.8)(D, 3 * D)),
            "wo": _w((lambda *s: torch.randn(*s, dtype=torch.bfloat16) * 0.05)(D, D)),
        }
    _A_in = _A_setup()

    def _B_setup():
        B, S, D, H = 1, 2048, 512, 32
        Dh = D // H
        return {
            "x": _w((lambda *s: torch.randn(*s, dtype=torch.bfloat16) * 0.05)(B, S, D)),
            "wqkv": _w((lambda *s: torch.randn(*s, dtype=torch.bfloat16) * 1.6)(D, 3 * D)),
            "w_phi": _w((lambda *s: torch.randn(*s, dtype=torch.bfloat16) * 1.6)(H, Dh, Dh) * (Dh ** -0.5)),
            "wo": _w((lambda *s: torch.randn(*s, dtype=torch.bfloat16) * 0.05)(D, D)),
        }
    _B_in = _B_setup()

    return {
        "A_x": _A_in["x"],
        "A_wqkv": _A_in["wqkv"],
        "A_wo": _A_in["wo"],
        "B_x": _B_in["x"],
        "B_wqkv": _B_in["wqkv"],
        "B_w_phi": _B_in["w_phi"],
        "B_wo": _B_in["wo"],
        "gate_w": _w((lambda *s: torch.randn(*s, dtype=torch.bfloat16) * 0.05)(512) * 16.0),
    }

def reference(A_x, A_wqkv, A_wo, B_x, B_wqkv, B_w_phi, B_wo, gate_w):
    def _A_ref(x, wqkv, wo):
        B, S, D = x.shape
        H = 32; Dh = D // H
        qkv = (lambda x, *d: x.permute(*d))((x @ wqkv).reshape(B, S, 3, H, Dh), 2, 0, 3, 1, 4)
        q, k, v = qkv[0], qkv[1], qkv[2]
        scale = Dh ** -0.5
        scores = torch.einsum("bhqd,bhkd->bhqk", q, k) * scale
        p = _softmax(scores, axis=-1)
        o = torch.einsum("bhqk,bhkd->bhqd", p, v)
        return (lambda x, *d: x.permute(*d))(o, 0, 2, 1, 3).reshape(B, S, D) @ wo

    def _B_ref(x, wqkv, w_phi, wo):
        B, S, D = x.shape
        H = 32; Dh = D // H
        qkv = (lambda x, *d: x.permute(*d))((x @ wqkv).reshape(B, S, 3, H, Dh), 2, 0, 3, 1, 4)
        q, k, v = qkv[0], qkv[1], qkv[2]
        pq = torch.einsum("bhsd,hdf->bhsf", q, w_phi)
        pk = torch.einsum("bhsd,hdf->bhsf", k, w_phi)
        phi_q = torch.concatenate([_softmax(pq, axis=-1), _softmax(-pq, axis=-1)], axis=-1)
        phi_k = torch.concatenate([_softmax(pk, axis=-1), _softmax(-pk, axis=-1)], axis=-1)
        kv = torch.einsum("bhsf,bhsd->bhfd", phi_k, v)
        z  = torch.einsum("bhsf->bhf", phi_k)
        num = torch.einsum("bhqf,bhfd->bhqd", phi_q, kv)
        den = torch.einsum("bhqf,bhf->bhq", phi_q, z)[..., None] + 1e-6
        o = num / den
        return (lambda x, *d: x.permute(*d))(o, 0, 2, 1, 3).reshape(B, S, D) @ wo

    x = A_x
    g = _sigmoid((x * gate_w).sum(-1, keepdims=True))
    a = _A_ref(x, A_wqkv, A_wo)
    b = _B_ref(x, B_wqkv, B_w_phi, B_wo)
    return g * a + (1 - g) * b
```

```cpp
#include <hip/hip_runtime.h>
#include <hip/hip_bf16.h>
#include <stdint.h>

typedef short bf16x8 __attribute__((ext_vector_type(8)));
typedef short s16x4 __attribute__((ext_vector_type(4)));
typedef float f32x16 __attribute__((ext_vector_type(16)));
typedef float f32x4 __attribute__((ext_vector_type(4)));
typedef float f32x2 __attribute__((ext_vector_type(2)));
typedef unsigned u32x4 __attribute__((ext_vector_type(4)));
typedef unsigned u32x2 __attribute__((ext_vector_type(2)));
typedef __bf16 bf16x2_t __attribute__((ext_vector_type(2)));
#define LAS __attribute__((address_space(3)))
#define GAS __attribute__((address_space(1)))
#define DI __device__ __forceinline__
#define MFMA(a, b, c) __builtin_amdgcn_mfma_f32_32x32x16_bf16((a), (b), (c), 0, 0, 0)
#define SBAR() __builtin_amdgcn_sched_barrier(0)

constexpr int SEQ = 2048, DM = 512, NH = 32;
constexpr float LOG2E = 1.4426950408889634f;
constexpr size_t WS_Q = 0;
constexpr size_t WS_KV = WS_Q + (2u << 20);
constexpr size_t WS_PHIQ = WS_KV + (4u << 20);
constexpr size_t WS_O = WS_PHIQ + (4u << 20);
constexpr size_t WS_KVP = WS_O + (4u << 20);
constexpr int KVP_BYTES = 17 * 2 * 16 * 4;
constexpr size_t WS_G = WS_KVP + (2u << 20);

DI unsigned pk2(float a, float b) { f32x2 v = {a, b}; return __builtin_bit_cast(unsigned, __builtin_convertvector(v, bf16x2_t)); }
DI bf16x8 pack8(const f32x16& x, int s) {
    u32x4 p = {pk2(x[8 * s], x[8 * s + 1]), pk2(x[8 * s + 2], x[8 * s + 3]), pk2(x[8 * s + 4], x[8 * s + 5]), pk2(x[8 * s + 6], x[8 * s + 7])};
    return __builtin_bit_cast(bf16x8, p);
}
DI float swap32(float v) {
    auto r = __builtin_amdgcn_permlane32_swap(__float_as_uint(v), __float_as_uint(v), false, false);
    return __uint_as_float((threadIdx.x & 32) ? r[0] : r[1]);
}
DI float max32(float v) { auto r = __builtin_amdgcn_permlane32_swap(__float_as_uint(v), __float_as_uint(v), false, false); return __builtin_fmaxf(__uint_as_float(r[0]), __uint_as_float(r[1])); }
DI float sum32(float v) { auto r = __builtin_amdgcn_permlane32_swap(__float_as_uint(v), __float_as_uint(v), false, false); return __uint_as_float(r[0]) + __uint_as_float(r[1]); }
template <int OFF> DI void glds16(unsigned voff, const void* sbase, unsigned ldsdst) {
    unsigned keep;
    asm volatile("s_mov_b32 %0, m0\n\ts_mov_b32 m0, %3\n\ts_nop 0\n\tglobal_load_lds_dwordx4 %1, %2 offset:%4\n\ts_mov_b32 m0, %0"
                 : "=&s"(keep) : "v"(voff), "s"(sbase), "s"(ldsdst), "i"(OFF) : "memory");
}
typedef short v4i16_t __attribute__((ext_vector_type(4)));
DI s16x4 trread(LAS const char* p) { return __builtin_bit_cast(s16x4, __builtin_amdgcn_ds_read_tr16_b64_v4i16((LAS v4i16_t*)p)); }
DI bf16x8 trfrag(LAS const char* p, int d2) { s16x4 lo = trread(p), hi = trread(p + d2); return __builtin_shufflevector(lo, hi, 0, 1, 2, 3, 4, 5, 6, 7); }
#define WAIT_BAR(N) asm volatile("s_waitcnt vmcnt(" #N ") lgkmcnt(0)\n\ts_barrier" ::: "memory")

DI void block_map(int b, int& c, int& r) { const int xcd = b & 7, idx = b >> 3; c = 4 * (xcd & 3) + (idx & 3); r = 8 * (xcd >> 2) + (idx >> 2); }

DI f32x16 feature_map(bf16x8 wf, bf16x8 xf) {
    f32x16 p = MFMA(wf, xf, f32x16{});
#pragma unroll
    for (int s = 0; s < 2; ++s) {
        float m = p[8 * s];
#pragma unroll
        for (int j = 1; j < 8; ++j) m = __builtin_fmaxf(m, p[8 * s + j]);
        m = max32(m);
        const float nm = -m * LOG2E;
        float sum = 0.f;
#pragma unroll
        for (int j = 0; j < 8; ++j) { p[8 * s + j] = __builtin_amdgcn_exp2f(__builtin_fmaf(p[8 * s + j], LOG2E, nm)); sum += p[8 * s + j]; }
        sum = sum32(sum);
        const float rs = __builtin_amdgcn_rcpf(sum);
#pragma unroll
        for (int j = 0; j < 8; ++j) p[8 * s + j] *= rs;
    }
    return p;
}

constexpr int K1_SLOT = 40960;
__global__ __launch_bounds__(512) void k1_qkv(const uint16_t* __restrict__ x, const uint16_t* __restrict__ WA, const uint16_t* __restrict__ WB,
                                              const uint16_t* __restrict__ wphi, const uint16_t* __restrict__ gatew, char* __restrict__ ws) {
    __shared__ __attribute__((aligned(16))) char lds[4 * K1_SLOT];
    const int tid = threadIdx.x, lane = tid & 63, w = __builtin_amdgcn_readfirstlane(tid >> 6);
    const int r32 = lane & 31, h = lane >> 5, tt = w & 3, br = w >> 2;
    int c, r; block_map(blockIdx.x, c, r);
    const unsigned lds0 = (unsigned)(uintptr_t)lds;
    unsigned xo[2], wo[3];
#pragma unroll
    for (int u = 0; u < 2; ++u) { const int row = 16 * w + 8 * u + (lane >> 3); xo[u] = (unsigned)(row * 1024 + (((lane & 7) ^ ((row >> 1) & 7)) << 4)); }
#pragma unroll
    for (int u = 0; u < 3; ++u) { const int id = 3 * tt + u, ct = id >> 2, kq = id & 3; wo[u] = (unsigned)((16 * kq + (lane >> 2)) * 3072 + ct * 1024 + (lane & 3) * 16); }
    const char* xb = (const char*)x + (size_t)r * 128 * 1024;
    const char* wb = (const char*)(br ? WB : WA) + c * 64;
    auto issue = [&](int kc) {
        const unsigned sb = lds0 + (kc & 3) * K1_SLOT;
        glds16<0>(xo[0], xb + kc * 128, sb + w * 2048);
        glds16<0>(xo[1], xb + kc * 128, sb + w * 2048 + 1024);
        glds16<0>(wo[0], wb + kc * 64 * 3072, sb + 16384 + w * 3072);
        glds16<0>(wo[1], wb + kc * 64 * 3072, sb + 16384 + w * 3072 + 1024);
        glds16<0>(wo[2], wb + kc * 64 * 3072, sb + 16384 + w * 3072 + 2048);
    };
    issue(0); issue(1); issue(2); issue(3);
    LAS const char* L = (LAS const char*)lds;
    const int xrow = tt * 32 + r32;
    const int xa0 = xrow * 128 + ((h ^ ((xrow >> 1) & 7)) << 4);
    const int wa0 = 16384 + br * 12288 + (8 * h + ((lane & 15) >> 2)) * 64 + ((lane >> 4) & 1) * 32 + (lane & 3) * 8;
    f32x16 aq = {}, ak = {}, av = {};
#define K1STEP(kc, N)                                                                                      \
    do {                                                                                                   \
        WAIT_BAR(N);                                                                                       \
        if ((kc) >= 1 && (kc) + 3 < 8) issue((kc) + 3);                                                    \
        LAS const char* sb = L + ((kc) & 3) * K1_SLOT;                                                     \
        _Pragma("unroll") for (int s = 0; s < 4; ++s) {                                                    \
            const bf16x8 xf = *(LAS const bf16x8*)(sb + (xa0 ^ (s << 5)));                                 \
            const bf16x8 wq = trfrag(sb + wa0 + s * 1024, 256), wk = trfrag(sb + wa0 + 4096 + s * 1024, 256), wv = trfrag(sb + wa0 + 8192 + s * 1024, 256); \
            aq = MFMA(wq, xf, aq); ak = MFMA(wk, xf, ak); av = MFMA(xf, wv, av);                           \
        }                                                                                                  \
    } while (0)
    K1STEP(0, 15); K1STEP(1, 10); K1STEP(2, 10); K1STEP(3, 10); K1STEP(4, 10); K1STEP(5, 10); K1STEP(6, 5); K1STEP(7, 0);
#undef K1STEP
    const int tile = 4 * r + tt;
    {
        const int tok = 128 * r + 8 * c + w;
        const bf16x8 xv = *(const bf16x8*)(x + (size_t)tok * DM + lane * 8), gv = *(const bf16x8*)(gatew + lane * 8);
        float s = 0.f;
#pragma unroll
        for (int j = 0; j < 8; ++j) s = __builtin_fmaf(__uint_as_float((unsigned)(uint16_t)xv[j] << 16), __uint_as_float((unsigned)(uint16_t)gv[j] << 16), s);
#pragma unroll
        for (int o = 32; o >= 1; o >>= 1) s += __shfl_xor(s, o);
        if (lane == 0) ((float*)(ws + WS_G))[tok] = __builtin_amdgcn_rcpf(1.f + __builtin_amdgcn_exp2f(-s * LOG2E));
    }
    asm volatile("s_waitcnt lgkmcnt(0)\n\ts_barrier" ::: "memory");
    if (br == 0) {
        const float qs = 0.25f * LOG2E;
#pragma unroll
        for (int i = 0; i < 16; ++i) aq[i] *= qs;
#pragma unroll
        for (int a = 0; a < 2; ++a) {
            const size_t ht = (size_t)(2 * c + a) * 64 + tile;
            *(bf16x8*)(ws + WS_Q + ht * 1024 + lane * 16) = pack8(aq, a);
            *(bf16x8*)(ws + WS_KV + ht * 2048 + lane * 16) = pack8(ak, a);
        }
        const size_t htv = (size_t)(2 * c + (r32 >> 4)) * 64 + tile;
#pragma unroll
        for (int s = 0; s < 2; ++s) *(bf16x8*)(ws + WS_KV + htv * 2048 + 1024 + s * 512 + h * 256 + (r32 & 15) * 16) = pack8(av, s);
    } else {
        LAS char* T = (LAS char*)lds + tt * 2048;
        LAS float* R = (LAS float*)((LAS char*)lds + 8192) + tt * 2048;
        const bf16x8 vf[2] = {pack8(av, 0), pack8(av, 1)};
#pragma unroll
        for (int a = 0; a < 2; ++a) {
            const int head = 2 * c + a;
            bf16x8 wf;
#pragma unroll
            for (int j = 0; j < 8; ++j) wf[j] = (short)(wphi[head * 256 + ((j & 3) + 8 * (j >> 2) + 4 * h) * 16 + (r32 & 15)] ^ ((r32 & 16) ? 0x8000 : 0));
            const f32x16 pq = feature_map(wf, pack8(aq, a));
            const size_t ht = (size_t)head * 64 + tile;
#pragma unroll
            for (int s = 0; s < 2; ++s) *(bf16x8*)(ws + WS_PHIQ + ht * 2048 + s * 1024 + lane * 16) = pack8(pq, s);
            const f32x16 pk = feature_map(wf, pack8(ak, a));
#pragma unroll
            for (int g = 0; g < 4; ++g) *(LAS u32x2*)(T + r32 * 64 + 8 * (2 * g + h)) = (u32x2){pk2(pk[4 * g], pk[4 * g + 1]), pk2(pk[4 * g + 2], pk[4 * g + 3])};
            asm volatile("s_waitcnt lgkmcnt(0)" ::: "memory");
            f32x16 kv = {};
            const bool keep = (r32 >> 4) == a;
#pragma unroll
            for (int s = 0; s < 2; ++s) {
                const bf16x8 pf = trfrag(T + (16 * s + 4 * h + ((lane & 15) >> 2)) * 64 + ((lane >> 4) & 1) * 32 + (lane & 3) * 8, 512);
                bf16x8 ve;
#pragma unroll
                for (int j = 0; j < 8; ++j) ve[j] = keep ? vf[s][j] : (short)0x3F80;
                kv = MFMA(pf, ve, kv);
            }
            asm volatile("s_waitcnt lgkmcnt(0)" ::: "memory");
#pragma unroll
            for (int i = 0; i < 16; ++i) R[a * 1024 + i * 64 + lane] = kv[i];
        }
    }
    __syncthreads();
    LAS const float* R0 = (LAS const float*)((LAS char*)lds + 8192);
    for (int e = tid; e < 2 * 544; e += 512) {
        const int a = e / 544, q = e % 544, col = q >> 5, hh = (q >> 4) & 1, reg = q & 15;
        const int ln = hh * 32 + (col < 16 ? a * 16 + col : (1 - a) * 16);
        const int o = a * 1024 + reg * 64 + ln;
        ((float*)(ws + WS_KVP + ((size_t)(2 * c + a) * 16 + r) * KVP_BYTES))[q] = R0[o] + R0[2048 + o] + R0[4096 + o] + R0[6144 + o];
    }
}

constexpr int K2_STAGE = 32768;
template <bool SAFE>
DI void attn_pass(char* lds, const char* kvsrc, unsigned voff, unsigned lds0, int w, int lane, bf16x8 qf, f32x16& o) {
    auto issue = [&](int st) {
        const unsigned dst = lds0 + (st & 3) * K2_STAGE + w * 4096;
        const char* src = kvsrc + st * 16384;
        glds16<0>(voff, src, dst); glds16<1024>(voff, src, dst); glds16<2048>(voff, src, dst); glds16<3072>(voff, src, dst);
    };
    issue(0); issue(1); issue(2); issue(3);
    LAS const char* L = (LAS const char*)lds + (w >> 2) * 16384;
    const int ka = lane * 16, va = 1024 + (lane >> 5) * 256 + (lane & 15) * 16;
    const bool vreal = (lane & 16) == 0;
    const bf16x8 ones = {0x3F80, 0x3F80, 0x3F80, 0x3F80, 0x3F80, 0x3F80, 0x3F80, 0x3F80};
    f32x16 negm = {};
    float m = -INFINITY;
    o = f32x16{};
    for (int st = 0; st < 8; ++st) {
        if (st == 0) { WAIT_BAR(12); } else if (st < 6) { WAIT_BAR(8); } else if (st == 6) { WAIT_BAR(4); } else { WAIT_BAR(0); }
        if (st >= 1 && st + 3 < 8) issue(st + 3);
        LAS const char* sb = L + (st & 3) * K2_STAGE;
        if (!SAFE && st == 0) {
            const f32x16 s0 = MFMA(*(LAS const bf16x8*)(sb + ka), qf, f32x16{});
            float mm = s0[0];
#pragma unroll
            for (int i = 1; i < 16; ++i) mm = __builtin_fmaxf(mm, s0[i]);
            mm = max32(mm);
#pragma unroll
            for (int i = 0; i < 16; ++i) negm[i] = -mm;
        }
#pragma unroll
        for (int t = 0; t < 8; ++t) {
            const bf16x8 kf = *(LAS const bf16x8*)(sb + t * 2048 + ka);
            bf16x8 v0 = *(LAS const bf16x8*)(sb + t * 2048 + va), v1 = *(LAS const bf16x8*)(sb + t * 2048 + va + 512);
            v0 = vreal ? v0 : ones; v1 = vreal ? v1 : ones;
            f32x16 s = MFMA(kf, qf, negm);
            if (SAFE) {
                float mm = s[0];
#pragma unroll
                for (int i = 1; i < 16; ++i) mm = __builtin_fmaxf(mm, s[i]);
                mm = __builtin_fmaxf(max32(mm), m);
                const float al = __builtin_amdgcn_exp2f(m - mm);
                m = mm;
#pragma unroll
                for (int i = 0; i < 16; ++i) { o[i] *= al; s[i] -= mm; }
            }
#pragma unroll
            for (int i = 0; i < 16; ++i) s[i] = __builtin_amdgcn_exp2f(s[i]);
            o = MFMA(v0, pack8(s, 0), o);
            o = MFMA(v1, pack8(s, 1), o);
        }
    }
}

__global__ __launch_bounds__(512) void k2_attn(char* __restrict__ ws) {
    __shared__ __attribute__((aligned(16))) char lds[4 * K2_STAGE + 16384];
    const int tid = threadIdx.x, lane = tid & 63, w = __builtin_amdgcn_readfirstlane(tid >> 6);
    const int r32 = lane & 31, h = lane >> 5, tt = w & 3, a = w >> 2;
    int c, r; block_map(blockIdx.x, c, r);
    const unsigned lds0 = (unsigned)(uintptr_t)lds;
    const int head = 2 * c + a, tile = 4 * r + tt;
    const size_t ht = (size_t)head * 64 + tile;
    const bf16x8 qf = *(const bf16x8*)(ws + WS_Q + ht * 1024 + lane * 16);
    const char* kvsrc = ws + WS_KV + (size_t)head * 131072 + tt * 4096;
    f32x16 o;
    attn_pass<false>(lds, kvsrc, lane * 16, lds0, w, lane, qf, o);
    float rl = __builtin_amdgcn_rcpf(o[8]);
    bool bad = !(o[8] < 1.8446744e19f);
#pragma unroll
    for (int i = 0; i < 8; ++i) bad |= !(__builtin_fabsf(o[i]) < 3.0e38f);
    if (__syncthreads_or(bad)) {
        attn_pass<true>(lds, kvsrc, lane * 16, lds0, w, lane, qf, o);
        rl = __builtin_amdgcn_rcpf(o[8]);
    }
#pragma unroll
    for (int i = 0; i < 8; ++i) o[i] *= rl;
    *(bf16x8*)(ws + WS_O + ((size_t)tile * 32 + head) * 1024 + lane * 16) = pack8(o, 0);
    f32x16 kv = {};
    const int col = r32 < 16 ? r32 : 16;
#pragma unroll
    for (int p = 0; p < 4; ++p) {
        const f32x4* src = (const f32x4*)(ws + WS_KVP + ((size_t)head * 16 + 4 * tt + p) * KVP_BYTES + (col * 2 + h) * 64);
#pragma unroll
        for (int g = 0; g < 4; ++g) { const f32x4 v = src[g]; kv[4 * g] += v[0]; kv[4 * g + 1] += v[1]; kv[4 * g + 2] += v[2]; kv[4 * g + 3] += v[3]; }
    }
    __syncthreads();
    LAS float* R = (LAS float*)lds;
#pragma unroll
    for (int i = 0; i < 16; ++i) R[w * 1024 + i * 64 + lane] = kv[i];
    __syncthreads();
#pragma unroll
    for (int i = 0; i < 16; ++i) { const int o_ = a * 4096 + i * 64 + lane; kv[i] = R[o_] + R[o_ + 1024] + R[o_ + 2048] + R[o_ + 3072]; }
    f32x16 ob = {};
#pragma unroll
    for (int s = 0; s < 2; ++s) ob = MFMA(pack8(kv, s), *(const bf16x8*)(ws + WS_PHIQ + ht * 2048 + s * 1024 + lane * 16), ob);
    const float rd = __builtin_amdgcn_rcpf(ob[8] + 1e-6f);
#pragma unroll
    for (int i = 0; i < 8; ++i) ob[i] *= rd;
    *(bf16x8*)(ws + WS_O + (2u << 20) + ((size_t)tile * 32 + head) * 1024 + lane * 16) = pack8(ob, 0);
}

__global__ __launch_bounds__(512) void k3_out(const uint16_t* __restrict__ woA, const uint16_t* __restrict__ woB, const char* __restrict__ ws, uint16_t* __restrict__ out) {
    __shared__ __attribute__((aligned(16))) char lds[131072];
    const int tid = threadIdx.x, lane = tid & 63, w = __builtin_amdgcn_readfirstlane(tid >> 6);
    const int r32 = lane & 31, h = lane >> 5, br = w >> 2, tt = (w >> 1) & 1, nt = w & 1;
    const int xcd = blockIdx.x & 7, idx = blockIdx.x >> 3;
    const int rb = xcd * 4 + (idx >> 3), nb = idx & 7;
    const unsigned lds0 = (unsigned)(uintptr_t)lds;
    {
        const char* src = (const char*)(br ? woB : woA) + nb * 128 + (size_t)(w & 3) * 128 * 1024;
        const int row = lane >> 3;
        const unsigned voff = (unsigned)(row * 1024 + (((lane & 7) ^ (((row >> 1) & 1) << 2)) << 4));
        const unsigned dst = lds0 + br * 65536 + (w & 3) * 16384;
#pragma unroll
        for (int i = 0; i < 16; ++i) glds16<0>(voff, src + i * 8192, dst + i * 1024);
    }
    const int tile = rb * 2 + tt;
    const char* osrc = ws + WS_O + (size_t)br * (2u << 20) + (size_t)tile * 32768 + lane * 16;
    bf16x8 of[32];
#pragma unroll
    for (int k = 0; k < 32; ++k) of[k] = *(const bf16x8*)(osrc + k * 1024);
    const float g = ((const float*)(ws + WS_G))[tile * 32 + r32];
    asm volatile("s_waitcnt vmcnt(0)\n\ts_barrier" ::: "memory");
    const int q = (lane & 15) >> 2;
    LAS const char* wp = (LAS const char*)lds + br * 65536 + (4 * h + q) * 128 + ((nt ^ (q >> 1)) * 64) + ((lane >> 4) & 1) * 32 + (lane & 3) * 8;
    f32x16 acc = {};
#pragma unroll
    for (int k = 0; k < 32; ++k) acc = MFMA(trfrag(wp + k * 2048, 1024), of[k], acc);
    __syncthreads();
    LAS float* R = (LAS float*)lds + (w & 3) * 1024;
    if (br == 1) {
#pragma unroll
        for (int i = 0; i < 16; ++i) R[i * 64 + lane] = acc[i] * (1.f - g);
    }
    __syncthreads();
    if (br == 0) {
#pragma unroll
        for (int i = 0; i < 16; ++i) acc[i] = __builtin_fmaf(acc[i], g, R[i * 64 + lane]);
        uint16_t* dst = out + (size_t)(tile * 32 + r32) * DM + nb * 64 + nt * 32 + 4 * h;
#pragma unroll
        for (int gq = 0; gq < 4; ++gq) *(u32x2*)(dst + 8 * gq) = (u32x2){pk2(acc[4 * gq], acc[4 * gq + 1]), pk2(acc[4 * gq + 2], acc[4 * gq + 3])};
    }
}

extern "C" void kernel_launch(void* const* d_in, const int* in_sizes, int n_in, void* d_out, int out_size, void* d_ws, size_t ws_size, hipStream_t stream) {
    const uint16_t* x = (const uint16_t*)d_in[0];
    const uint16_t* WA = (const uint16_t*)d_in[1];
    const uint16_t* woA = (const uint16_t*)d_in[2];
    const uint16_t* WB = (const uint16_t*)d_in[4];
    const uint16_t* wphi = (const uint16_t*)d_in[5];
    const uint16_t* woB = (const uint16_t*)d_in[6];
    const uint16_t* gatew = (const uint16_t*)d_in[7];
    char* ws = (char*)d_ws;
    k1_qkv<<<256, 512, 0, stream>>>(x, WA, WB, wphi, gatew, ws);
    k2_attn<<<256, 512, 0, stream>>>(ws);
    k3_out<<<256, 512, 0, stream>>>(woA, woB, ws, (uint16_t*)d_out);
}
```

```cpp
#include <hip/hip_runtime.h>
#include <hip/hip_bf16.h>
#include <stdint.h>
#define STAMP(ws, k, i)
#define TS_DECL
#define TS(i)
#define TS_DUMP(ws)

typedef short bf16x8 __attribute__((ext_vector_type(8)));
typedef short s16x4 __attribute__((ext_vector_type(4)));
typedef float f32x16 __attribute__((ext_vector_type(16)));
typedef float f32x4 __attribute__((ext_vector_type(4)));
typedef float f32x2 __attribute__((ext_vector_type(2)));
typedef unsigned u32x4 __attribute__((ext_vector_type(4)));
typedef unsigned u32x2 __attribute__((ext_vector_type(2)));
typedef __bf16 bf16x2_t __attribute__((ext_vector_type(2)));
typedef unsigned long long u64;
#define LAS __attribute__((address_space(3)))
#define GAS __attribute__((address_space(1)))
#define DI __device__ __forceinline__
#define MFMA(a, b, c) __builtin_amdgcn_mfma_f32_32x32x16_bf16((a), (b), (c), 0, 0, 0)
#define SBAR() __builtin_amdgcn_sched_barrier(0)
#define RLX_AGENT __ATOMIC_RELAXED, __HIP_MEMORY_SCOPE_AGENT
#define EXP2(x) __builtin_amdgcn_exp2f(x)

constexpr float LOG2E = 1.4426950408889634f;
constexpr unsigned REC = 2176;
constexpr unsigned WS_KV = 0;
constexpr unsigned WS_O = WS_KV + 32 * 64 * REC;
constexpr unsigned WS_PK = WS_O + (4u << 20);
constexpr unsigned WS_KVF = WS_PK + 32 * 32 * 2176;
constexpr unsigned WS_F1 = WS_KVF + 32 * 2176;
constexpr unsigned WS_F2 = WS_F1 + 2048;
constexpr unsigned WS_F3 = WS_F2 + 8192;
constexpr unsigned WS_F5 = WS_F3 + 8192;
constexpr unsigned WS_F4 = WS_F5 + 8192;
constexpr unsigned WS_EP = WS_F4 + 16384;
constexpr unsigned WS_END = WS_EP + 128;
constexpr int L_X = 0, L_RING = 65536;
constexpr int L_PHK = 65536, L_PHQ = 81920;
constexpr int L_WOA = 0, L_WOB = 65536;
constexpr int L_WPHI = 131072, L_G = L_WPHI + 2048, L_BAD = L_G + 256, L_SEEN = L_BAD + 4, L_MCNT = L_BAD + 8, L_HCNT = L_BAD + 12, L_OBS = L_BAD + 16, L_DUMMY = L_BAD + 256;
constexpr int L_QA = L_WPHI + 3072, L_KA = L_QA + 8192, L_VA = L_KA + 8192, L_EX = L_QA;
constexpr int LDS_BYTES = 163840;
#define KV_SPLIT 1
#define TOUCH_DELAY 4
#define WO_T0 4500
#define WO_DT 1700
#define PK_DELAY 24
#define KV_AUX 16
#define F1_STEP 13
#define PRIO_SWITCH 24
constexpr unsigned SPIN_CAP = 1u << 20;

DI unsigned pk2(float a, float b) { f32x2 v = {a, b}; return __builtin_bit_cast(unsigned, __builtin_convertvector(v, bf16x2_t)); }
DI bf16x8 pack8(const f32x16& x, int s) {
    u32x4 p = {pk2(x[8 * s], x[8 * s + 1]), pk2(x[8 * s + 2], x[8 * s + 3]), pk2(x[8 * s + 4], x[8 * s + 5]), pk2(x[8 * s + 6], x[8 * s + 7])};
    return __builtin_bit_cast(bf16x8, p);
}
DI float fmx(float a, float b) { return __builtin_elementwise_maximum(a, b); }
DI float max32(float v) { auto r = __builtin_amdgcn_permlane32_swap(__float_as_uint(v), __float_as_uint(v), false, false); return fmx(__uint_as_float(r[0]), __uint_as_float(r[1])); }
DI float lmax16(const f32x16& s) {
    const float a = fmx(fmx(s[0], s[1]), s[2]), b = fmx(fmx(s[3], s[4]), s[5]), c = fmx(fmx(s[6], s[7]), s[8]), d = fmx(fmx(s[9], s[10]), s[11]), e = fmx(fmx(s[12], s[13]), s[14]);
    return fmx(fmx(fmx(a, b), c), fmx(fmx(d, e), s[15]));
}
DI float max16(const f32x16& s) { return max32(lmax16(s)); }
DI void glds2(unsigned v0, unsigned v1, const void* sbase, unsigned ldsdst) {
    unsigned keep;
    asm volatile("s_mov_b32 %0, m0\n\ts_mov_b32 m0, %4\n\ts_nop 0\n\tglobal_load_lds_dwordx4 %1, %3\n\tglobal_load_lds_dwordx4 %2, %3 offset:1024\n\ts_mov_b32 m0, %0"
                 : "=&s"(keep) : "v"(v0), "v"(v1), "s"(sbase), "s"(ldsdst) : "memory");
}
DI void glds4(const unsigned (&v)[4], const void* sbase, unsigned ldsdst) {
    unsigned keep;
    asm volatile("s_mov_b32 %0, m0\n\ts_mov_b32 m0, %6\n\ts_nop 0\n\tglobal_load_lds_dwordx4 %1, %5\n\tglobal_load_lds_dwordx4 %2, %5 offset:1024\n\t"
                 "global_load_lds_dwordx4 %3, %5 offset:2048\n\tglobal_load_lds_dwordx4 %4, %5 offset:3072\n\ts_mov_b32 m0, %0"
                 : "=&s"(keep) : "v"(v[0]), "v"(v[1]), "v"(v[2]), "v"(v[3]), "s"(sbase), "s"(ldsdst) : "memory");
}
DI void touch(unsigned voff, const void* sbase, unsigned ldsdst) {
    unsigned keep;
    asm volatile("s_mov_b32 %0, m0\n\ts_mov_b32 m0, %3\n\ts_nop 0\n\tglobal_load_lds_dword %1, %2 sc1\n\ts_mov_b32 m0, %0"
                 : "=&s"(keep) : "v"(voff), "s"(sbase), "s"(ldsdst) : "memory");
}
typedef short v4i16_t __attribute__((ext_vector_type(4)));
DI s16x4 trread(LAS const char* p) { return __builtin_bit_cast(s16x4, __builtin_amdgcn_ds_read_tr16_b64_v4i16((LAS v4i16_t*)p)); }
DI bf16x8 trfrag(LAS const char* p, int d2) { s16x4 lo = trread(p), hi = trread(p + d2); return __builtin_shufflevector(lo, hi, 0, 1, 2, 3, 4, 5, 6, 7); }
DI bf16x8 ldsfrag(LAS const char* p) { return *(LAS const bf16x8*)p; }
#define BAR() asm volatile("s_waitcnt lgkmcnt(0)\n\ts_barrier" ::: "memory")
#define VM_BAR(N) asm volatile("s_waitcnt vmcnt(" #N ") lgkmcnt(0)\n\ts_barrier" ::: "memory")
#define DRAIN() asm volatile("s_waitcnt vmcnt(0)" ::: "memory")
typedef __amdgpu_buffer_rsrc_t rsrc_t;
DI bf16x8 ld16(rsrc_t r, unsigned voff, unsigned soff) { return __builtin_bit_cast(bf16x8, __builtin_amdgcn_raw_buffer_load_b128(r, voff, soff, 16)); }
DI void st16(rsrc_t r, bf16x8 v, unsigned voff, unsigned soff) { __builtin_amdgcn_raw_buffer_store_b128(__builtin_bit_cast(u32x4, v), r, voff, soff, 16); }
DI void st16kv(rsrc_t r, bf16x8 v, unsigned voff, unsigned soff) { __builtin_amdgcn_raw_buffer_store_b128(__builtin_bit_cast(u32x4, v), r, voff, soff, KV_AUX); }
DI unsigned lds_word(LAS char* p) { return __builtin_amdgcn_readfirstlane(*(LAS volatile unsigned*)p); }
DI void lds_wait(LAS char* p, unsigned want) {
    for (unsigned spins = 0; spins < (SPIN_CAP << 6) && lds_word(p) < want; ++spins) __builtin_amdgcn_s_sleep(1);
    asm volatile("" ::: "memory");
}
DI void lds_arrive(LAS char* p, int lane) {
    asm volatile("s_waitcnt lgkmcnt(0)" ::: "memory");
    if (lane == 0) __hip_atomic_fetch_add((LAS unsigned*)p, 1u, __ATOMIC_RELAXED, __HIP_MEMORY_SCOPE_WORKGROUP);
}
DI void set_flag(char* ws, unsigned off, u64 tag) { __hip_atomic_store((GAS u64*)(ws + off), tag, RLX_AGENT); }
template <int FIRST, int GAP> DI void wait_flags(char* ws, unsigned off, int n, u64 tag, int lane) {
    GAS u64* p = (GAS u64*)(ws + off) + (lane < n ? lane : 0);
    if (FIRST) __builtin_amdgcn_s_sleep(FIRST);
    for (unsigned spins = 0; spins < SPIN_CAP; ++spins) {
        const u64 v = __hip_atomic_load(p, RLX_AGENT);
        if (__all(v == tag)) break;
        __builtin_amdgcn_s_sleep(GAP);
    }
    __builtin_amdgcn_fence(__ATOMIC_ACQUIRE, "wavefront");
    asm volatile("" ::: "memory");
}

DI void softmax16(f32x16& p) {
    const float nm = -lmax16(p) * LOG2E;
    f32x2 e[8];
#pragma unroll
    for (int i = 0; i < 8; ++i) { e[i] = __builtin_elementwise_fma((f32x2){p[2 * i], p[2 * i + 1]}, (f32x2){LOG2E, LOG2E}, (f32x2){nm, nm}); e[i][0] = EXP2(e[i][0]); e[i][1] = EXP2(e[i][1]); }
    const f32x2 t = ((e[0] + e[1]) + (e[2] + e[3])) + ((e[4] + e[5]) + (e[6] + e[7]));
    const float rs = __builtin_amdgcn_rcpf(t[0] + t[1]);
#pragma unroll
    for (int i = 0; i < 8; ++i) { e[i] *= rs; p[2 * i] = e[i][0]; p[2 * i + 1] = e[i][1]; }
}

__global__ __launch_bounds__(768) void fused_kernel(const uint16_t* __restrict__ x, const uint16_t* __restrict__ WA, const uint16_t* __restrict__ WB,
                                                    const uint16_t* __restrict__ wphi, const uint16_t* __restrict__ gatew,
                                                    const uint16_t* __restrict__ woA, const uint16_t* __restrict__ woB,
                                                    uint16_t* __restrict__ out, char* ws) {
    __shared__ __attribute__((aligned(16))) char lds[LDS_BYTES];
    LAS char* L = (LAS char*)lds;
    const unsigned lds0 = (unsigned)(uintptr_t)lds;
    const int tid = threadIdx.x, lane = tid & 63, w = __builtin_amdgcn_readfirstlane(tid >> 6);
    const int r32 = lane & 31, h = lane >> 5;
    const int xcd = blockIdx.x & 7, idx = blockIdx.x >> 3;
    const int qd = 2 * (xcd & 3) + (idx & 1), tl = 2 * (idx >> 1) + (xcd >> 2);
    const int s0 = ((tl >> 1) + 4 * (qd & 1)) & 7;
    const int otl = 4 * xcd + (idx >> 3), ocg = idx & 7;
    const rsrc_t rs = __builtin_amdgcn_make_buffer_rsrc(ws, 0, WS_END, 0x00020000);
    u64 tag;
#define EPOCH_BAR(N) do { asm volatile("s_load_dwordx2 %0, %1, 0x0\n\ts_waitcnt vmcnt(" #N ") lgkmcnt(0)\n\ts_barrier" : "=s"(tag) : "s"(ws + WS_EP) : "memory"); tag += 1; } while (0)
    const int q4 = (lane & 15) >> 2;
    const int L_VB = L_X + s0 * 8192;
    const int trl = ((lane >> 4) & 1) * 32 + (lane & 3) * 8;
    STAMP(ws, 0, 0);
    TS_DECL; TS(0); TS(16);

    if (w >= 8) {
        const int j = w - 8;
        const unsigned vsw = (unsigned)(((lane & 7) ^ (((lane >> 4) & 1) << 2)) << 4);
        unsigned vw[4];
#pragma unroll
        for (int u = 0; u < 4; ++u) vw[u] = (unsigned)((lane >> 3) * 3072) + vsw + u * (8 * 3072 - 1024);
        const unsigned vx0 = (unsigned)((lane >> 3) * 1024 + (((lane & 7) ^ (lane >> 4)) << 4)), vx1 = (vx0 ^ 64u) + 8192 - 1024;
        const char* xb = (const char*)x + (size_t)(64 * tl + 16 * j) * 1024;
        const char* w1b = (const char*)WA + (size_t)((j & 1) * 32) * 3072 + qd * 128 + 1024 + (j >> 1) * 1024;
        const char* w2b = (j == 0 ? (const char*)WA : (const char*)WB + (j - 1) * 1024) + qd * 128;
        auto issue_x = [&](int i) { const int kc = (s0 + i) & 7; glds2(vx0, vx1, xb + kc * 128, lds0 + L_X + kc * 8192 + j * 2048); };
        auto issue_w1 = [&](int i) {
            glds4(vw, w1b + ((s0 + i) & 7) * (64 * 3072), lds0 + L_RING + ((i + 4) % 6) * 16384 + j * 4096);
        };
        auto issue_w2 = [&](int i) {
            const char* wb = w2b + ((s0 + i) & 7) * (64 * 3072);
            glds4(vw, wb, lds0 + L_RING + (i % 3) * 32768 + j * 8192);
            glds4(vw, wb + 32 * 3072, lds0 + L_RING + (i % 3) * 32768 + j * 8192 + 4096);
        };
        __builtin_amdgcn_s_setprio(3);
        issue_w1(0); issue_x(0); issue_x(1); issue_x(2); issue_x(3);
        bf16x8 gw, wph;
        asm volatile("global_load_dwordx4 %0, %1, %2" : "=v"(gw) : "v"((lane & 15) * 64 + (lane >> 4) * 16), "s"(gatew) : "memory");
        asm volatile("global_load_dwordx4 %0, %1, %2" : "=v"(wph) : "v"(lane * 16), "s"(wphi + qd * 1024 + (j & 1) * 512) : "memory");
        issue_w1(1);
        VM_BAR(12); issue_w1(2); issue_w1(3);
        VM_BAR(8); issue_x(4); issue_w1(4);
        EPOCH_BAR(10); issue_x(5); issue_w1(5);
        VM_BAR(12); issue_x(6); issue_w1(6);
        VM_BAR(12); issue_x(7); issue_w1(7);
        VM_BAR(12); issue_w2(0);
        VM_BAR(14); issue_w2(1);
        VM_BAR(16);
        f32x4 gl = {};
        const int grow = 16 * j + (lane & 15);
        int gxa = L_X + grow * 128 + (((lane >> 4) ^ ((grow >> 1) & 7)) << 4);
#pragma unroll
        for (int i = 8; i < 16; ++i) {
            if (i < 15) VM_BAR(8); else VM_BAR(0);
            if (i == 8) asm volatile("" : "+v"(gw), "+v"(wph));
            if (i < 14) issue_w2(i - 6);
            if (i == 14) {
                if (j < 2) *(LAS bf16x8*)(L + L_WPHI + j * 1024 + lane * 16) = wph;
                if (j == 2 && lane < 8) *(LAS unsigned*)(L + L_BAD + lane * 4) = 0;
            }
            const int kc = (s0 + i) & 7;
#pragma unroll
            for (int u = 0; u < 2; ++u) {
                const bf16x8 xg = ldsfrag(L + (gxa ^ (u << 6)) + kc * 8192);
                gl = __builtin_amdgcn_mfma_f32_16x16x32_bf16((lane & 15) == 2 * kc + u ? gw : bf16x8{}, xg, gl, 0, 0, 0);
            }
        }
        {
            float z = (gl[0] + gl[1]) + (gl[2] + gl[3]);
            z += __shfl_xor(z, 16); z += __shfl_xor(z, 32);
            if (lane < 16) *(LAS float*)(L + L_G + grow * 4) = __builtin_amdgcn_rcpf(1.f + EXP2(-z * LOG2E));
        }
        TS(1);
        if (j == 0) {
            wait_flags<0, 2>(ws, WS_F1 + qd * 256, 32, tag, lane);
            *(LAS volatile unsigned*)(L + L_SEEN) = 1;
        }
        TS(2);
        BAR();
        TS(3);
        const u64 tp2 = __builtin_amdgcn_s_memtime();
        const int head = 4 * qd + j;
        bf16x8 phq[2][2]; float g2[2];
        {
            f32x16 kv = {};
            const bool keep = (r32 >> 4) == (j & 1);
            bf16x8 pf[2][2], ve[2][2];
#pragma unroll
            for (int tt = 0; tt < 2; ++tt) {
#pragma unroll
                for (int s = 0; s < 2; ++s) {
                    pf[tt][s] = trfrag(L + L_PHK + (j * 2 + tt) * 2048 + (16 * s + 4 * h + q4) * 64 + trl, 512);
                    ve[tt][s] = ldsfrag(L + L_VB + (((j >> 1) * 2 + tt) * 2 + s) * 1024 + lane * 16);
                    phq[tt][s] = ldsfrag(L + L_PHQ + ((j * 2 + tt) * 2 + s) * 1024 + lane * 16);
                }
                g2[tt] = 1.f - *(LAS const float*)(L + L_G + (tt * 32 + r32) * 4);
            }
            lds_arrive(L + L_HCNT, lane);
            __builtin_amdgcn_s_sleep(PK_DELAY);
#pragma unroll
            for (int tt = 0; tt < 2; ++tt)
#pragma unroll
                for (int s = 0; s < 2; ++s) {
#pragma unroll
                    for (int e = 0; e < 8; ++e) ve[tt][s][e] = keep ? ve[tt][s][e] : (short)0x3F80;
                    kv = MFMA(pf[tt][s], ve[tt][s], kv);
                }
            const int col = keep ? (r32 & 15) : 16;
            const unsigned vo = (keep || (r32 & 15) == 0) ? (unsigned)((col * 2 + h) * 16) : 0x40000000u;
#pragma unroll
            for (int g = 0; g < 4; ++g) st16(rs, __builtin_bit_cast(bf16x8, (f32x4){kv[4 * g], kv[4 * g + 1], kv[4 * g + 2], kv[4 * g + 3]}), vo + g * 544, WS_PK + (head * 32 + tl) * 2176);
            DRAIN();
            if (lane == 0) set_flag(ws, WS_F2 + (head * 32 + tl) * 8, tag);
        }
        unsigned vo[4];
#pragma unroll
        for (int u = 0; u < 4; ++u) vo[u] = (unsigned)((lane >> 3) * 1024) + vsw + u * (8192 - 1024);
        lds_wait(L + L_HCNT, 4);
        int wo_done = 0;
        auto wo_service = [&]() {
            if (wo_done < 2 && __builtin_amdgcn_s_memtime() - tp2 >= (u64)(WO_T0 + WO_DT * (j + 4 * wo_done))) {
                const char* src = (const char*)(wo_done ? woA : woB) + (size_t)(128 * j) * 1024 + ocg * 128;
#pragma unroll
                for (int i = 0; i < 4; ++i) glds4(vo, src + i * 32768, lds0 + (wo_done ? L_WOA : L_WOB) + j * 16384 + i * 4096);
                ++wo_done;
            }
        };
        auto wait_slow = [&](unsigned off, int n) {
            GAS u64* p = (GAS u64*)(ws + off) + (lane < n ? lane : 0);
            for (unsigned spins = 0; spins < SPIN_CAP; ++spins) {
#pragma unroll 1
                for (int z = 0; z < (spins ? 2 : 6); ++z) { wo_service(); __builtin_amdgcn_s_sleep(6); }
                if (__all(__hip_atomic_load(p, RLX_AGENT) == tag)) break;
            }
            __builtin_amdgcn_fence(__ATOMIC_ACQUIRE, "wavefront");
            asm volatile("" ::: "memory");
        };
        STAMP(ws, 0, 1);
        if (tl < 17) {
            wait_slow(WS_F2 + head * 256, 32);
            const unsigned so = WS_PK + head * 32 * 2176 + tl * 128, v1 = (unsigned)((lane >> 3) * 2176 + (lane & 7) * 16);
            f32x4 a = (__builtin_bit_cast(f32x4, ld16(rs, v1, so)) + __builtin_bit_cast(f32x4, ld16(rs, v1, so + 8 * 2176))) +
                      (__builtin_bit_cast(f32x4, ld16(rs, v1, so + 16 * 2176)) + __builtin_bit_cast(f32x4, ld16(rs, v1, so + 24 * 2176)));
#pragma unroll
            for (int o = 8; o < 64; o <<= 1) {
#pragma unroll
                for (int e = 0; e < 4; ++e) a[e] += __shfl_xor(a[e], o);
            }
            st16(rs, __builtin_bit_cast(bf16x8, a), lane < 8 ? (unsigned)(lane * 16) : 0x40000000u, WS_KVF + head * 2176 + tl * 128);
            DRAIN();
            if (lane == 0) set_flag(ws, WS_F3 + (head * 32 + tl) * 8, tag);
        } else {
#pragma unroll 1
            for (int z = 0; z < 10; ++z) { wo_service(); __builtin_amdgcn_s_sleep(6); }
        }
        STAMP(ws, 0, 2);
        wait_slow(WS_F3 + head * 256, 17);
        {
            const unsigned v2 = (unsigned)(((r32 < 16 ? r32 : 16) * 2 + h) * 16);
            f32x16 kv;
#pragma unroll
            for (int g = 0; g < 4; ++g) { const f32x4 t = __builtin_bit_cast(f32x4, ld16(rs, v2 + g * 544, WS_KVF + head * 2176)); kv[4 * g] = t[0]; kv[4 * g + 1] = t[1]; kv[4 * g + 2] = t[2]; kv[4 * g + 3] = t[3]; }
            const bf16x8 k0 = pack8(kv, 0), k1 = pack8(kv, 1);
#pragma unroll
            for (int tt = 0; tt < 2; ++tt) {
                f32x16 ob = MFMA(k0, phq[tt][0], f32x16{});
                ob = MFMA(k1, phq[tt][1], ob);
                const float rd = g2[tt] * __builtin_amdgcn_rcpf(ob[8] + 1e-6f);
#pragma unroll
                for (int i = 0; i < 8; ++i) ob[i] *= rd;
                st16(rs, pack8(ob, 0), lane * 16, WS_O + (2u << 20) + ((2 * tl + tt) * 32 + head) * 1024);
                st16(rs, (bf16x8){-1, -1, -1, -1, -1, -1, -1, -1}, lane * 16, WS_O + ((2 * tl + tt) * 32 + head) * 1024);
            }
            DRAIN();
            if (lane == 0) set_flag(ws, WS_F5 + (tl * 32 + head) * 8, tag);
        }
        STAMP(ws, 0, 3);
        wait_slow(WS_F5 + (otl * 32 + 8 * j) * 8, 8);
        lds_arrive(L + L_OBS, lane);
        if (lane < 16) touch((lane >> 3) * 32768 + (lane & 7) * 128, ws + WS_O + (2u << 20) + (2 * otl * 32 + 8 * j + ocg) * 1024, lds0 + L_DUMMY);
        while (wo_done < 2) { wo_service(); __builtin_amdgcn_s_sleep(6); }
        STAMP(ws, 0, 5);
        VM_BAR(0);
        TS_DUMP(ws);
        return;
    }

    const int role = w >> 2, p = (w >> 1) & 1, tt = w & 1;
    const int hq = w >> 1, kh = w & 1, head = 4 * qd + hq;
    const unsigned vK = lane * 16;
    const unsigned vV0 = r32 < 16 ? (unsigned)(1024 + h * 256 + r32 * 16) : ((r32 & 11) == 0 ? 2048u : 0x40000000u);
    const unsigned vV1 = r32 < 16 ? vV0 + 512 : vV0;
    const unsigned kvb = WS_KV + (head * 64 + kh) * REC;
#define KVOFF(t) (kvb + (((t) + tl) & 31) * (2 * REC))
    bf16x8 kr[4], v0r[4], v1r[4];
    {
        const int xrow = tt * 32 + r32;
        const int xa0 = L_X + xrow * 128 + ((h ^ ((xrow >> 1) & 7)) << 4);
        const int wa0 = L_RING + (8 * h + q4) * 128 + ((p ^ (q4 >> 1)) * 64) + trl;
        bf16x8 xf[4], w0[4], w1[4], own[2];
        f32x16 a0 = {}, a1 = {};
        const int tile = 2 * tl + tt;
        int wsb[3], xsb[4];
#pragma unroll
        for (int i = 0; i < 3; ++i) { wsb[i] = wa0 + i * 32768; asm volatile("" : "+v"(wsb[i])); }
#pragma unroll
        for (int s = 0; s < 4; ++s) { xsb[s] = xa0 ^ (s << 5); asm volatile("" : "+v"(xsb[s])); }
        auto reads = [&](int i, int s, int ROLE) {
            const int kc = (s0 + i) & 7;
            xf[s] = ldsfrag(L + (xsb[s] + kc * 8192));
            if (i < 8) w0[s] = trfrag(L + wsb[((i + 4) % 6) >> 1] + (i & 1) * 16384 + ROLE * 8192 + s * 2048, 512);
            else {
                LAS const char* wsl = L + wsb[(i - 8) % 3] + ROLE * 16384 + s * 2048;
                w0[s] = trfrag(wsl, 512);
                w1[s] = trfrag(wsl + 8192, 512);
            }
        };
        auto mmas = [&](int i, int s, int ROLE) {
            if (i < 8) a0 = ROLE ? MFMA(xf[s], w0[s], a0) : MFMA(w0[s], xf[s], a0);
            else {
                a0 = MFMA(w0[s], xf[s], a0);
                a1 = ROLE ? MFMA(xf[s], w1[s], a1) : MFMA(w1[s], xf[s], a1);
            }
        };
        auto publish = [&](int ROLE, int part) {
            const unsigned so = WS_KV + ((4 * qd + 2 * p) * 64 + tile) * REC;
            if (ROLE == 0) st16kv(rs, own[part], lane * 16, so + part * 64 * REC);
            else {
                st16kv(rs, own[part], (unsigned)((r32 >> 4) * 64 * REC + 1024 + h * 256 + (r32 & 15) * 16) + part * 512, so);
                const bf16x8 ones = {0x3F80, 0x3F80, 0x3F80, 0x3F80, 0x3F80, 0x3F80, 0x3F80, 0x3F80};
                if (part == 0) st16kv(rs, ones, lane < 2 ? (unsigned)(lane * 64 * REC + 2048) : 0x40000000u, so);
            }
        };
        auto gemm = [&](int ROLE) {
#pragma unroll
            for (int i = 0; i < 16; ++i) {
                if (i == F1_STEP) { VM_BAR(0); if (tid == 0) set_flag(ws, WS_F1 + (qd * 32 + tl) * 8, tag); } else if (i == 0) EPOCH_BAR(63); else BAR();
                if (i < 15) TS(i + 1); else TS(17);
                SBAR();
                if (ROLE == 1 && i > 0) {
#pragma unroll
                    for (int s = 0; s < 4; ++s) mmas(i - 1, s, ROLE);
                    SBAR();
                }
#pragma unroll
                for (int s = 0; s < 4; ++s) reads(i, s, ROLE);
                SBAR();
                if (ROLE == 0) {
#pragma unroll
                    for (int s = 0; s < 4; ++s) mmas(i, s, ROLE);
                    SBAR();
                }
                if (i == 7 + ROLE) { own[0] = pack8(a0, 0); own[1] = pack8(a0, 1); a0 = f32x16{}; publish(ROLE, 0); if (!KV_SPLIT) publish(ROLE, 1); }
                if (KV_SPLIT && i == 7 + ROLE + KV_SPLIT) publish(ROLE, 1);
                SBAR();
            }
            bf16x8 wf[2];
#pragma unroll
            for (int a = 0; a < 2; ++a) wf[a] = trfrag(L + L_WPHI + (2 * p + a) * 512 + (4 * h + q4) * 32 + (((lane >> 4) & 1) * 2 + ((lane >> 1) & 1)) * 8, 256);
            TS(18);
            if (ROLE == 1) {
#pragma unroll
                for (int s = 0; s < 4; ++s) mmas(15, s, ROLE);
            }
            const short sg = (lane & 4) ? (short)0x8000 : (short)0;
#pragma unroll
            for (int a = 0; a < 2; ++a)
#pragma unroll
                for (int e = 0; e < 8; ++e) wf[a][e] ^= sg;
            const f32x16& fin = ROLE ? a0 : a1;
            f32x16 f0 = MFMA(wf[0], pack8(fin, 0), f32x16{}), f1 = MFMA(wf[1], pack8(fin, 1), f32x16{});
            if (ROLE == 0) {
                const float qs = 0.25f * LOG2E;
#pragma unroll
                for (int i = 0; i < 16; ++i) a0[i] *= qs;
#pragma unroll
                for (int a = 0; a < 2; ++a) {
                    *(LAS bf16x8*)(L + L_QA + ((2 * p + a) * 2 + tt) * 1024 + lane * 16) = pack8(a0, a);
                    *(LAS bf16x8*)(L + L_KA + ((2 * p + a) * 2 + tt) * 1024 + lane * 16) = own[a];
                }
            } else {
#pragma unroll
                for (int s = 0; s < 2; ++s) {
                    *(LAS bf16x8*)(L + L_VB + ((p * 2 + tt) * 2 + s) * 1024 + lane * 16) = pack8(a1, s);
                    *(LAS bf16x8*)(L + L_VA + ((p * 2 + tt) * 2 + s) * 1024 + lane * 16) = own[s];
                }
            }
            TS(19);
            lds_wait(L + L_SEEN, 1);
#pragma unroll
            for (int t = 1; t < 3; ++t) { kr[t] = ld16(rs, vK, KVOFF(t)); v0r[t] = ld16(rs, vV0, KVOFF(t)); v1r[t] = ld16(rs, vV1, KVOFF(t)); SBAR(); }
            TS(22);
            softmax16(f0); softmax16(f1);
#pragma unroll
            for (int a = 0; a < 2; ++a) {
                const f32x16& f = a ? f1 : f0;
                if (ROLE == 0) {
#pragma unroll
                    for (int s = 0; s < 2; ++s) *(LAS bf16x8*)(L + L_PHQ + (((2 * p + a) * 2 + tt) * 2 + s) * 1024 + lane * 16) = pack8(f, s);
                } else {
#pragma unroll
                    for (int g = 0; g < 4; ++g)
                        *(LAS u32x2*)(L + L_PHK + ((2 * p + a) * 2 + tt) * 2048 + r32 * 64 + 8 * (2 * g + h)) = (u32x2){pk2(f[4 * g], f[4 * g + 1]), pk2(f[4 * g + 2], f[4 * g + 3])};
                }
            }
        };
        if (role == 0) gemm(0); else gemm(1);
    }
    TS(20);
    BAR();
    TS(21);
    const bf16x8 qa = ldsfrag(L + L_QA + (hq * 2) * 1024 + lane * 16), qb = ldsfrag(L + L_QA + (hq * 2 + 1) * 1024 + lane * 16);
    kr[0] = ldsfrag(L + L_KA + (hq * 2 + kh) * 1024 + lane * 16);
    v0r[0] = ldsfrag(L + L_VA + (((hq >> 1) * 2 + kh) * 2) * 1024 + (h * 32 + (hq & 1) * 16 + (r32 & 15)) * 16);
    v1r[0] = ldsfrag(L + L_VA + (((hq >> 1) * 2 + kh) * 2 + 1) * 1024 + (h * 32 + (hq & 1) * 16 + (r32 & 15)) * 16);
    const float gate = *(LAS const float*)(L + L_G + (kh * 32 + r32) * 4);
    kr[3] = ld16(rs, vK, KVOFF(3)); v0r[3] = ld16(rs, vV0, KVOFF(3)); v1r[3] = bf16x8{};
    lds_arrive(L + L_MCNT, lane);
    TS(23);
    float mref; bool bad;
    {
        const float ma = max16(MFMA(kr[0], qa, f32x16{})), mb = max16(MFMA(kr[0], qb, f32x16{}));
        mref = fmx(ma, mb);
        bad = !(__builtin_fabsf(ma - mb) <= 40.f);
        const short cf = (r32 & 11) == 0 ? (short)0x3F80 : (short)0;
#pragma unroll
        for (int e = 0; e < 8; ++e) { v0r[0][e] = r32 < 16 ? v0r[0][e] : cf; v1r[0][e] = r32 < 16 ? v1r[0][e] : cf; }
    }
    TS(24);
    STAMP(ws, 0, 4);
    const int ot = w & 1, kq = w >> 1;
    f32x16 oa = {}, ob = {};
    float ma_ = mref, mb_ = mref;
    {
        f32x16 negm;
#pragma unroll
        for (int i = 0; i < 16; ++i) negm[i] = -mref;
        f32x16 sa = MFMA(kr[0], qa, negm), sb;
        u32x4 p1, p2 = {0u, 0u, 0u, 0u};
#define E(S, i) S[i] = EXP2(S[i])
#define C(P, S, i) P[(i) & 3] = pk2(S[2 * (i)], S[2 * (i) + 1])
#define PHASE(S, M1, M2, M3, LD1, LD2, LD3)                                                       \
    do {                                                                                          \
        M1; LD1; SBAR();                                                                          \
        E(S, 0); E(S, 1); E(S, 2); E(S, 3); E(S, 4); SBAR();                                      \
        M2; LD2; SBAR();                                                                          \
        E(S, 5); E(S, 6); E(S, 7); SBAR(); C(p1, S, 0); C(p1, S, 1); C(p1, S, 2); C(p1, S, 3); SBAR(); E(S, 8); E(S, 9); SBAR(); \
        M3; LD3; SBAR();                                                                          \
        E(S, 10); E(S, 11); E(S, 12); E(S, 13); E(S, 14); E(S, 15); SBAR();                       \
        C(p2, S, 4); C(p2, S, 5); C(p2, S, 6); C(p2, S, 7); SBAR();                               \
    } while (0)
#define P1 __builtin_bit_cast(bf16x8, p1)
#define P2 __builtin_bit_cast(bf16x8, p2)
        const unsigned obo = WS_O + (2u << 20) + ((2 * otl + ot) * 32 + 8 * kq) * 1024;
        auto body = [&](int t0, bool LAST) {
#pragma unroll
            for (int u = 0; u < 4; ++u) {
                const int t = t0 + u;
                PHASE(sa, sb = MFMA(kr[u], qb, negm), ob = MFMA(v1r[(u + 3) & 3], P2, ob), oa = MFMA(v0r[u], P1, oa),
                      kr[u] = ld16(rs, vK, LAST ? obo + u * 1024 : KVOFF(t + 4)), if (!LAST || u == 0) v1r[(u + 3) & 3] = ld16(rs, vV1, KVOFF(t + 3)), (void)0);
                PHASE(sb, if (!LAST || u < 3) sa = MFMA(kr[(u + 1) & 3], qa, negm), oa = MFMA(v1r[u], P2, oa), ob = MFMA(v0r[u], P1, ob),
                      (void)0, (void)0, v0r[u] = LAST ? ld16(rs, vK, obo + (4 + u) * 1024) : ld16(rs, vV0, KVOFF(t + 4)));
            }
        };
        if (w < 4) __builtin_amdgcn_s_setprio(2); else __builtin_amdgcn_s_setprio(1);
        STAMP(ws, 3, 0);
        body(0, false);
        for (int t0 = 4; t0 < 28; t0 += 4) {
            if (w < 4 && t0 == PRIO_SWITCH) __builtin_amdgcn_s_setprio(0);
            STAMP(ws, 3, t0 >> 2);
            body(t0, false);
        }
        STAMP(ws, 3, 7);
        lds_wait(L + L_OBS, 4);
        body(28, true);
        STAMP(ws, 3, 8);
        ob = MFMA(v1r[3], P2, ob);
#undef PHASE
#undef E
#undef C
    }
    STAMP(ws, 0, 5);
    {
        float t = fmx(oa[8], ob[8]) * 1.0e19f;
#pragma unroll
        for (int i = 0; i < 8; ++i) t = fmx(fmx(t, __builtin_fabsf(oa[i])), __builtin_fabsf(ob[i]));
        bad |= !(t < 1.0e38f);
    }
    lds_wait(L + L_MCNT, 8);
    LAS float* EXW = (LAS float*)(L + L_EX) + w * 640 + lane;
    LAS const float* EXR = (LAS const float*)(L + L_EX) + (w ^ 1) * 640 + lane;
    auto put_partial = [&]() {
#pragma unroll
        for (int i = 0; i < 9; ++i) EXW[i * 64] = kh ? oa[i] : ob[i];
        EXW[9 * 64] = kh ? ma_ : mb_;
    };
    put_partial();
    if (__any(bad) && lane == 0) *(LAS volatile unsigned*)(L + L_BAD) = 1;
    BAR();
    if (__builtin_amdgcn_readfirstlane(*(LAS volatile unsigned*)(L + L_BAD))) {
        oa = f32x16{}; ob = f32x16{}; ma_ = -INFINITY; mb_ = -INFINITY;
        for (int t = 0; t < 32; ++t) {
            const bf16x8 kf = ld16(rs, vK, KVOFF(t)), v0 = ld16(rs, vV0, KVOFF(t)), v1 = ld16(rs, vV1, KVOFF(t));
#pragma unroll
            for (int z = 0; z < 2; ++z) {
                f32x16& o = z ? ob : oa; float& m = z ? mb_ : ma_;
                f32x16 s = MFMA(kf, z ? qb : qa, f32x16{});
                const float mn = fmx(max16(s), m), al = EXP2(m - mn);
                m = mn;
#pragma unroll
                for (int i = 0; i < 16; ++i) { o[i] *= al; s[i] = EXP2(s[i] - mn); }
                o = MFMA(v0, pack8(s, 0), o);
                o = MFMA(v1, pack8(s, 1), o);
            }
        }
        put_partial();
        BAR();
    }
    STAMP(ws, 0, 12);
    {
        f32x16 o = kh ? ob : oa;
        const float m0 = kh ? mb_ : ma_, m1 = EXR[9 * 64], mm = fmx(m0, m1), f0 = EXP2(m0 - mm), f1 = EXP2(m1 - mm);
#pragma unroll
        for (int i = 0; i < 9; ++i) o[i] = o[i] * f0 + EXR[i * 64] * f1;
        const float rl = gate * __builtin_amdgcn_rcpf(o[8]);
#pragma unroll
        for (int i = 0; i < 8; ++i) o[i] *= rl;
        st16(rs, __builtin_bit_cast(bf16x8, __builtin_elementwise_min(__builtin_bit_cast(u32x4, pack8(o, 0)), (u32x4){0xFFFFFFFEu, 0xFFFFFFFEu, 0xFFFFFFFEu, 0xFFFFFFFEu})),
             lane * 16, WS_O + ((2 * tl + kh) * 32 + head) * 1024);
        SBAR();
    }
    f32x16 acc[2] = {};
    const int wpo = kq * 16384 + (4 * h + q4) * 128 + trl;
#pragma unroll
    for (int k = 0; k < 8; ++k)
#pragma unroll
        for (int nt = 0; nt < 2; ++nt) acc[nt] = MFMA(trfrag(L + L_WOB + wpo + k * 2048 + ((nt ^ (q4 >> 1)) * 64), 1024), k < 4 ? kr[k & 3] : v0r[k & 3], acc[nt]);
    STAMP(ws, 0, 6);
    {
        const unsigned oao = WS_O + ((2 * otl + ot) * 32 + 8 * kq) * 1024;
        bf16x8 of[8], wa[8][2];
#pragma unroll
        for (int k = 0; k < 8; ++k) of[k] = ld16(rs, lane * 16 + (k & 3) * 1024, oao + (k & 4) * 1024);
        SBAR();
#pragma unroll
        for (int k = 0; k < 8; ++k)
#pragma unroll
            for (int nt = 0; nt < 2; ++nt) wa[k][nt] = trfrag(L + L_WOA + wpo + k * 2048 + ((nt ^ (q4 >> 1)) * 64), 1024);
        BAR();
#pragma unroll
        for (int k = 0; k < 8; ++k) {
            for (unsigned spins = 0; spins < SPIN_CAP; ++spins) {
                const u32x4 v = __builtin_bit_cast(u32x4, of[k]);
                if (!__any(__builtin_elementwise_max(__builtin_elementwise_max(v[0], v[1]), __builtin_elementwise_max(v[2], v[3])) == 0xFFFFFFFFu)) break;
                asm volatile("" ::: "memory");
#pragma unroll
                for (int z = k; z < 8; ++z) of[z] = ld16(rs, lane * 16 + (z & 3) * 1024, oao + (z & 4) * 1024);
            }
            if (k == 7) STAMP(ws, 0, 7);
            acc[0] = MFMA(wa[k][0], of[k], acc[0]);
            acc[1] = MFMA(wa[k][1], of[k], acc[1]);
        }
    }
    LAS char* PR = L + L_WOB + kq * 16384 + ot * 8192;
#pragma unroll
    for (int z = 0; z < 4; ++z)
#pragma unroll
        for (int g = 0; g < 2; ++g) {
            const int b = 8 * (z & 1) + 4 * g;
            *(LAS f32x4*)(PR + (z * 2 + g) * 1024 + lane * 16) = (f32x4){acc[z >> 1][b], acc[z >> 1][b + 1], acc[z >> 1][b + 2], acc[z >> 1][b + 3]};
        }
    STAMP(ws, 0, 8);
    BAR();
    {
        f32x4 r0 = {}, r1 = {};
#pragma unroll
        for (int z = 0; z < 4; ++z) {
            LAS const char* src = L + L_WOB + z * 16384 + ot * 8192 + kq * 2048 + lane * 16;
            r0 += *(LAS const f32x4*)src; r1 += *(LAS const f32x4*)(src + 1024);
        }
        unsigned a0 = pk2(r0[0], r0[1]), a1 = pk2(r0[2], r0[3]), b0 = pk2(r1[0], r1[1]), b1 = pk2(r1[2], r1[3]);
        auto s0_ = __builtin_amdgcn_permlane32_swap(a0, b0, false, false);
        auto s1_ = __builtin_amdgcn_permlane32_swap(a1, b1, false, false);
        *(u32x4*)(out + (size_t)(64 * otl + 32 * ot + r32) * 512 + 64 * ocg + 16 * kq + 8 * h) = (u32x4){s0_[0], s1_[0], s0_[1], s1_[1]};
    }
    if (blockIdx.x == 0 && tid == 0) set_flag(ws, WS_EP, tag);
    STAMP(ws, 0, 9);
    TS_DUMP(ws);
}

extern "C" void kernel_launch(void* const* d_in, const int* in_sizes, int n_in, void* d_out, int out_size, void* d_ws, size_t ws_size, hipStream_t stream) {
    fused_kernel<<<256, 768, 0, stream>>>((const uint16_t*)d_in[0], (const uint16_t*)d_in[1], (const uint16_t*)d_in[4], (const uint16_t*)d_in[5], (const uint16_t*)d_in[7],
                                          (const uint16_t*)d_in[2], (const uint16_t*)d_in[6], (uint16_t*)d_out, (char*)d_ws);
}
```

```cpp
#include <hip/hip_runtime.h>
#include <hip/hip_bf16.h>
#include <stdint.h>
#define STAMP(ws, k, i)
#define TS_DECL
#define TS(i)
#define TS_DUMP(ws)

typedef short bf16x8 __attribute__((ext_vector_type(8)));
typedef short s16x4 __attribute__((ext_vector_type(4)));
typedef float f32x16 __attribute__((ext_vector_type(16)));
typedef float f32x4 __attribute__((ext_vector_type(4)));
typedef float f32x2 __attribute__((ext_vector_type(2)));
typedef unsigned u32x4 __attribute__((ext_vector_type(4)));
typedef unsigned u32x2 __attribute__((ext_vector_type(2)));
typedef __bf16 bf16x2_t __attribute__((ext_vector_type(2)));
typedef unsigned long long u64;
#define LAS __attribute__((address_space(3)))
#define GAS __attribute__((address_space(1)))
#define DI __device__ __forceinline__
#define MFMA(a, b, c) __builtin_amdgcn_mfma_f32_32x32x16_bf16((a), (b), (c), 0, 0, 0)
#define SBAR() __builtin_amdgcn_sched_barrier(0)
#define RLX_AGENT __ATOMIC_RELAXED, __HIP_MEMORY_SCOPE_AGENT
#define EXP2(x) __builtin_amdgcn_exp2f(x)

constexpr float LOG2E = 1.4426950408889634f;
constexpr unsigned REC = 2176;
constexpr unsigned WS_KV = 0;
constexpr unsigned WS_O = WS_KV + 32 * 64 * REC;
constexpr unsigned WS_PK = WS_O + (4u << 20);
constexpr unsigned WS_KVF = WS_PK + 32 * 32 * 2176;
constexpr unsigned WS_F1 = WS_KVF + 32 * 2176;
constexpr unsigned WS_F2 = WS_F1 + 2048;
constexpr unsigned WS_F3 = WS_F2 + 8192;
constexpr unsigned WS_F5 = WS_F3 + 8192;
constexpr unsigned WS_F4 = WS_F5 + 8192;
constexpr unsigned WS_EP = WS_F4 + 16384;
constexpr unsigned WS_END = WS_EP + 128;
constexpr int L_X = 0, L_RING = 65536;
constexpr int L_PHK = 65536, L_PHQ = 81920;
constexpr int L_WOA = 0, L_WOB = 65536;
constexpr int L_WPHI = 131072, L_G = L_WPHI + 2048, L_BAD = L_G + 256, L_SEEN = L_BAD + 4, L_MCNT = L_BAD + 8, L_HCNT = L_BAD + 12, L_OBS = L_BAD + 16, L_DUMMY = L_BAD + 256;
constexpr int L_QA = L_WPHI + 3072, L_KA = L_QA + 8192, L_VA = L_KA + 8192, L_EX = L_QA;
constexpr int LDS_BYTES = 163840;
#define KV_SPLIT 1
#define TOUCH_DELAY 4
#define WO_T0 4500
#define WO_DT 1700
#define PK_DELAY 24
#define KV_AUX 16
#define F1_STEP 13
#define PRIO_SWITCH 24
constexpr unsigned SPIN_CAP = 1u << 20;

DI unsigned pk2(float a, float b) { f32x2 v = {a, b}; return __builtin_bit_cast(unsigned, __builtin_convertvector(v, bf16x2_t)); }
DI bf16x8 pack8(const f32x16& x, int s) {
    u32x4 p = {pk2(x[8 * s], x[8 * s + 1]), pk2(x[8 * s + 2], x[8 * s + 3]), pk2(x[8 * s + 4], x[8 * s + 5]), pk2(x[8 * s + 6], x[8 * s + 7])};
    return __builtin_bit_cast(bf16x8, p);
}
DI float fmx(float a, float b) { return __builtin_elementwise_maximum(a, b); }
DI float max32(float v) { auto r = __builtin_amdgcn_permlane32_swap(__float_as_uint(v), __float_as_uint(v), false, false); return fmx(__uint_as_float(r[0]), __uint_as_float(r[1])); }
DI float lmax16(const f32x16& s) {
    const float a = fmx(fmx(s[0], s[1]), s[2]), b = fmx(fmx(s[3], s[4]), s[5]), c = fmx(fmx(s[6], s[7]), s[8]), d = fmx(fmx(s[9], s[10]), s[11]), e = fmx(fmx(s[12], s[13]), s[14]);
    return fmx(fmx(fmx(a, b), c), fmx(fmx(d, e), s[15]));
}
DI float max16(const f32x16& s) { return max32(lmax16(s)); }
DI void glds2(unsigned v0, unsigned v1, const void* sbase, unsigned ldsdst) {
    unsigned keep;
    asm volatile("s_mov_b32 %0, m0\n\ts_mov_b32 m0, %4\n\ts_nop 0\n\tglobal_load_lds_dwordx4 %1, %3\n\tglobal_load_lds_dwordx4 %2, %3 offset:1024\n\ts_mov_b32 m0, %0"
                 : "=&s"(keep) : "v"(v0), "v"(v1), "s"(sbase), "s"(ldsdst) : "memory");
}
DI void glds4(const unsigned (&v)[4], const void* sbase, unsigned ldsdst) {
    unsigned keep;
    asm volatile("s_mov_b32 %0, m0\n\ts_mov_b32 m0, %6\n\ts_nop 0\n\tglobal_load_lds_dwordx4 %1, %5\n\tglobal_load_lds_dwordx4 %2, %5 offset:1024\n\t"
                 "global_load_lds_dwordx4 %3, %5 offset:2048\n\tglobal_load_lds_dwordx4 %4, %5 offset:3072\n\ts_mov_b32 m0, %0"
                 : "=&s"(keep) : "v"(v[0]), "v"(v[1]), "v"(v[2]), "v"(v[3]), "s"(sbase), "s"(ldsdst) : "memory");
}
DI void touch(unsigned voff, const void* sbase, unsigned ldsdst) {
    unsigned keep;
    asm volatile("s_mov_b32 %0, m0\n\ts_mov_b32 m0, %3\n\ts_nop 0\n\tglobal_load_lds_dword %1, %2 sc1\n\ts_mov_b32 m0, %0"
                 : "=&s"(keep) : "v"(voff), "s"(sbase), "s"(ldsdst) : "memory");
}
typedef short v4i16_t __attribute__((ext_vector_type(4)));
DI s16x4 trread(LAS const char* p) { return __builtin_bit_cast(s16x4, __builtin_amdgcn_ds_read_tr16_b64_v4i16((LAS v4i16_t*)p)); }
DI bf16x8 trfrag(LAS const char* p, int d2) { s16x4 lo = trread(p), hi = trread(p + d2); return __builtin_shufflevector(lo, hi, 0, 1, 2, 3, 4, 5, 6, 7); }
DI bf16x8 ldsfrag(LAS const char* p) { return *(LAS const bf16x8*)p; }
#define BAR() asm volatile("s_waitcnt lgkmcnt(0)\n\ts_barrier" ::: "memory")
#define VM_BAR(N) asm volatile("s_waitcnt vmcnt(" #N ") lgkmcnt(0)\n\ts_barrier" ::: "memory")
#define DRAIN() asm volatile("s_waitcnt vmcnt(0)" ::: "memory")
typedef __amdgpu_buffer_rsrc_t rsrc_t;
DI bf16x8 ld16(rsrc_t r, unsigned voff, unsigned soff) { return __builtin_bit_cast(bf16x8, __builtin_amdgcn_raw_buffer_load_b128(r, voff, soff, 16)); }
DI void st16(rsrc_t r, bf16x8 v, unsigned voff, unsigned soff) { __builtin_amdgcn_raw_buffer_store_b128(__builtin_bit_cast(u32x4, v), r, voff, soff, 16); }
DI void st16kv(rsrc_t r, bf16x8 v, unsigned voff, unsigned soff) { __builtin_amdgcn_raw_buffer_store_b128(__builtin_bit_cast(u32x4, v), r, voff, soff, KV_AUX); }
DI unsigned lds_word(LAS char* p) { return __builtin_amdgcn_readfirstlane(*(LAS volatile unsigned*)p); }
DI void lds_wait(LAS char* p, unsigned want) {
    for (unsigned spins = 0; spins < (SPIN_CAP << 6) && lds_word(p) < want; ++spins) __builtin_amdgcn_s_sleep(1);
    asm volatile("" ::: "memory");
}
DI void lds_arrive(LAS char* p, int lane) {
    asm volatile("s_waitcnt lgkmcnt(0)" ::: "memory");
    if (lane == 0) __hip_atomic_fetch_add((LAS unsigned*)p, 1u, __ATOMIC_RELAXED, __HIP_MEMORY_SCOPE_WORKGROUP);
}
DI void set_flag(char* ws, unsigned off, u64 tag) { __hip_atomic_store((GAS u64*)(ws + off), tag, RLX_AGENT); }
template <int FIRST, int GAP> DI void wait_flags(char* ws, unsigned off, int n, u64 tag, int lane) {
    GAS u64* p = (GAS u64*)(ws + off) + (lane < n ? lane : 0);
    if (FIRST) __builtin_amdgcn_s_sleep(FIRST);
    for (unsigned spins = 0; spins < SPIN_CAP; ++spins) {
        const u64 v = __hip_atomic_load(p, RLX_AGENT);
        if (__all(v == tag)) break;
        __builtin_amdgcn_s_sleep(GAP);
    }
    __builtin_amdgcn_fence(__ATOMIC_ACQUIRE, "wavefront");
    asm volatile("" ::: "memory");
}

DI void softmax16(f32x16& p) {
    const float nm = -lmax16(p) * LOG2E;
    f32x2 e[8];
#pragma unroll
    for (int i = 0; i < 8; ++i) { e[i] = __builtin_elementwise_fma((f32x2){p[2 * i], p[2 * i + 1]}, (f32x2){LOG2E, LOG2E}, (f32x2){nm, nm}); e[i][0] = EXP2(e[i][0]); e[i][1] = EXP2(e[i][1]); }
    const f32x2 t = ((e[0] + e[1]) + (e[2] + e[3])) + ((e[4] + e[5]) + (e[6] + e[7]));
    const float rs = __builtin_amdgcn_rcpf(t[0] + t[1]);
#pragma unroll
    for (int i = 0; i < 8; ++i) { e[i] *= rs; p[2 * i] = e[i][0]; p[2 * i + 1] = e[i][1]; }
}

__global__ __launch_bounds__(768) void fused_kernel(const uint16_t* __restrict__ x, const uint16_t* __restrict__ WA, const uint16_t* __restrict__ WB,
                                                    const uint16_t* __restrict__ wphi, const uint16_t* __restrict__ gatew,
                                                    const uint16_t* __restrict__ woA, const uint16_t* __restrict__ woB,
                                                    uint16_t* __restrict__ out, char* ws) {
    __shared__ __attribute__((aligned(16))) char lds[LDS_BYTES];
    LAS char* L = (LAS char*)lds;
    const u64 pc0 = __builtin_amdgcn_s_getpc();
    const unsigned lds0 = (unsigned)(uintptr_t)lds;
    const int tid = threadIdx.x, lane = tid & 63, w = __builtin_amdgcn_readfirstlane(tid >> 6);
    const int r32 = lane & 31, h = lane >> 5;
    const int xcd = blockIdx.x & 7, idx = blockIdx.x >> 3;
    const int qd = 2 * (xcd & 3) + (idx & 1), tl = 2 * (idx >> 1) + (xcd >> 2);
    const int s0 = ((tl >> 1) + 4 * (qd & 1)) & 7;
    const int otl = 4 * xcd + (idx >> 3), ocg = idx & 7;
    const rsrc_t rs = __builtin_amdgcn_make_buffer_rsrc(ws, 0, WS_END, 0x00020000);
    u64 tag;
#define EPOCH_BAR(N) do { asm volatile("s_load_dwordx2 %0, %1, 0x0\n\ts_waitcnt vmcnt(" #N ") lgkmcnt(0)\n\ts_barrier" : "=s"(tag) : "s"(ws + WS_EP) : "memory"); tag += 1; } while (0)
    const int q4 = (lane & 15) >> 2;
    const int L_VB = L_X + s0 * 8192;
    const int trl = ((lane >> 4) & 1) * 32 + (lane & 3) * 8;
    STAMP(ws, 0, 0);
    TS_DECL; TS(0); TS(16);

    if (w >= 8) {
        const int j = w - 8;
        const unsigned vsw = (unsigned)(((lane & 7) ^ (((lane >> 4) & 1) << 2)) << 4);
        unsigned vw[4];
#pragma unroll
        for (int u = 0; u < 4; ++u) vw[u] = (unsigned)((lane >> 3) * 3072) + vsw + u * (8 * 3072 - 1024);
        const unsigned vx0 = (unsigned)((lane >> 3) * 1024 + (((lane & 7) ^ (lane >> 4)) << 4)), vx1 = (vx0 ^ 64u) + 8192 - 1024;
        const char* xb = (const char*)x + (size_t)(64 * tl + 16 * j) * 1024;
        const char* w1b = (const char*)WA + (size_t)((j & 1) * 32) * 3072 + qd * 128 + 1024 + (j >> 1) * 1024;
        const char* w2b = (j == 0 ? (const char*)WA : (const char*)WB + (j - 1) * 1024) + qd * 128;
        auto issue_x = [&](int i) { const int kc = (s0 + i) & 7; glds2(vx0, vx1, xb + kc * 128, lds0 + L_X + kc * 8192 + j * 2048); };
        auto issue_w1 = [&](int i) {
            glds4(vw, w1b + ((s0 + i) & 7) * (64 * 3072), lds0 + L_RING + ((i + 4) % 6) * 16384 + j * 4096);
        };
        auto issue_w2 = [&](int i) {
            const char* wb = w2b + ((s0 + i) & 7) * (64 * 3072);
            glds4(vw, wb, lds0 + L_RING + (i % 3) * 32768 + j * 8192);
            glds4(vw, wb + 32 * 3072, lds0 + L_RING + (i % 3) * 32768 + j * 8192 + 4096);
        };
        __builtin_amdgcn_s_setprio(3);
        issue_w1(0); issue_x(0); issue_x(1); issue_x(2); issue_x(3);
        bf16x8 gw, wph;
        asm volatile("global_load_dwordx4 %0, %1, %2" : "=v"(gw) : "v"((lane & 15) * 64 + (lane >> 4) * 16), "s"(gatew) : "memory");
        asm volatile("global_load_dwordx4 %0, %1, %2" : "=v"(wph) : "v"(lane * 16), "s"(wphi + qd * 1024 + (j & 1) * 512) : "memory");
        issue_w1(1);
        VM_BAR(12); issue_w1(2); issue_w1(3);
        VM_BAR(8); issue_x(4); issue_w1(4);
        EPOCH_BAR(10); issue_x(5); issue_w1(5);
        VM_BAR(12); issue_x(6); issue_w1(6);
        VM_BAR(12); issue_x(7); issue_w1(7);
        VM_BAR(12); issue_w2(0);
        VM_BAR(14); issue_w2(1);
        VM_BAR(16);
        f32x4 gl = {};
        const int grow = 16 * j + (lane & 15);
        int gxa = L_X + grow * 128 + (((lane >> 4) ^ ((grow >> 1) & 7)) << 4);
#pragma unroll
        for (int i = 8; i < 16; ++i) {
            if (i < 15) VM_BAR(8); else VM_BAR(0);
            if (i == 8) asm volatile("" : "+v"(gw), "+v"(wph));
            if (i < 14) issue_w2(i - 6);
            if (i == 14) {
                if (j < 2) *(LAS bf16x8*)(L + L_WPHI + j * 1024 + lane * 16) = wph;
                if (j == 2 && lane < 8) *(LAS unsigned*)(L + L_BAD + lane * 4) = 0;
            }
            const int kc = (s0 + i) & 7;
#pragma unroll
            for (int u = 0; u < 2; ++u) {
                const bf16x8 xg = ldsfrag(L + (gxa ^ (u << 6)) + kc * 8192);
                gl = __builtin_amdgcn_mfma_f32_16x16x32_bf16((lane & 15) == 2 * kc + u ? gw : bf16x8{}, xg, gl, 0, 0, 0);
            }
        }
        {
            float z = (gl[0] + gl[1]) + (gl[2] + gl[3]);
            z += __shfl_xor(z, 16); z += __shfl_xor(z, 32);
            if (lane < 16) *(LAS float*)(L + L_G + grow * 4) = __builtin_amdgcn_rcpf(1.f + EXP2(-z * LOG2E));
        }
        TS(1);
        if (j == 0) {
            wait_flags<0, 2>(ws, WS_F1 + qd * 256, 32, tag, lane);
            *(LAS volatile unsigned*)(L + L_SEEN) = 1;
        }
        TS(2);
        BAR();
        TS(3);
        const u64 tp2 = __builtin_amdgcn_s_memtime();
        const int head = 4 * qd + j;
        bf16x8 phq[2][2]; float g2[2];
        {
            f32x16 kv = {};
            const bool keep = (r32 >> 4) == (j & 1);
            bf16x8 pf[2][2], ve[2][2];
#pragma unroll
            for (int tt = 0; tt < 2; ++tt) {
#pragma unroll
                for (int s = 0; s < 2; ++s) {
                    pf[tt][s] = trfrag(L + L_PHK + (j * 2 + tt) * 2048 + (16 * s + 4 * h + q4) * 64 + trl, 512);
                    ve[tt][s] = ldsfrag(L + L_VB + (((j >> 1) * 2 + tt) * 2 + s) * 1024 + lane * 16);
                    phq[tt][s] = ldsfrag(L + L_PHQ + ((j * 2 + tt) * 2 + s) * 1024 + lane * 16);
                }
                g2[tt] = 1.f - *(LAS const float*)(L + L_G + (tt * 32 + r32) * 4);
            }
            lds_arrive(L + L_HCNT, lane);
            __builtin_amdgcn_s_sleep(PK_DELAY);
#pragma unroll
            for (int tt = 0; tt < 2; ++tt)
#pragma unroll
                for (int s = 0; s < 2; ++s) {
#pragma unroll
                    for (int e = 0; e < 8; ++e) ve[tt][s][e] = keep ? ve[tt][s][e] : (short)0x3F80;
                    kv = MFMA(pf[tt][s], ve[tt][s], kv);
                }
            const int col = keep ? (r32 & 15) : 16;
            const unsigned vo = (keep || (r32 & 15) == 0) ? (unsigned)((col * 2 + h) * 16) : 0x40000000u;
#pragma unroll
            for (int g = 0; g < 4; ++g) st16(rs, __builtin_bit_cast(bf16x8, (f32x4){kv[4 * g], kv[4 * g + 1], kv[4 * g + 2], kv[4 * g + 3]}), vo + g * 544, WS_PK + (head * 32 + tl) * 2176);
            DRAIN();
            if (lane == 0) set_flag(ws, WS_F2 + (head * 32 + tl) * 8, tag);
        }
        unsigned vo[4];
#pragma unroll
        for (int u = 0; u < 4; ++u) vo[u] = (unsigned)((lane >> 3) * 1024) + vsw + u * (8192 - 1024);
        lds_wait(L + L_HCNT, 4);
        int wo_done = 0;
        auto wo_service = [&]() {
            if (wo_done < 2 && __builtin_amdgcn_s_memtime() - tp2 >= (u64)(WO_T0 + WO_DT * (j + 4 * wo_done))) {
                const char* src = (const char*)(wo_done ? woA : woB) + (size_t)(128 * j) * 1024 + ocg * 128;
#pragma unroll
                for (int i = 0; i < 4; ++i) glds4(vo, src + i * 32768, lds0 + (wo_done ? L_WOA : L_WOB) + j * 16384 + i * 4096);
                ++wo_done;
            }
        };
        auto wait_slow = [&](unsigned off, int n) {
            GAS u64* p = (GAS u64*)(ws + off) + (lane < n ? lane : 0);
            for (unsigned spins = 0; spins < SPIN_CAP; ++spins) {
#pragma unroll 1
                for (int z = 0; z < (spins ? 2 : 6); ++z) { wo_service(); __builtin_amdgcn_s_sleep(6); }
                if (__all(__hip_atomic_load(p, RLX_AGENT) == tag)) break;
            }
            __builtin_amdgcn_fence(__ATOMIC_ACQUIRE, "wavefront");
            asm volatile("" ::: "memory");
        };
        STAMP(ws, 0, 1);
        if (tl < 17) {
            wait_slow(WS_F2 + head * 256, 32);
            const unsigned so = WS_PK + head * 32 * 2176 + tl * 128, v1 = (unsigned)((lane >> 3) * 2176 + (lane & 7) * 16);
            f32x4 a = (__builtin_bit_cast(f32x4, ld16(rs, v1, so)) + __builtin_bit_cast(f32x4, ld16(rs, v1, so + 8 * 2176))) +
                      (__builtin_bit_cast(f32x4, ld16(rs, v1, so + 16 * 2176)) + __builtin_bit_cast(f32x4, ld16(rs, v1, so + 24 * 2176)));
#pragma unroll
            for (int o = 8; o < 64; o <<= 1) {
#pragma unroll
                for (int e = 0; e < 4; ++e) a[e] += __shfl_xor(a[e], o);
            }
            st16(rs, __builtin_bit_cast(bf16x8, a), lane < 8 ? (unsigned)(lane * 16) : 0x40000000u, WS_KVF + head * 2176 + tl * 128);
            DRAIN();
            if (lane == 0) set_flag(ws, WS_F3 + (head * 32 + tl) * 8, tag);
        } else {
#pragma unroll 1
            for (int z = 0; z < 10; ++z) { wo_service(); __builtin_amdgcn_s_sleep(6); }
        }
        STAMP(ws, 0, 2);
        wait_slow(WS_F3 + head * 256, 17);
        {
            const unsigned v2 = (unsigned)(((r32 < 16 ? r32 : 16) * 2 + h) * 16);
            f32x16 kv;
#pragma unroll
            for (int g = 0; g < 4; ++g) { const f32x4 t = __builtin_bit_cast(f32x4, ld16(rs, v2 + g * 544, WS_KVF + head * 2176)); kv[4 * g] = t[0]; kv[4 * g + 1] = t[1]; kv[4 * g + 2] = t[2]; kv[4 * g + 3] = t[3]; }
            const bf16x8 k0 = pack8(kv, 0), k1 = pack8(kv, 1);
#pragma unroll
            for (int tt = 0; tt < 2; ++tt) {
                f32x16 ob = MFMA(k0, phq[tt][0], f32x16{});
                ob = MFMA(k1, phq[tt][1], ob);
                const float rd = g2[tt] * __builtin_amdgcn_rcpf(ob[8] + 1e-6f);
#pragma unroll
                for (int i = 0; i < 8; ++i) ob[i] *= rd;
                st16(rs, pack8(ob, 0), lane * 16, WS_O + (2u << 20) + ((2 * tl + tt) * 32 + head) * 1024);
                st16(rs, (bf16x8){-1, -1, -1, -1, -1, -1, -1, -1}, lane * 16, WS_O + ((2 * tl + tt) * 32 + head) * 1024);
            }
            DRAIN();
            if (lane == 0) set_flag(ws, WS_F5 + (tl * 32 + head) * 8, tag);
        }
        STAMP(ws, 0, 3);
        wait_slow(WS_F5 + (otl * 32 + 8 * j) * 8, 8);
        lds_arrive(L + L_OBS, lane);
        if (lane < 16) touch((lane >> 3) * 32768 + (lane & 7) * 128, ws + WS_O + (2u << 20) + (2 * otl * 32 + 8 * j + ocg) * 1024, lds0 + L_DUMMY);
        while (wo_done < 2) { wo_service(); __builtin_amdgcn_s_sleep(6); }
        STAMP(ws, 0, 5);
        VM_BAR(0);
        return;
    }

    if (w == 0) {
        const char* a = (const char*)(pc0 & ~127ull) + (idx * 9 + lane) * 128;
        if (lane == 9) {
            a = ws + (size_t)(idx < 22 ? idx : 0) * 524288;
            if (idx >= 22) a = idx < 25 ? (const char*)WB + (idx - 22) * 786368 : idx < 28 ? (const char*)out + (idx - 25) * 1048512 : idx < 30 ? (const char*)woA + (idx - 28) * 524224 : (const char*)woB + (idx - 30) * 524224;
        }
        unsigned keep;
        if (lane < 10) asm volatile("s_mov_b32 %0, m0\n\ts_mov_b32 m0, %2\n\ts_nop 0\n\tglobal_load_lds_dword %1, off\n\ts_mov_b32 m0, %0" : "=&s"(keep) : "v"(a), "s"(lds0 + L_RING + 3 * 16384) : "memory");
    }
    const int role = w >> 2, p = (w >> 1) & 1, tt = w & 1;
    const int hq = w >> 1, kh = w & 1, head = 4 * qd + hq;
    const unsigned vK = lane * 16;
    const unsigned vV0 = r32 < 16 ? (unsigned)(1024 + h * 256 + r32 * 16) : ((r32 & 11) == 0 ? 2048u : 0x40000000u);
    const unsigned vV1 = r32 < 16 ? vV0 + 512 : vV0;
    const unsigned kvb = WS_KV + (head * 64 + kh) * REC;
#define KVOFF(t) (kvb + (((t) + tl) & 31) * (2 * REC))
    bf16x8 kr[4], v0r[4], v1r[4];
    {
        const int xrow = tt * 32 + r32;
        const int xa0 = L_X + xrow * 128 + ((h ^ ((xrow >> 1) & 7)) << 4);
        const int wa0 = L_RING + (8 * h + q4) * 128 + ((p ^ (q4 >> 1)) * 64) + trl;
        bf16x8 xf[4], w0[4], w1[4], own[2];
        f32x16 a0 = {}, a1 = {};
        const int tile = 2 * tl + tt;
        int wsb[3], xsb[4];
#pragma unroll
        for (int i = 0; i < 3; ++i) { wsb[i] = wa0 + i * 32768; asm volatile("" : "+v"(wsb[i])); }
#pragma unroll
        for (int s = 0; s < 4; ++s) { xsb[s] = xa0 ^ (s << 5); asm volatile("" : "+v"(xsb[s])); }
        auto reads = [&](int i, int s, int ROLE) {
            const int kc = (s0 + i) & 7;
            xf[s] = ldsfrag(L + (xsb[s] + kc * 8192));
            if (i < 8) w0[s] = trfrag(L + wsb[((i + 4) % 6) >> 1] + (i & 1) * 16384 + ROLE * 8192 + s * 2048, 512);
            else {
                LAS const char* wsl = L + wsb[(i - 8) % 3] + ROLE * 16384 + s * 2048;
                w0[s] = trfrag(wsl, 512);
                w1[s] = trfrag(wsl + 8192, 512);
            }
        };
        auto mmas = [&](int i, int s, int ROLE) {
            if (i < 8) a0 = ROLE ? MFMA(xf[s], w0[s], a0) : MFMA(w0[s], xf[s], a0);
            else {
                a0 = MFMA(w0[s], xf[s], a0);
                a1 = ROLE ? MFMA(xf[s], w1[s], a1) : MFMA(w1[s], xf[s], a1);
            }
        };
        auto publish = [&](int ROLE, int part) {
            const unsigned so = WS_KV + ((4 * qd + 2 * p) * 64 + tile) * REC;
            if (ROLE == 0) st16kv(rs, own[part], lane * 16, so + part * 64 * REC);
            else {
                st16kv(rs, own[part], (unsigned)((r32 >> 4) * 64 * REC + 1024 + h * 256 + (r32 & 15) * 16) + part * 512, so);
                const bf16x8 ones = {0x3F80, 0x3F80, 0x3F80, 0x3F80, 0x3F80, 0x3F80, 0x3F80, 0x3F80};
                if (part == 0) st16kv(rs, ones, lane < 2 ? (unsigned)(lane * 64 * REC + 2048) : 0x40000000u, so);
            }
        };
        auto gemm = [&](int ROLE) {
#pragma unroll
            for (int i = 0; i < 16; ++i) {
                if (i == F1_STEP) { VM_BAR(0); if (tid == 0) set_flag(ws, WS_F1 + (qd * 32 + tl) * 8, tag); } else if (i == 0) EPOCH_BAR(63); else if (i == 2) VM_BAR(0); else BAR();
                if (i < 15) TS(i + 1); else TS(17);
                SBAR();
                if (ROLE == 1 && i > 0) {
#pragma unroll
                    for (int s = 0; s < 4; ++s) mmas(i - 1, s, ROLE);
                    SBAR();
                }
#pragma unroll
                for (int s = 0; s < 4; ++s) reads(i, s, ROLE);
                SBAR();
                if (ROLE == 0) {
#pragma unroll
                    for (int s = 0; s < 4; ++s) mmas(i, s, ROLE);
                    SBAR();
                }
                if (i == 7 + ROLE) { own[0] = pack8(a0, 0); own[1] = pack8(a0, 1); a0 = f32x16{}; publish(ROLE, 0); if (!KV_SPLIT) publish(ROLE, 1); }
                if (KV_SPLIT && i == 7 + ROLE + KV_SPLIT) publish(ROLE, 1);
                SBAR();
            }
            bf16x8 wf[2];
#pragma unroll
            for (int a = 0; a < 2; ++a) wf[a] = trfrag(L + L_WPHI + (2 * p + a) * 512 + (4 * h + q4) * 32 + (((lane >> 4) & 1) * 2 + ((lane >> 1) & 1)) * 8, 256);
            TS(18);
            if (ROLE == 1) {
#pragma unroll
                for (int s = 0; s < 4; ++s) mmas(15, s, ROLE);
            }
            const short sg = (lane & 4) ? (short)0x8000 : (short)0;
#pragma unroll
            for (int a = 0; a < 2; ++a)
#pragma unroll
                for (int e = 0; e < 8; ++e) wf[a][e] ^= sg;
            const f32x16& fin = ROLE ? a0 : a1;
            f32x16 f0 = MFMA(wf[0], pack8(fin, 0), f32x16{}), f1 = MFMA(wf[1], pack8(fin, 1), f32x16{});
            if (ROLE == 0) {
                const float qs = 0.25f * LOG2E;
#pragma unroll
                for (int i = 0; i < 16; ++i) a0[i] *= qs;
#pragma unroll
                for (int a = 0; a < 2; ++a) {
                    *(LAS bf16x8*)(L + L_QA + ((2 * p + a) * 2 + tt) * 1024 + lane * 16) = pack8(a0, a);
                    *(LAS bf16x8*)(L + L_KA + ((2 * p + a) * 2 + tt) * 1024 + lane * 16) = own[a];
                }
            } else {
#pragma unroll
                for (int s = 0; s < 2; ++s) {
                    *(LAS bf16x8*)(L + L_VB + ((p * 2 + tt) * 2 + s) * 1024 + lane * 16) = pack8(a1, s);
                    *(LAS bf16x8*)(L + L_VA + ((p * 2 + tt) * 2 + s) * 1024 + lane * 16) = own[s];
                }
            }
            TS(19);
            lds_wait(L + L_SEEN, 1);
#pragma unroll
            for (int t = 1; t < 3; ++t) { kr[t] = ld16(rs, vK, KVOFF(t)); v0r[t] = ld16(rs, vV0, KVOFF(t)); v1r[t] = ld16(rs, vV1, KVOFF(t)); SBAR(); }
            TS(22);
            softmax16(f0); softmax16(f1);
#pragma unroll
            for (int a = 0; a < 2; ++a) {
                const f32x16& f = a ? f1 : f0;
                if (ROLE == 0) {
#pragma unroll
                    for (int s = 0; s < 2; ++s) *(LAS bf16x8*)(L + L_PHQ + (((2 * p + a) * 2 + tt) * 2 + s) * 1024 + lane * 16) = pack8(f, s);
                } else {
#pragma unroll
                    for (int g = 0; g < 4; ++g)
                        *(LAS u32x2*)(L + L_PHK + ((2 * p + a) * 2 + tt) * 2048 + r32 * 64 + 8 * (2 * g + h)) = (u32x2){pk2(f[4 * g], f[4 * g + 1]), pk2(f[4 * g + 2], f[4 * g + 3])};
                }
            }
        };
        if (role == 0) gemm(0); else gemm(1);
    }
    TS(20);
    BAR();
    TS(21);
    const bf16x8 qa = ldsfrag(L + L_QA + (hq * 2) * 1024 + lane * 16), qb = ldsfrag(L + L_QA + (hq * 2 + 1) * 1024 + lane * 16);
    kr[0] = ldsfrag(L + L_KA + (hq * 2 + kh) * 1024 + lane * 16);
    v0r[0] = ldsfrag(L + L_VA + (((hq >> 1) * 2 + kh) * 2) * 1024 + (h * 32 + (hq & 1) * 16 + (r32 & 15)) * 16);
    v1r[0] = ldsfrag(L + L_VA + (((hq >> 1) * 2 + kh) * 2 + 1) * 1024 + (h * 32 + (hq & 1) * 16 + (r32 & 15)) * 16);
    const float gate = *(LAS const float*)(L + L_G + (kh * 32 + r32) * 4);
    kr[3] = ld16(rs, vK, KVOFF(3)); v0r[3] = ld16(rs, vV0, KVOFF(3)); v1r[3] = bf16x8{};
    lds_arrive(L + L_MCNT, lane);
    TS(23);
    float mref; bool bad;
    {
        const float ma = max16(MFMA(kr[0], qa, f32x16{})), mb = max16(MFMA(kr[0], qb, f32x16{}));
        mref = fmx(ma, mb);
        bad = !(__builtin_fabsf(ma - mb) <= 40.f);
        const short cf = (r32 & 11) == 0 ? (short)0x3F80 : (short)0;
#pragma unroll
        for (int e = 0; e < 8; ++e) { v0r[0][e] = r32 < 16 ? v0r[0][e] : cf; v1r[0][e] = r32 < 16 ? v1r[0][e] : cf; }
    }
    TS(24);
    STAMP(ws, 0, 4);
    const int ot = w & 1, kq = w >> 1;
    f32x16 oa = {}, ob = {};
    float ma_ = mref, mb_ = mref;
    {
        f32x16 negm;
#pragma unroll
        for (int i = 0; i < 16; ++i) negm[i] = -mref;
        f32x16 sa = MFMA(kr[0], qa, negm), sb;
        u32x4 p1, p2 = {0u, 0u, 0u, 0u};
#define E(S, i) S[i] = EXP2(S[i])
#define C(P, S, i) P[(i) & 3] = pk2(S[2 * (i)], S[2 * (i) + 1])
#define PHASE(S, M1, M2, M3, LD1, LD2, LD3)                                                       \
    do {                                                                                          \
        M1; LD1; SBAR();                                                                          \
        E(S, 0); E(S, 1); E(S, 2); E(S, 3); E(S, 4); SBAR();                                      \
        M2; LD2; SBAR();                                                                          \
        E(S, 5); E(S, 6); E(S, 7); SBAR(); C(p1, S, 0); C(p1, S, 1); C(p1, S, 2); C(p1, S, 3); SBAR(); E(S, 8); E(S, 9); SBAR(); \
        M3; LD3; SBAR();                                                                          \
        E(S, 10); E(S, 11); E(S, 12); E(S, 13); E(S, 14); E(S, 15); SBAR();                       \
        C(p2, S, 4); C(p2, S, 5); C(p2, S, 6); C(p2, S, 7); SBAR();                               \
    } while (0)
#define P1 __builtin_bit_cast(bf16x8, p1)
#define P2 __builtin_bit_cast(bf16x8, p2)
        const unsigned obo = WS_O + (2u << 20) + ((2 * otl + ot) * 32 + 8 * kq) * 1024;
        auto body = [&](int t0, bool LAST) {
#pragma unroll
            for (int u = 0; u < 4; ++u) {
                const int t = t0 + u;
                PHASE(sa, sb = MFMA(kr[u], qb, negm), ob = MFMA(v1r[(u + 3) & 3], P2, ob), oa = MFMA(v0r[u], P1, oa),
                      kr[u] = ld16(rs, vK, LAST ? obo + u * 1024 : KVOFF(t + 4)), if (!LAST || u == 0) v1r[(u + 3) & 3] = ld16(rs, vV1, KVOFF(t + 3)), (void)0);
                PHASE(sb, if (!LAST || u < 3) sa = MFMA(kr[(u + 1) & 3], qa, negm), oa = MFMA(v1r[u], P2, oa), ob = MFMA(v0r[u], P1, ob),
                      (void)0, (void)0, v0r[u] = LAST ? ld16(rs, vK, obo + (4 + u) * 1024) : ld16(rs, vV0, KVOFF(t + 4)));
            }
        };
        if (w < 4) __builtin_amdgcn_s_setprio(2); else __builtin_amdgcn_s_setprio(1);
        STAMP(ws, 3, 0);
        body(0, false);
        for (int t0 = 4; t0 < 28; t0 += 4) {
            if (w < 4 && t0 == PRIO_SWITCH) __builtin_amdgcn_s_setprio(0);
            STAMP(ws, 3, t0 >> 2);
            body(t0, false);
        }
        STAMP(ws, 3, 7);
        lds_wait(L + L_OBS, 4);
        body(28, true);
        STAMP(ws, 3, 8);
        ob = MFMA(v1r[3], P2, ob);
#undef PHASE
#undef E
#undef C
    }
    STAMP(ws, 0, 5);
    {
        float t = fmx(oa[8], ob[8]) * 1.0e19f;
#pragma unroll
        for (int i = 0; i < 8; ++i) t = fmx(fmx(t, __builtin_fabsf(oa[i])), __builtin_fabsf(ob[i]));
        bad |= !(t < 1.0e38f);
    }
    lds_wait(L + L_MCNT, 8);
    LAS float* EXW = (LAS float*)(L + L_EX) + w * 640 + lane;
    LAS const float* EXR = (LAS const float*)(L + L_EX) + (w ^ 1) * 640 + lane;
    auto put_partial = [&]() {
#pragma unroll
        for (int i = 0; i < 9; ++i) EXW[i * 64] = kh ? oa[i] : ob[i];
        EXW[9 * 64] = kh ? ma_ : mb_;
    };
    put_partial();
    if (__any(bad) && lane == 0) *(LAS volatile unsigned*)(L + L_BAD) = 1;
    BAR();
    if (__builtin_amdgcn_readfirstlane(*(LAS volatile unsigned*)(L + L_BAD))) {
        oa = f32x16{}; ob = f32x16{}; ma_ = -INFINITY; mb_ = -INFINITY;
        for (int t = 0; t < 32; ++t) {
            const bf16x8 kf = ld16(rs, vK, KVOFF(t)), v0 = ld16(rs, vV0, KVOFF(t)), v1 = ld16(rs, vV1, KVOFF(t));
#pragma unroll
            for (int z = 0; z < 2; ++z) {
                f32x16& o = z ? ob : oa; float& m = z ? mb_ : ma_;
                f32x16 s = MFMA(kf, z ? qb : qa, f32x16{});
                const float mn = fmx(max16(s), m), al = EXP2(m - mn);
                m = mn;
#pragma unroll
                for (int i = 0; i < 16; ++i) { o[i] *= al; s[i] = EXP2(s[i] - mn); }
                o = MFMA(v0, pack8(s, 0), o);
                o = MFMA(v1, pack8(s, 1), o);
            }
        }
        put_partial();
        BAR();
    }
    STAMP(ws, 0, 12);
    {
        f32x16 o = kh ? ob : oa;
        const float m0 = kh ? mb_ : ma_, m1 = EXR[9 * 64], mm = fmx(m0, m1), f0 = EXP2(m0 - mm), f1 = EXP2(m1 - mm);
#pragma unroll
        for (int i = 0; i < 9; ++i) o[i] = o[i] * f0 + EXR[i * 64] * f1;
        const float rl = gate * __builtin_amdgcn_rcpf(o[8]);
#pragma unroll
        for (int i = 0; i < 8; ++i) o[i] *= rl;
        st16(rs, __builtin_bit_cast(bf16x8, __builtin_elementwise_min(__builtin_bit_cast(u32x4, pack8(o, 0)), (u32x4){0xFFFFFFFEu, 0xFFFFFFFEu, 0xFFFFFFFEu, 0xFFFFFFFEu})),
             lane * 16, WS_O + ((2 * tl + kh) * 32 + head) * 1024);
        SBAR();
    }
    f32x16 acc[2] = {};
    const int wpo = kq * 16384 + (4 * h + q4) * 128 + trl;
#pragma unroll
    for (int k = 0; k < 8; ++k)
#pragma unroll
        for (int nt = 0; nt < 2; ++nt) acc[nt] = MFMA(trfrag(L + L_WOB + wpo + k * 2048 + ((nt ^ (q4 >> 1)) * 64), 1024), k < 4 ? kr[k & 3] : v0r[k & 3], acc[nt]);
    STAMP(ws, 0, 6);
    {
        const unsigned oao = WS_O + ((2 * otl + ot) * 32 + 8 * kq) * 1024;
        bf16x8 of[8], wa[8][2];
#pragma unroll
        for (int k = 0; k < 8; ++k) of[k] = ld16(rs, lane * 16 + (k & 3) * 1024, oao + (k & 4) * 1024);
        SBAR();
#pragma unroll
        for (int k = 0; k < 8; ++k)
#pragma unroll
            for (int nt = 0; nt < 2; ++nt) wa[k][nt] = trfrag(L + L_WOA + wpo + k * 2048 + ((nt ^ (q4 >> 1)) * 64), 1024);
        BAR();
#pragma unroll
        for (int k = 0; k < 8; ++k) {
            for (unsigned spins = 0; spins < SPIN_CAP; ++spins) {
                const u32x4 v = __builtin_bit_cast(u32x4, of[k]);
                if (!__any(__builtin_elementwise_max(__builtin_elementwise_max(v[0], v[1]), __builtin_elementwise_max(v[2], v[3])) == 0xFFFFFFFFu)) break;
                asm volatile("" ::: "memory");
#pragma unroll
                for (int z = k; z < 8; ++z) of[z] = ld16(rs, lane * 16 + (z & 3) * 1024, oao + (z & 4) * 1024);
            }
            if (k == 7) STAMP(ws, 0, 7);
            acc[0] = MFMA(wa[k][0], of[k], acc[0]);
            acc[1] = MFMA(wa[k][1], of[k], acc[1]);
        }
    }
    LAS char* PR = L + L_WOB + kq * 16384 + ot * 8192;
#pragma unroll
    for (int z = 0; z < 4; ++z)
#pragma unroll
        for (int g = 0; g < 2; ++g) {
            const int b = 8 * (z & 1) + 4 * g;
            *(LAS f32x4*)(PR + (z * 2 + g) * 1024 + lane * 16) = (f32x4){acc[z >> 1][b], acc[z >> 1][b + 1], acc[z >> 1][b + 2], acc[z >> 1][b + 3]};
        }
    STAMP(ws, 0, 8);
    BAR();
    {
        f32x4 r0 = {}, r1 = {};
#pragma unroll
        for (int z = 0; z < 4; ++z) {
            LAS const char* src = L + L_WOB + z * 16384 + ot * 8192 + kq * 2048 + lane * 16;
            r0 += *(LAS const f32x4*)src; r1 += *(LAS const f32x4*)(src + 1024);
        }
        unsigned a0 = pk2(r0[0], r0[1]), a1 = pk2(r0[2], r0[3]), b0 = pk2(r1[0], r1[1]), b1 = pk2(r1[2], r1[3]);
        auto s0_ = __builtin_amdgcn_permlane32_swap(a0, b0, false, false);
        auto s1_ = __builtin_amdgcn_permlane32_swap(a1, b1, false, false);
        *(u32x4*)(out + (size_t)(64 * otl + 32 * ot + r32) * 512 + 64 * ocg + 16 * kq + 8 * h) = (u32x4){s0_[0], s1_[0], s0_[1], s1_[1]};
    }
    if (blockIdx.x == 0 && tid == 0) set_flag(ws, WS_EP, tag);
    STAMP(ws, 0, 9);
    TS_DUMP(ws);
}

extern "C" void kernel_launch(void* const* d_in, const int* in_sizes, int n_in, void* d_out, int out_size, void* d_ws, size_t ws_size, hipStream_t stream) {
    fused_kernel<<<256, 768, 0, stream>>>((const uint16_t*)d_in[0], (const uint16_t*)d_in[1], (const uint16_t*)d_in[4], (const uint16_t*)d_in[5], (const uint16_t*)d_in[7],
                                          (const uint16_t*)d_in[2], (const uint16_t*)d_in[6], (uint16_t*)d_out, (char*)d_ws);
}
```

```cpp
#include <hip/hip_runtime.h>
#include <hip/hip_bf16.h>
#include <stdint.h>
#define HTS_DUMP(ws)
#define STAMP(ws, k, i)
#define TS_DECL
#define TS(i)
#define TS_DUMP(ws)

typedef short bf16x8 __attribute__((ext_vector_type(8)));
typedef short s16x4 __attribute__((ext_vector_type(4)));
typedef float f32x16 __attribute__((ext_vector_type(16)));
typedef float f32x4 __attribute__((ext_vector_type(4)));
typedef float f32x2 __attribute__((ext_vector_type(2)));
typedef unsigned u32x4 __attribute__((ext_vector_type(4)));
typedef unsigned u32x2 __attribute__((ext_vector_type(2)));
typedef __bf16 bf16x2_t __attribute__((ext_vector_type(2)));
typedef unsigned long long u64;
#define LAS __attribute__((address_space(3)))
#define GAS __attribute__((address_space(1)))
#define DI __device__ __forceinline__
#define MFMA(a, b, c) __builtin_amdgcn_mfma_f32_32x32x16_bf16((a), (b), (c), 0, 0, 0)
#define SBAR() __builtin_amdgcn_sched_barrier(0)
#define RLX_AGENT __ATOMIC_RELAXED, __HIP_MEMORY_SCOPE_AGENT
#define EXP2(x) __builtin_amdgcn_exp2f(x)

constexpr float LOG2E = 1.4426950408889634f;
constexpr unsigned BLK = 16512;
constexpr unsigned WS_KV = 0;
constexpr unsigned WS_O = WS_KV + 256 * BLK;
constexpr unsigned WS_PK = WS_O + (4u << 20);
constexpr unsigned WS_KVF = WS_PK + 32 * 32 * 2176;
constexpr unsigned WS_F1 = WS_KVF + 32 * 2176;
constexpr unsigned WS_F2 = WS_F1 + 2048;
constexpr unsigned WS_F3 = WS_F2 + 8192;
constexpr unsigned WS_F5 = WS_F3 + 8192;
constexpr unsigned WS_EP = WS_F5 + 8192;
constexpr unsigned WS_END = WS_EP + 128;
constexpr int L_X = 0, L_RING = 65536;
constexpr int L_PHK = 65536, L_PHQ = 81920, L_KA = 0, L_VA = 8192, L_VB = 16384;
constexpr int L_WOA = 0, L_WOB = 65536;
constexpr int L_WPHI = 131072, L_G = L_WPHI + 2048, L_SYNC = L_G + 256, L_SEEN = L_SYNC, L_MCNT = L_SYNC + 4, L_HCNT = L_SYNC + 8, L_OBS = L_SYNC + 12, L_PAIR = L_SYNC + 16, L_DUMMY = L_SYNC + 256;
constexpr int L_M = L_WPHI + 3072, L_QA = L_M + 1024, L_EX = L_QA + 8192;
constexpr int LDS_BYTES = 163840;
#define KV_SPLIT 0
#define RETRY_SLEEP 4
#define WO_T0 4500
#define WO_DT 1700
#define PK_DELAY 24
#define KV_AUX 16
#define F1_STEP 12
#define PRIO_SWITCH 24
constexpr unsigned SPIN_CAP = 1u << 20;

DI unsigned pk2(float a, float b) { f32x2 v = {a, b}; return __builtin_bit_cast(unsigned, __builtin_convertvector(v, bf16x2_t)); }
DI bf16x8 pack8(const f32x16& x, int s) {
    u32x4 p = {pk2(x[8 * s], x[8 * s + 1]), pk2(x[8 * s + 2], x[8 * s + 3]), pk2(x[8 * s + 4], x[8 * s + 5]), pk2(x[8 * s + 6], x[8 * s + 7])};
    return __builtin_bit_cast(bf16x8, p);
}
DI float fmx(float a, float b) { return __builtin_elementwise_maximum(a, b); }
DI float max32(float v) { auto r = __builtin_amdgcn_permlane32_swap(__float_as_uint(v), __float_as_uint(v), false, false); return fmx(__uint_as_float(r[0]), __uint_as_float(r[1])); }
DI float lmax16(const f32x16& s) {
    const float a = fmx(fmx(s[0], s[1]), s[2]), b = fmx(fmx(s[3], s[4]), s[5]), c = fmx(fmx(s[6], s[7]), s[8]), d = fmx(fmx(s[9], s[10]), s[11]), e = fmx(fmx(s[12], s[13]), s[14]);
    return fmx(fmx(fmx(a, b), c), fmx(fmx(d, e), s[15]));
}
DI float max16(const f32x16& s) { return max32(lmax16(s)); }
DI void glds2(unsigned v0, unsigned v1, const void* sbase, unsigned ldsdst) {
    unsigned keep;
    asm volatile("s_mov_b32 %0, m0\n\ts_mov_b32 m0, %4\n\ts_nop 0\n\tglobal_load_lds_dwordx4 %1, %3\n\tglobal_load_lds_dwordx4 %2, %3 offset:1024\n\ts_mov_b32 m0, %0"
                 : "=&s"(keep) : "v"(v0), "v"(v1), "s"(sbase), "s"(ldsdst) : "memory");
}
DI void glds4(const unsigned (&v)[4], const void* sbase, unsigned ldsdst) {
    unsigned keep;
    asm volatile("s_mov_b32 %0, m0\n\ts_mov_b32 m0, %6\n\ts_nop 0\n\tglobal_load_lds_dwordx4 %1, %5\n\tglobal_load_lds_dwordx4 %2, %5 offset:1024\n\t"
                 "global_load_lds_dwordx4 %3, %5 offset:2048\n\tglobal_load_lds_dwordx4 %4, %5 offset:3072\n\ts_mov_b32 m0, %0"
                 : "=&s"(keep) : "v"(v[0]), "v"(v[1]), "v"(v[2]), "v"(v[3]), "s"(sbase), "s"(ldsdst) : "memory");
}
DI void touch(unsigned voff, const void* sbase, unsigned ldsdst) {
    unsigned keep;
    asm volatile("s_mov_b32 %0, m0\n\ts_mov_b32 m0, %3\n\ts_nop 0\n\tglobal_load_lds_dword %1, %2 sc1\n\ts_mov_b32 m0, %0"
                 : "=&s"(keep) : "v"(voff), "s"(sbase), "s"(ldsdst) : "memory");
}
typedef short v4i16_t __attribute__((ext_vector_type(4)));
DI s16x4 trread(LAS const char* p) { return __builtin_bit_cast(s16x4, __builtin_amdgcn_ds_read_tr16_b64_v4i16((LAS v4i16_t*)p)); }
DI bf16x8 trfrag(LAS const char* p, int d2) { s16x4 lo = trread(p), hi = trread(p + d2); return __builtin_shufflevector(lo, hi, 0, 1, 2, 3, 4, 5, 6, 7); }
DI bf16x8 ldsfrag(LAS const char* p) { return *(LAS const bf16x8*)p; }
#define BAR() asm volatile("s_waitcnt lgkmcnt(0)\n\ts_barrier" ::: "memory")
#define VM_BAR(N) asm volatile("s_waitcnt vmcnt(" #N ") lgkmcnt(0)\n\ts_barrier" ::: "memory")
#define DRAIN() asm volatile("s_waitcnt vmcnt(0)" ::: "memory")
typedef __amdgpu_buffer_rsrc_t rsrc_t;
DI bf16x8 ld16(rsrc_t r, unsigned voff, unsigned soff) { return __builtin_bit_cast(bf16x8, __builtin_amdgcn_raw_buffer_load_b128(r, voff, soff, 16)); }
DI void st16(rsrc_t r, bf16x8 v, unsigned voff, unsigned soff) { __builtin_amdgcn_raw_buffer_store_b128(__builtin_bit_cast(u32x4, v), r, voff, soff, 16); }
DI void st16kv(rsrc_t r, bf16x8 v, unsigned voff, unsigned soff) { __builtin_amdgcn_raw_buffer_store_b128(__builtin_bit_cast(u32x4, v), r, voff, soff, KV_AUX); }
DI unsigned lds_word(LAS char* p) { return __builtin_amdgcn_readfirstlane(*(LAS volatile unsigned*)p); }
DI void lds_wait(LAS char* p, unsigned want) {
#pragma unroll 1
    for (unsigned spins = 0; spins < (SPIN_CAP << 6) && lds_word(p) < want; ++spins) __builtin_amdgcn_s_sleep(1);
    asm volatile("" ::: "memory");
}
DI void lds_arrive(LAS char* p, int lane) {
    asm volatile("s_waitcnt lgkmcnt(0)" ::: "memory");
    if (lane == 0) __hip_atomic_fetch_add((LAS unsigned*)p, 1u, __ATOMIC_RELAXED, __HIP_MEMORY_SCOPE_WORKGROUP);
}
DI void set_flag(char* ws, unsigned off, u64 tag) { __hip_atomic_store((GAS u64*)(ws + off), tag, RLX_AGENT); }
template <int FIRST, int GAP> DI void wait_flags(char* ws, unsigned off, int n, u64 tag, int lane) {
    GAS u64* p = (GAS u64*)(ws + off) + (lane < n ? lane : 0);
    if (FIRST) __builtin_amdgcn_s_sleep(FIRST);
#pragma unroll 1
    for (unsigned spins = 0; spins < SPIN_CAP; ++spins) {
        const u64 v = __hip_atomic_load(p, RLX_AGENT);
        if (__all(v == tag)) break;
        __builtin_amdgcn_s_sleep(GAP);
    }
    __builtin_amdgcn_fence(__ATOMIC_ACQUIRE, "wavefront");
    asm volatile("" ::: "memory");
}

DI void softmax16(f32x16& p) {
    const float nm = -lmax16(p) * LOG2E;
    f32x2 e[8];
#pragma unroll
    for (int i = 0; i < 8; ++i) { e[i] = __builtin_elementwise_fma((f32x2){p[2 * i], p[2 * i + 1]}, (f32x2){LOG2E, LOG2E}, (f32x2){nm, nm}); e[i][0] = EXP2(e[i][0]); e[i][1] = EXP2(e[i][1]); }
    const f32x2 t = ((e[0] + e[1]) + (e[2] + e[3])) + ((e[4] + e[5]) + (e[6] + e[7]));
    const float rs = __builtin_amdgcn_rcpf(t[0] + t[1]);
#pragma unroll
    for (int i = 0; i < 8; ++i) { e[i] *= rs; p[2 * i] = e[i][0]; p[2 * i + 1] = e[i][1]; }
}

__global__ __launch_bounds__(768) void fused_kernel(const uint16_t* __restrict__ x, const uint16_t* __restrict__ WA, const uint16_t* __restrict__ WB,
                                                    const uint16_t* __restrict__ wphi, const uint16_t* __restrict__ gatew,
                                                    const uint16_t* __restrict__ woA, const uint16_t* __restrict__ woB,
                                                    uint16_t* __restrict__ out, char* ws) {
    __shared__ __attribute__((aligned(16))) char lds[LDS_BYTES];
    LAS char* L = (LAS char*)lds;
    u64 pc0; asm volatile("s_getpc_b64 %0" : "=s"(pc0));
    const unsigned lds0 = (unsigned)(uintptr_t)lds;
    const int tid = threadIdx.x, lane = tid & 63, w = __builtin_amdgcn_readfirstlane(tid >> 6);
    const int r32 = lane & 31, h = lane >> 5;
    const int xcd = blockIdx.x & 7, idx = blockIdx.x >> 3;
    const int qd = 2 * (xcd & 3) + (idx & 1), tl = 2 * (idx >> 1) + (xcd >> 2);
    const int s0 = ((tl >> 1) + 4 * (qd & 1)) & 7;
    const int otl = 4 * xcd + (idx >> 3), ocg = idx & 7;
    const rsrc_t rs = __builtin_amdgcn_make_buffer_rsrc(ws, 0, WS_END, 0x00020000);
    u64 tag;
#define EPOCH_BAR(N) do { asm volatile("s_load_dwordx2 %0, %1, 0x0\n\ts_waitcnt vmcnt(" #N ") lgkmcnt(0)\n\ts_barrier" : "=s"(tag) : "s"(ws + WS_EP) : "memory"); tag += 1; } while (0)
    const int q4 = (lane & 15) >> 2;
    const int trl = ((lane >> 4) & 1) * 32 + (lane & 3) * 8;
    STAMP(ws, 0, 0);
    TS_DECL; TS(0); TS(16);

    asm volatile("" :: "s"(x), "s"(WA), "s"(WB), "s"(wphi), "s"(gatew), "s"(ws));
    if (__builtin_expect(w >= 8, 1)) {
        const int j = w - 8;
        const unsigned vsw = (unsigned)(((lane & 7) ^ (((lane >> 4) & 1) << 2)) << 4);
        unsigned vw[4];
#pragma unroll
        for (int u = 0; u < 4; ++u) vw[u] = (unsigned)((lane >> 3) * 3072) + vsw + u * (8 * 3072 - 1024);
        const unsigned vx0 = (unsigned)((lane >> 3) * 1024 + (((lane & 7) ^ (lane >> 4)) << 4)), vx1 = (vx0 ^ 64u) + 8192 - 1024;
        const char* xb = (const char*)x + (size_t)(64 * tl + 16 * j) * 1024;
        const char* w1b = (const char*)WA + (size_t)((j & 1) * 32) * 3072 + qd * 128 + 1024 + (j >> 1) * 1024;
        const char* w2b = (j == 0 ? (const char*)WA : (const char*)WB + (j - 1) * 1024) + qd * 128;
        auto issue_x = [&](int i) { const int kc = (s0 + i) & 7; glds2(vx0, vx1, xb + kc * 128, lds0 + L_X + i * 8192 + j * 2048); };
        auto issue_w1 = [&](int i) {
            glds4(vw, w1b + ((s0 + i) & 7) * (64 * 3072), lds0 + L_RING + ((i + 4) % 6) * 16384 + j * 4096);
        };
        auto issue_w2 = [&](int i) {
            const char* wb = w2b + ((s0 + i) & 7) * (64 * 3072);
            glds4(vw, wb, lds0 + L_RING + (i % 3) * 32768 + j * 8192);
            glds4(vw, wb + 32 * 3072, lds0 + L_RING + (i % 3) * 32768 + j * 8192 + 4096);
        };
        __builtin_amdgcn_s_setprio(3);
        TS(4);
        issue_w1(0); issue_x(0); issue_x(1); issue_x(2); issue_x(3);
        bf16x8 gw, wph;
        asm volatile("global_load_dwordx4 %0, %1, %2" : "=v"(gw) : "v"((lane & 15) * 64 + (lane >> 4) * 16), "s"(gatew) : "memory");
        asm volatile("global_load_dwordx4 %0, %1, %2" : "=v"(wph) : "v"(lane * 16), "s"(wphi + qd * 1024 + (j & 1) * 512) : "memory");
        issue_w1(1);
        TS(5);
        VM_BAR(12); issue_w1(2); issue_w1(3);
        VM_BAR(8); issue_x(4); issue_w1(4);
        EPOCH_BAR(10); issue_x(5); issue_w1(5);
        VM_BAR(12); issue_x(6); issue_w1(6);
        VM_BAR(12); issue_x(7); issue_w1(7);
        VM_BAR(12); issue_w2(0);
        VM_BAR(14); issue_w2(1);
        VM_BAR(16);
        f32x4 gl = {};
        const int grow = 16 * j + (lane & 15);
        int gxa = L_X + grow * 128 + (((lane >> 4) ^ ((grow >> 1) & 7)) << 4);
#pragma unroll
        for (int i = 8; i < 16; ++i) {
            if (i < 15) VM_BAR(8); else VM_BAR(0);
            if (i == 8) asm volatile("" : "+v"(gw), "+v"(wph));
            const int kc = (s0 + i) & 7;
            const bf16x8 xg0 = ldsfrag(L + gxa + (i - 8) * 8192), xg1 = ldsfrag(L + (gxa ^ 64) + (i - 8) * 8192);
            const bf16x8 ga0 = (lane & 15) == 2 * kc ? gw : bf16x8{}, ga1 = (lane & 15) == 2 * kc + 1 ? gw : bf16x8{};
            SBAR();
            if (i < 14) issue_w2(i - 6);
            if (i == 14) {
                if (j < 2) *(LAS bf16x8*)(L + L_WPHI + j * 1024 + lane * 16) = wph;
                if (j == 2 && lane < 16) *(LAS unsigned*)(L + L_SYNC + lane * 4) = 0;
            }
            if (i == 15 && j == 0) {
                TS(1);
                wait_flags<0, 2>(ws, WS_F1 + qd * 256, 32, tag, lane);
                *(LAS volatile unsigned*)(L + L_SEEN) = 1;
                TS(2);
            }
            SBAR();
            gl = __builtin_amdgcn_mfma_f32_16x16x32_bf16(ga0, xg0, gl, 0, 0, 0);
            gl = __builtin_amdgcn_mfma_f32_16x16x32_bf16(ga1, xg1, gl, 0, 0, 0);
            SBAR();
        }
        {
            float z = (gl[0] + gl[1]) + (gl[2] + gl[3]);
            z += __shfl_xor(z, 16); z += __shfl_xor(z, 32);
            if (lane < 16) *(LAS float*)(L + L_G + grow * 4) = __builtin_amdgcn_rcpf(1.f + EXP2(-z * LOG2E));
        }
        BAR();
        TS(3);
        const u64 tp2 = __builtin_amdgcn_s_memtime();
        __builtin_amdgcn_s_sleep(6);
        const int head = 4 * qd + j;
        bf16x8 phq[2][2]; float g2[2];
        {
            f32x16 kv = {};
            const bool keep = (r32 >> 4) == (j & 1);
            bf16x8 pf[2][2], ve[2][2];
#pragma unroll
            for (int tt = 0; tt < 2; ++tt) {
#pragma unroll
                for (int s = 0; s < 2; ++s) {
                    { LAS const char* pp = L + L_PHK + (j * 2 + tt) * 2048 + (16 * s + 4 * h + q4) * 64; const int sw = trl ^ ((2 * h + (q4 >> 1)) * 8);
                      pf[tt][s] = __builtin_shufflevector(trread(pp + sw), trread(pp + 512 + (sw ^ 32)), 0, 1, 2, 3, 4, 5, 6, 7); }
                    ve[tt][s] = ldsfrag(L + L_VB + (((j >> 1) * 2 + tt) * 2 + s) * 1024 + lane * 16);
                    phq[tt][s] = ldsfrag(L + L_PHQ + ((j * 2 + tt) * 2 + s) * 1024 + lane * 16);
                }
                g2[tt] = 1.f - *(LAS const float*)(L + L_G + (tt * 32 + r32) * 4);
            }
            lds_arrive(L + L_HCNT, lane);
            __builtin_amdgcn_s_sleep(PK_DELAY);
#pragma unroll
            for (int tt = 0; tt < 2; ++tt)
#pragma unroll
                for (int s = 0; s < 2; ++s) {
#pragma unroll
                    for (int e = 0; e < 8; ++e) ve[tt][s][e] = keep ? ve[tt][s][e] : (short)0x3F80;
                    kv = MFMA(pf[tt][s], ve[tt][s], kv);
                }
            const int col = keep ? (r32 & 15) : 16;
            const unsigned vo = (keep || (r32 & 15) == 0) ? (unsigned)((col * 2 + h) * 16) : 0x40000000u;
#pragma unroll
            for (int g = 0; g < 4; ++g) st16(rs, __builtin_bit_cast(bf16x8, (f32x4){kv[4 * g], kv[4 * g + 1], kv[4 * g + 2], kv[4 * g + 3]}), vo + g * 544, WS_PK + ((qd * 32 + tl) * 4 + j) * 2176);
            DRAIN();
            if (lane == 0) set_flag(ws, WS_F2 + (head * 32 + tl) * 8, tag);
        }
        unsigned vo[4];
#pragma unroll
        for (int u = 0; u < 4; ++u) vo[u] = (unsigned)((lane >> 3) * 1024) + vsw + u * (8192 - 1024);
        lds_wait(L + L_HCNT, 4);
        int wo_done = 0;
        auto wo_service = [&]() {
            if (wo_done < 2 && __builtin_amdgcn_s_memtime() - tp2 >= (u64)(WO_T0 + WO_DT * (j + 4 * wo_done))) {
                const char* src = (const char*)(wo_done ? woA : woB) + (size_t)(128 * j) * 1024 + ocg * 128;
#pragma unroll
                for (int i = 0; i < 4; ++i) glds4(vo, src + i * 32768, lds0 + (wo_done ? L_WOA : L_WOB) + j * 16384 + i * 4096);
                ++wo_done;
            }
        };
        auto wait_slow = [&](unsigned off, int n) {
            GAS u64* p = (GAS u64*)(ws + off) + (lane < n ? lane : 0);
#pragma unroll 1
            for (unsigned spins = 0; spins < SPIN_CAP; ++spins) {
#pragma unroll 1
                for (int z = 0; z < (spins ? 2 : 6); ++z) { wo_service(); __builtin_amdgcn_s_sleep(6); }
                if (__all(__hip_atomic_load(p, RLX_AGENT) == tag)) break;
            }
            __builtin_amdgcn_fence(__ATOMIC_ACQUIRE, "wavefront");
            asm volatile("" ::: "memory");
        };
        STAMP(ws, 0, 1);
        if (tl < 17) {
            wait_slow(WS_F2 + head * 256, 32);
            const unsigned so = WS_PK + (qd * 128 + j) * 2176 + tl * 128, v1 = (unsigned)((lane >> 3) * 8704 + (lane & 7) * 16);
            f32x4 a = (__builtin_bit_cast(f32x4, ld16(rs, v1, so)) + __builtin_bit_cast(f32x4, ld16(rs, v1, so + 8 * 8704))) +
                      (__builtin_bit_cast(f32x4, ld16(rs, v1, so + 16 * 8704)) + __builtin_bit_cast(f32x4, ld16(rs, v1, so + 24 * 8704)));
#pragma unroll
            for (int o = 8; o < 64; o <<= 1) {
#pragma unroll
                for (int e = 0; e < 4; ++e) a[e] += __shfl_xor(a[e], o);
            }
            st16(rs, __builtin_bit_cast(bf16x8, a), lane < 8 ? (unsigned)(lane * 16) : 0x40000000u, WS_KVF + head * 2176 + tl * 128);
            DRAIN();
            if (lane == 0) set_flag(ws, WS_F3 + (head * 32 + tl) * 8, tag);
        } else {
#pragma unroll 1
            for (int z = 0; z < 10; ++z) { wo_service(); __builtin_amdgcn_s_sleep(6); }
        }
        STAMP(ws, 0, 2);
        wait_slow(WS_F3 + head * 256, 17);
        {
            const unsigned v2 = (unsigned)(((r32 < 16 ? r32 : 16) * 2 + h) * 16);
            f32x16 kv;
#pragma unroll
            for (int g = 0; g < 4; ++g) { const f32x4 t = __builtin_bit_cast(f32x4, ld16(rs, v2 + g * 544, WS_KVF + head * 2176)); kv[4 * g] = t[0]; kv[4 * g + 1] = t[1]; kv[4 * g + 2] = t[2]; kv[4 * g + 3] = t[3]; }
            const bf16x8 k0 = pack8(kv, 0), k1 = pack8(kv, 1);
#pragma unroll
            for (int tt = 0; tt < 2; ++tt) {
                f32x16 ob = MFMA(k0, phq[tt][0], f32x16{});
                ob = MFMA(k1, phq[tt][1], ob);
                const float rd = g2[tt] * __builtin_amdgcn_rcpf(ob[8] + 1e-6f);
#pragma unroll
                for (int i = 0; i < 8; ++i) ob[i] *= rd;
                st16(rs, pack8(ob, 0), lane * 16, WS_O + (2u << 20) + ((2 * tl + tt) * 32 + head) * 1024);
                st16(rs, (bf16x8){-1, -1, -1, -1, -1, -1, -1, -1}, lane * 16, WS_O + ((2 * tl + tt) * 32 + head) * 1024);
            }
            DRAIN();
            if (lane == 0) set_flag(ws, WS_F5 + (tl * 32 + head) * 8, tag);
        }
        STAMP(ws, 0, 3);
        wait_slow(WS_F5 + (otl * 32 + 8 * j) * 8, 8);
        if (lane < 16) touch((lane >> 3) * 32768 + (lane & 7) * 128, ws + WS_O + (2u << 20) + (2 * otl * 32 + 8 * j + ocg) * 1024, lds0 + L_DUMMY);
        while (wo_done < 2) { wo_service(); __builtin_amdgcn_s_sleep(6); }
        DRAIN();
        lds_arrive(L + L_OBS, lane);
        STAMP(ws, 0, 5);
        HTS_DUMP(ws);
        return;
    }

    if (w == 0) {
        const char* a = (const char*)(pc0 & ~127ull) + (idx * 8 + lane) * 128;
        if (lane == 8) {
            a = ws + (size_t)(idx < 21 ? idx : 0) * 524288;
            if (idx >= 22) a = idx < 25 ? (const char*)WB + (idx - 22) * 786368 : idx < 28 ? (const char*)out + (idx - 25) * 1048512 : idx < 30 ? (const char*)woA + (idx - 28) * 524224 : (const char*)woB + (idx - 30) * 524224;
        }
        unsigned keep;
        if (lane < 9) asm volatile("s_mov_b32 %0, m0\n\ts_mov_b32 m0, %2\n\ts_nop 0\n\tglobal_load_lds_dword %1, off\n\ts_mov_b32 m0, %0" : "=&s"(keep) : "v"(a), "s"(lds0 + L_RING + 3 * 16384) : "memory");
    }
    const int role = w >> 2, p = (w >> 1) & 1, tt = w & 1;
    const int hq = w >> 1, kh = w & 1, head = 4 * qd + hq;
    const unsigned vK = w * 2048 + lane * 16;
    const unsigned vV0 = r32 < 16 ? (unsigned)(w * 2048 + 1024 + h * 256 + r32 * 16) : ((r32 & 11) == 0 ? 16384u : 0x40000000u);
    const unsigned vV1 = r32 < 16 ? vV0 + 512 : vV0;
    const unsigned kvb = WS_KV + qd * 32 * BLK;
#define KVOFF(t) (kvb + (((t) + tl) & 31) * BLK)
    bf16x8 kr[4], v0r[4], v1r[4];
    {
        const int xrow = tt * 32 + r32;
        const int xa0 = L_X + xrow * 128 + ((h ^ ((xrow >> 1) & 7)) << 4);
        const int wa0 = L_RING + (8 * h + q4) * 128 + ((p ^ (q4 >> 1)) * 64) + trl;
        bf16x8 xf[2][4], w0[2][4], w1[2][4], own[2];
        f32x16 a0 = {}, a1 = {};
        int wsb[3], xsb[4];
#pragma unroll
        for (int i = 0; i < 3; ++i) { wsb[i] = wa0 + i * 32768; asm volatile("" : "+v"(wsb[i])); }
#pragma unroll
        for (int s = 0; s < 4; ++s) { xsb[s] = xa0 ^ (s << 5); asm volatile("" : "+v"(xsb[s])); }
        auto rx = [&](int i, int s) { xf[i & 1][s] = ldsfrag(L + xsb[s] + (i & 7) * 8192); };
        auto r0 = [&](int i, int s, int ROLE) {
            w0[i & 1][s] = i < 8 ? trfrag(L + wsb[((i + 4) % 6) >> 1] + (i & 1) * 16384 + ROLE * 8192 + s * 2048, 512) : trfrag(L + wsb[(i - 8) % 3] + ROLE * 16384 + s * 2048, 512);
        };
        auto r1 = [&](int i, int s, int ROLE) { if (i >= 8) w1[i & 1][s] = trfrag(L + wsb[(i - 8) % 3] + ROLE * 16384 + 8192 + s * 2048, 512); };
        auto m0 = [&](int i, int s, int ROLE) { if (i >= 0) a0 = (ROLE && i < 8) ? MFMA(xf[i & 1][s], w0[i & 1][s], a0) : MFMA(w0[i & 1][s], xf[i & 1][s], a0); SBAR(); };
        auto m1 = [&](int i, int s, int ROLE) { if (i >= 8) a1 = ROLE ? MFMA(xf[i & 1][s], w1[i & 1][s], a1) : MFMA(w1[i & 1][s], xf[i & 1][s], a1); SBAR(); };
        auto publish = [&](int ROLE, int part) {
            const unsigned so = kvb + tl * BLK + (4 * p + tt) * 2048;
            if (ROLE == 0) st16kv(rs, own[part], lane * 16, so + part * 4096);
            else st16kv(rs, own[part], (unsigned)((r32 >> 4) * 4096 + 1024 + h * 256 + (r32 & 15) * 16) + part * 512, so);
            const bf16x8 ones = {0x3F80, 0x3F80, 0x3F80, 0x3F80, 0x3F80, 0x3F80, 0x3F80, 0x3F80};
            if (ROLE == 0 && part == 0) st16kv(rs, ones, tid < 8 ? (unsigned)(16384 + lane * 16) : 0x40000000u, so);
        };
        auto gemm = [&](int ROLE) {
            bf16x8 wf[2];
#pragma unroll
            for (int i = 0; i < 16; ++i) {
                if (i == F1_STEP) { VM_BAR(0); if (tid == 0) set_flag(ws, WS_F1 + (qd * 32 + tl) * 8, tag); } else if (i == 0) EPOCH_BAR(63); else if (i == 2) { __builtin_amdgcn_s_waitcnt(0x0F70); BAR(); } else BAR();
                if (i == 0 || i == 1 || i == 8) TS(i + 1); else if (i == 15) TS(17);
                SBAR();
                auto slot = [&](int sg) {
                    if (KV_SPLIT) {
                        if (sg == ROLE || sg == 4 + ROLE) { if (p == 0) publish(ROLE, sg >= 4); }
                        if (sg == 2 + ROLE || sg == 6 + ROLE) { if (p == 1) publish(ROLE, sg >= 4); }
                    } else if (sg == 0) { publish(ROLE, 0); publish(ROLE, 1); }
                    SBAR();
                };
                m0(i - 1, 0, ROLE); rx(i, 0); r0(i, 0, ROLE); SBAR();
                m1(i - 1, 0, ROLE); r1(i, 0, ROLE); rx(i, 1); SBAR();
                m0(i - 1, 1, ROLE); r0(i, 1, ROLE); r1(i, 1, ROLE); SBAR();
                m1(i - 1, 1, ROLE); rx(i, 2); r0(i, 2, ROLE); SBAR();
                slot(2 * (i - 8) - 1);
                m0(i - 1, 2, ROLE); r1(i, 2, ROLE); rx(i, 3); SBAR();
                m1(i - 1, 2, ROLE); r0(i, 3, ROLE); r1(i, 3, ROLE); SBAR();
                m0(i - 1, 3, ROLE);
                if (i == 15) {
#pragma unroll
                    for (int a = 0; a < 2; ++a) wf[a] = trfrag(L + L_WPHI + (2 * p + a) * 512 + (4 * h + q4) * 32 + (((lane >> 4) & 1) * 2 + ((lane >> 1) & 1)) * 8, 256);
                    SBAR();
                }
                m1(i - 1, 3, ROLE);
                if (i == 8) { own[0] = pack8(a0, 0); own[1] = pack8(a0, 1); a0 = f32x16{}; }
                slot(2 * (i - 8));
                if (i == 9 + ROLE) {
#pragma unroll
                    for (int a = 0; a < 2; ++a) *(LAS bf16x8*)(L + (ROLE ? L_VA + ((p * 2 + tt) * 2 + a) * 1024 : L_KA + ((2 * p + a) * 2 + tt) * 1024) + lane * 16) = own[a];
                }
                SBAR();
            }
#pragma unroll
            for (int s = 0; s < 4; ++s) { if (ROLE) m0(15, s, ROLE); else m1(15, s, ROLE); }
#pragma unroll
            for (int s = 0; s < 4; ++s) { if (ROLE) m1(15, s, ROLE); else m0(15, s, ROLE); }
            TS(18);
            kr[0] = ldsfrag(L + L_KA + (hq * 2 + kh) * 1024 + lane * 16);
            v0r[0] = ldsfrag(L + L_VA + (((hq >> 1) * 2 + kh) * 2) * 1024 + (h * 32 + (hq & 1) * 16 + (r32 & 15)) * 16);
            v1r[0] = ldsfrag(L + L_VA + (((hq >> 1) * 2 + kh) * 2 + 1) * 1024 + (h * 32 + (hq & 1) * 16 + (r32 & 15)) * 16);
            const short sg = (lane & 4) ? (short)0x8000 : (short)0;
#pragma unroll
            for (int a = 0; a < 2; ++a)
#pragma unroll
                for (int e = 0; e < 8; ++e) wf[a][e] ^= sg;
            const f32x16& fin = ROLE ? a0 : a1;
            f32x16 f0 = MFMA(wf[0], pack8(fin, 0), f32x16{}), f1 = MFMA(wf[1], pack8(fin, 1), f32x16{});
            const short cf = (r32 & 11) == 0 ? (short)0x3F80 : (short)0;
#pragma unroll
            for (int e = 0; e < 8; ++e) { v0r[0][e] = r32 < 16 ? v0r[0][e] : cf; v1r[0][e] = r32 < 16 ? v1r[0][e] : cf; }
            auto first_loads = [&](int t) {
                if (t == 1) lds_wait(L + L_SEEN, 1);
                kr[t] = ld16(rs, vK, KVOFF(t)); v0r[t] = ld16(rs, vV0, KVOFF(t)); v1r[t] = ld16(rs, vV1, KVOFF(t));
                SBAR();
            };
            if (ROLE == 1) first_loads(1);
            TS(19);
            if (ROLE == 0) {
                const float qs = 0.25f * LOG2E;
#pragma unroll
                for (int i = 0; i < 16; ++i) a0[i] *= qs;
#pragma unroll
                for (int a = 0; a < 2; ++a) {
                    const bf16x8 qf = pack8(a0, a);
                    *(LAS bf16x8*)(L + L_QA + ((2 * p + a) * 2 + tt) * 1024 + lane * 16) = qf;
                    const float m = max16(MFMA(own[a], qf, f32x16{}));
                    if (h == 0) *(LAS float*)(L + L_M + (((2 * p + a) * 2 + tt) * 32 + r32) * 4) = m;
                }
            } else {
#pragma unroll
                for (int s = 0; s < 2; ++s) *(LAS bf16x8*)(L + L_VB + ((p * 2 + tt) * 2 + s) * 1024 + lane * 16) = pack8(a1, s);
            }
            if (ROLE == 0) { SBAR(); first_loads(1); }
            softmax16(f0); softmax16(f1);
            TS(22);
#pragma unroll
            for (int a = 0; a < 2; ++a) {
                const f32x16& f = a ? f1 : f0;
                if (ROLE == 0) {
#pragma unroll
                    for (int s = 0; s < 2; ++s) *(LAS bf16x8*)(L + L_PHQ + (((2 * p + a) * 2 + tt) * 2 + s) * 1024 + lane * 16) = pack8(f, s);
                } else {
#pragma unroll
                    for (int g = 0; g < 4; ++g)
                        *(LAS u32x2*)(L + L_PHK + ((2 * p + a) * 2 + tt) * 2048 + r32 * 64 + 8 * ((2 * g + h) ^ ((r32 >> 1) & 7))) = (u32x2){pk2(f[4 * g], f[4 * g + 1]), pk2(f[4 * g + 2], f[4 * g + 3])};
                }
            }
            SBAR();
            first_loads(2);
        };
        if (role == 0) gemm(0); else gemm(1);
    }
    TS(20);
    BAR();
    TS(21);
    const bf16x8 qa = ldsfrag(L + L_QA + (hq * 2 + (kh ^ 1)) * 1024 + lane * 16), qb = ldsfrag(L + L_QA + (hq * 2 + kh) * 1024 + lane * 16);
    const float m0 = *(LAS const float*)(L + L_M + (hq * 64 + r32) * 4), m1 = *(LAS const float*)(L + L_M + (hq * 64 + 32 + r32) * 4);
    TS(23);
    const int ot = w & 1, kq = w >> 1;
    f32x16 oa, ob;
    {
        f32x16 negm;
        const float nm = -fmx(m0, m1);
#pragma unroll
        for (int i = 0; i < 16; ++i) negm[i] = nm;
        f32x16 sa = MFMA(kr[0], qa, negm), sb;
        kr[3] = ld16(rs, vK, KVOFF(3)); v0r[3] = ld16(rs, vV0, KVOFF(3));
        TS(24);
        STAMP(ws, 0, 4);
        u32x4 p1, p2;
#define E(S, i) S[i] = EXP2(S[i])
#define C(P, S, i) P[(i) & 3] = pk2(S[2 * (i)], S[2 * (i) + 1])
#define PHASE(S, M1, M2, M3, LD1, LD2, LD3)                                                       \
    do {                                                                                          \
        M1; LD1; SBAR();                                                                          \
        E(S, 0); E(S, 1); E(S, 2); E(S, 3); E(S, 4); SBAR();                                      \
        M2; LD2; SBAR();                                                                          \
        E(S, 5); E(S, 6); E(S, 7); SBAR(); C(p1, S, 0); C(p1, S, 1); C(p1, S, 2); C(p1, S, 3); SBAR(); E(S, 8); E(S, 9); SBAR(); \
        M3; LD3; SBAR();                                                                          \
        E(S, 10); E(S, 11); E(S, 12); E(S, 13); E(S, 14); E(S, 15); SBAR();                       \
        C(p2, S, 4); C(p2, S, 5); C(p2, S, 6); C(p2, S, 7); SBAR();                               \
    } while (0)
#define P1 __builtin_bit_cast(bf16x8, p1)
#define P2 __builtin_bit_cast(bf16x8, p2)
        const unsigned obo = WS_O + (2u << 20) + ((2 * otl + ot) * 32 + 8 * kq) * 1024 - w * 2048;
        auto body = [&](int t0, bool FIRST, bool LAST) {
#pragma unroll
            for (int u = 0; u < 4; ++u) {
                const int t = t0 + u;
                const bool Z = FIRST && u == 0;
                PHASE(sa, sb = MFMA(kr[u], qb, negm), if (!Z) ob = MFMA(v1r[(u + 3) & 3], P2, ob), oa = MFMA(v0r[u], P1, Z ? f32x16{} : oa),
                      kr[u] = ld16(rs, vK, LAST ? obo + u * 1024 : KVOFF(t + 4)), if (!LAST || u == 0) v1r[(u + 3) & 3] = ld16(rs, vV1, KVOFF(t + 3)), (void)0);
                PHASE(sb, if (!LAST || u < 3) sa = MFMA(kr[(u + 1) & 3], qa, negm), oa = MFMA(v1r[u], P2, oa), ob = MFMA(v0r[u], P1, Z ? f32x16{} : ob),
                      (void)0, (void)0, v0r[u] = LAST ? ld16(rs, vK, obo + (4 + u) * 1024) : ld16(rs, vV0, KVOFF(t + 4)));
            }
        };
        if (w < 4) __builtin_amdgcn_s_setprio(2); else __builtin_amdgcn_s_setprio(1);
        STAMP(ws, 3, 0);
        body(0, true, false);
        for (int t0 = 4; t0 < 28; t0 += 4) {
            if (w < 4 && t0 == PRIO_SWITCH) __builtin_amdgcn_s_setprio(0);
            STAMP(ws, 3, t0 >> 2);
            body(t0, false, false);
        }
        STAMP(ws, 3, 7);
        lds_wait(L + L_OBS, 4);
        body(28, false, true);
        STAMP(ws, 3, 8);
        ob = MFMA(v1r[3], P2, ob);
#undef PHASE
#undef E
#undef C
    }
    __builtin_amdgcn_s_setprio(0);
    STAMP(ws, 0, 5);
    {
        LAS char* EXW = L + L_EX + w * 2304 + lane * 16;
        *(LAS f32x4*)EXW = (f32x4){oa[0], oa[1], oa[2], oa[3]};
        *(LAS f32x4*)(EXW + 1024) = (f32x4){oa[4], oa[5], oa[6], oa[7]};
        *(LAS float*)(L + L_EX + w * 2304 + 2048 + lane * 4) = oa[8];
        lds_arrive(L + L_PAIR + w * 4, lane);
    }
    const int wpo = kq * 16384 + (4 * h + q4) * 128 + trl;
    bf16x8 wv[8][2];
    auto wreads = [&](int base, int k0, int k1) {
#pragma unroll
        for (int k = k0; k < k1; ++k)
#pragma unroll
            for (int nt = 0; nt < 2; ++nt) wv[k][nt] = trfrag(L + base + wpo + k * 2048 + ((nt ^ (q4 >> 1)) * 64), 1024);
    };
    const float gate = *(LAS const float*)(L + L_G + (kh * 32 + r32) * 4);
    wreads(L_WOB, 0, 4);
    lds_wait(L + L_PAIR + (w ^ 1) * 4, 1);
    STAMP(ws, 0, 12);
    {
        LAS const char* EXR = L + L_EX + (w ^ 1) * 2304 + lane * 16;
        const f32x4 e0 = *(LAS const f32x4*)EXR, e1 = *(LAS const f32x4*)(EXR + 1024);
        ob[8] += *(LAS const float*)(L + L_EX + (w ^ 1) * 2304 + 2048 + lane * 4);
#pragma unroll
        for (int i = 0; i < 4; ++i) { ob[i] += e0[i]; ob[4 + i] += e1[i]; }
        float t = fmx(ob[8] * 1.0e19f, __builtin_amdgcn_rcpf(ob[8]) * 1.0e19f);
#pragma unroll
        for (int i = 0; i < 8; i += 2) t = fmx(fmx(t, __builtin_fabsf(ob[i])), __builtin_fabsf(ob[i + 1]));
        if (__builtin_expect(__any(!(t < 1.0e38f)), 0)) {
            ob = f32x16{};
            float m = -INFINITY;
            for (int t2 = 0; t2 < 64; ++t2) {
                const unsigned so = kvb + (t2 >> 1) * BLK, fl = (t2 & 1) * 2048, flv = r32 < 16 ? fl : 0;
                const bf16x8 kf = ld16(rs, vK ^ fl, so), v0 = ld16(rs, vV0 ^ flv, so), v1 = ld16(rs, vV1 ^ flv, so);
                f32x16 s = MFMA(kf, qb, f32x16{});
                const float mn = fmx(max16(s), m), al = EXP2(m - mn);
                m = mn;
#pragma unroll
                for (int i = 0; i < 16; ++i) { ob[i] *= al; s[i] = EXP2(s[i] - mn); }
                ob = MFMA(v0, pack8(s, 0), ob);
                ob = MFMA(v1, pack8(s, 1), ob);
            }
        }
        const float rl = gate * __builtin_amdgcn_rcpf(ob[8]);
#pragma unroll
        for (int i = 0; i < 8; ++i) ob[i] *= rl;
        st16(rs, __builtin_bit_cast(bf16x8, __builtin_elementwise_min(__builtin_bit_cast(u32x4, pack8(ob, 0)), (u32x4){0xFFFFFFFEu, 0xFFFFFFFEu, 0xFFFFFFFEu, 0xFFFFFFFEu})),
             lane * 16, WS_O + ((2 * tl + kh) * 32 + head) * 1024);
        SBAR();
    }
    STAMP(ws, 0, 6);
    f32x16 acc[2];
    {
        const unsigned oao = WS_O + ((2 * otl + ot) * 32 + 8 * kq) * 1024;
        bf16x8 of[8];
        wreads(L_WOB, 4, 8);
        SBAR();
#pragma unroll
        for (int k = 0; k < 4; ++k)
#pragma unroll
            for (int nt = 0; nt < 2; ++nt) acc[nt] = MFMA(wv[k][nt], kr[k], k ? acc[nt] : f32x16{});
        SBAR();
#pragma unroll
        for (int k = 0; k < 8; ++k) of[k] = ld16(rs, lane * 16 + (k & 3) * 1024, oao + (k & 4) * 1024);
        lds_arrive(L + L_PAIR + w * 4, lane);
        SBAR();
#pragma unroll
        for (int k = 4; k < 8; ++k)
#pragma unroll
            for (int nt = 0; nt < 2; ++nt) acc[nt] = MFMA(wv[k][nt], v0r[k & 3], acc[nt]);
        SBAR();
        wreads(L_WOA, 0, 8);
        SBAR();
        unsigned pend = 0xFF; int next = 0;
#pragma unroll 1
        for (unsigned spins = 0;; ++spins) {
#pragma unroll
            for (int k = 0; k < 8; ++k) {
                if (pend >> k & 1) {
                    const u32x4 v = __builtin_bit_cast(u32x4, of[k]);
                    if (!__any(__builtin_elementwise_max(__builtin_elementwise_max(v[0], v[1]), __builtin_elementwise_max(v[2], v[3])) == 0xFFFFFFFFu) || spins >= SPIN_CAP) pend &= ~(1u << k);
                }
                if (next == k && !(pend >> k & 1)) {
                    acc[0] = MFMA(wv[k][0], of[k], acc[0]);
                    acc[1] = MFMA(wv[k][1], of[k], acc[1]);
                    ++next;
                }
            }
            if (!pend) break;
            __builtin_amdgcn_s_sleep(RETRY_SLEEP);
            asm volatile("" ::: "memory");
#pragma unroll
            for (int k = 0; k < 8; ++k) if (pend >> k & 1) of[k] = ld16(rs, lane * 16 + (k & 3) * 1024, oao + (k & 4) * 1024);
        }
        STAMP(ws, 0, 7);
    }
    lds_wait(L + L_PAIR + (w ^ 1) * 4, 2);
    LAS char* PR = L + L_WOB + kq * 16384 + ot * 8192;
#pragma unroll
    for (int z = 0; z < 4; ++z)
#pragma unroll
        for (int g = 0; g < 2; ++g) {
            const int b = 8 * (z & 1) + 4 * g;
            *(LAS f32x4*)(PR + (z * 2 + g) * 1024 + lane * 16) = (f32x4){acc[z >> 1][b], acc[z >> 1][b + 1], acc[z >> 1][b + 2], acc[z >> 1][b + 3]};
        }
    STAMP(ws, 0, 8);
    BAR();
    {
        f32x4 r0 = {}, r1 = {};
#pragma unroll
        for (int z = 0; z < 4; ++z) {
            LAS const char* src = L + L_WOB + z * 16384 + ot * 8192 + kq * 2048 + lane * 16;
            r0 += *(LAS const f32x4*)src; r1 += *(LAS const f32x4*)(src + 1024);
        }
        unsigned a0 = pk2(r0[0], r0[1]), a1 = pk2(r0[2], r0[3]), b0 = pk2(r1[0], r1[1]), b1 = pk2(r1[2], r1[3]);
        auto s0_ = __builtin_amdgcn_permlane32_swap(a0, b0, false, false);
        auto s1_ = __builtin_amdgcn_permlane32_swap(a1, b1, false, false);
        *(u32x4*)(out + (size_t)(64 * otl + 32 * ot + r32) * 512 + 64 * ocg + 16 * kq + 8 * h) = (u32x4){s0_[0], s1_[0], s0_[1], s1_[1]};
    }
    if (blockIdx.x == 0 && tid == 0) set_flag(ws, WS_EP, tag);
    STAMP(ws, 0, 9);
    TS_DUMP(ws);
}

extern "C" void kernel_launch(void* const* d_in, const int* in_sizes, int n_in, void* d_out, int out_size, void* d_ws, size_t ws_size, hipStream_t stream) {
    fused_kernel<<<256, 768, 0, stream>>>((const uint16_t*)d_in[0], (const uint16_t*)d_in[1], (const uint16_t*)d_in[4], (const uint16_t*)d_in[5], (const uint16_t*)d_in[7],
                                          (const uint16_t*)d_in[2], (const uint16_t*)d_in[6], (uint16_t*)d_out, (char*)d_ws);
}
```

```cpp
#include <hip/hip_runtime.h>
#include <hip/hip_bf16.h>
#include <stdint.h>
#define HTS_DUMP(ws)
#define STAMP(ws, k, i)
#define TS_DECL
#define TS(i)
#define TS_DUMP(ws)

typedef short bf16x8 __attribute__((ext_vector_type(8)));
typedef short s16x4 __attribute__((ext_vector_type(4)));
typedef float f32x16 __attribute__((ext_vector_type(16)));
typedef float f32x4 __attribute__((ext_vector_type(4)));
typedef float f32x2 __attribute__((ext_vector_type(2)));
typedef unsigned u32x4 __attribute__((ext_vector_type(4)));
typedef unsigned u32x2 __attribute__((ext_vector_type(2)));
typedef __bf16 bf16x2_t __attribute__((ext_vector_type(2)));
typedef unsigned long long u64;
#define LAS __attribute__((address_space(3)))
#define GAS __attribute__((address_space(1)))
#define DI __device__ __forceinline__
#define MFMA(a, b, c) __builtin_amdgcn_mfma_f32_32x32x16_bf16((a), (b), (c), 0, 0, 0)
#define SBAR() __builtin_amdgcn_sched_barrier(0)
#define RLX_AGENT __ATOMIC_RELAXED, __HIP_MEMORY_SCOPE_AGENT
#define EXP2(x) __builtin_amdgcn_exp2f(x)

constexpr float LOG2E = 1.4426950408889634f;
constexpr unsigned BLK = 16512;
constexpr unsigned WS_KV = 0;
constexpr unsigned WS_O = WS_KV + 256 * BLK;
constexpr unsigned WS_PK = WS_O + (4u << 20);
constexpr unsigned WS_KVF = WS_PK + 32 * 32 * 2176;
constexpr unsigned WS_F1 = WS_KVF + 32 * 2176;
constexpr unsigned WS_F2 = WS_F1 + 2048;
constexpr unsigned WS_F3 = WS_F2 + 8192;
constexpr unsigned WS_F5 = WS_F3 + 8192;
constexpr unsigned WS_EP = WS_F5 + 8192;
constexpr unsigned WS_END = WS_EP + 128;
constexpr int L_X = 0, L_RING = 65536;
constexpr int L_PHK = 65536, L_PHQ = 81920, L_KA = 0, L_VA = 8192, L_VB = 16384;
constexpr int L_WOA = 0, L_WOB = 65536;
constexpr int L_WPHI = L_RING + 3 * 16384;
constexpr int L_G = 131072, L_SYNC = L_G + 256, L_SEEN = L_SYNC, L_MCNT = L_SYNC + 4, L_HCNT = L_SYNC + 8, L_OBS = L_SYNC + 12, L_PAIR = L_SYNC + 16, L_DUMMY = L_SYNC + 256;
constexpr int L_M = L_G + 1024, L_QA = L_M + 1024, L_EX = L_QA + 8192;
constexpr int LDS_BYTES = 163840;
#define GRP 4
#define RELOAD_ALL 1
#define KVL_AUX 16
#define RETRY_SLEEP 2
#define WO_T0 4500
#define WO_DT 1700
#define PK_DELAY 24
#define KV_AUX 16
#define F1_STEP 14
#define PRIO_SWITCH 24
constexpr unsigned SPIN_CAP = 1u << 20;

template <int N> struct IC { static constexpr int value = N; };
template <int I, int N, class F> DI void static_for(F&& f) { if constexpr (I < N) { f(IC<I>{}); static_for<I + 1, N>(f); } }
DI unsigned pk2(float a, float b) { f32x2 v = {a, b}; return __builtin_bit_cast(unsigned, __builtin_convertvector(v, bf16x2_t)); }
DI bf16x8 pack8(const f32x16& x, int s) {
    u32x4 p = {pk2(x[8 * s], x[8 * s + 1]), pk2(x[8 * s + 2], x[8 * s + 3]), pk2(x[8 * s + 4], x[8 * s + 5]), pk2(x[8 * s + 6], x[8 * s + 7])};
    return __builtin_bit_cast(bf16x8, p);
}
DI float fmx(float a, float b) { return __builtin_elementwise_maximum(a, b); }
DI float max32(float v) { auto r = __builtin_amdgcn_permlane32_swap(__float_as_uint(v), __float_as_uint(v), false, false); return fmx(__uint_as_float(r[0]), __uint_as_float(r[1])); }
DI float lmax16(const f32x16& s) {
    const float a = fmx(fmx(s[0], s[1]), s[2]), b = fmx(fmx(s[3], s[4]), s[5]), c = fmx(fmx(s[6], s[7]), s[8]), d = fmx(fmx(s[9], s[10]), s[11]), e = fmx(fmx(s[12], s[13]), s[14]);
    return fmx(fmx(fmx(a, b), c), fmx(fmx(d, e), s[15]));
}
DI float max16(const f32x16& s) { return max32(lmax16(s)); }
DI void glds2(unsigned v0, unsigned v1, const void* sbase, unsigned ldsdst) {
    unsigned keep;
    asm volatile("s_mov_b32 %0, m0\n\ts_mov_b32 m0, %4\n\ts_nop 0\n\tglobal_load_lds_dwordx4 %1, %3\n\tglobal_load_lds_dwordx4 %2, %3 offset:1024\n\ts_mov_b32 m0, %0"
                 : "=&s"(keep) : "v"(v0), "v"(v1), "s"(sbase), "s"(ldsdst) : "memory");
}
DI void glds4(const unsigned (&v)[4], const void* sbase, unsigned ldsdst) {
    unsigned keep;
    asm volatile("s_mov_b32 %0, m0\n\ts_mov_b32 m0, %6\n\ts_nop 0\n\tglobal_load_lds_dwordx4 %1, %5\n\tglobal_load_lds_dwordx4 %2, %5 offset:1024\n\t"
                 "global_load_lds_dwordx4 %3, %5 offset:2048\n\tglobal_load_lds_dwordx4 %4, %5 offset:3072\n\ts_mov_b32 m0, %0"
                 : "=&s"(keep) : "v"(v[0]), "v"(v[1]), "v"(v[2]), "v"(v[3]), "s"(sbase), "s"(ldsdst) : "memory");
}
DI void touch(unsigned voff, const void* sbase, unsigned ldsdst) {
    unsigned keep;
    asm volatile("s_mov_b32 %0, m0\n\ts_mov_b32 m0, %3\n\ts_nop 0\n\tglobal_load_lds_dword %1, %2 sc1\n\ts_mov_b32 m0, %0"
                 : "=&s"(keep) : "v"(voff), "s"(sbase), "s"(ldsdst) : "memory");
}
typedef short v4i16_t __attribute__((ext_vector_type(4)));
DI s16x4 trread(LAS const char* p) { return __builtin_bit_cast(s16x4, __builtin_amdgcn_ds_read_tr16_b64_v4i16((LAS v4i16_t*)p)); }
DI bf16x8 trfrag(LAS const char* p, int d2) { s16x4 lo = trread(p), hi = trread(p + d2); return __builtin_shufflevector(lo, hi, 0, 1, 2, 3, 4, 5, 6, 7); }
DI bf16x8 ldsfrag(LAS const char* p) { return *(LAS const bf16x8*)p; }
#define BAR() asm volatile("s_waitcnt lgkmcnt(0)\n\ts_barrier" ::: "memory")
#define VM_BAR(N) asm volatile("s_waitcnt vmcnt(" #N ") lgkmcnt(0)\n\ts_barrier" ::: "memory")
#define DRAIN() asm volatile("s_waitcnt vmcnt(0)" ::: "memory")
typedef __amdgpu_buffer_rsrc_t rsrc_t;
DI bf16x8 ld16(rsrc_t r, unsigned voff, unsigned soff) { return __builtin_bit_cast(bf16x8, __builtin_amdgcn_raw_buffer_load_b128(r, voff, soff, 16)); }
DI bf16x8 ldkv(rsrc_t r, unsigned voff, unsigned soff) { return __builtin_bit_cast(bf16x8, __builtin_amdgcn_raw_buffer_load_b128(r, voff, soff, KVL_AUX)); }
DI void st16(rsrc_t r, bf16x8 v, unsigned voff, unsigned soff) { __builtin_amdgcn_raw_buffer_store_b128(__builtin_bit_cast(u32x4, v), r, voff, soff, 16); }
DI void st16kv(rsrc_t r, bf16x8 v, unsigned voff, unsigned soff) { __builtin_amdgcn_raw_buffer_store_b128(__builtin_bit_cast(u32x4, v), r, voff, soff, KV_AUX); }
DI unsigned lds_word(LAS char* p) { return __builtin_amdgcn_readfirstlane(*(LAS volatile unsigned*)p); }
DI void lds_wait(LAS char* p, unsigned want) {
#pragma unroll 1
    for (unsigned spins = 0; spins < (SPIN_CAP << 6) && lds_word(p) < want; ++spins) __builtin_amdgcn_s_sleep(1);
    asm volatile("" ::: "memory");
}
DI void lds_arrive(LAS char* p, int lane) {
    asm volatile("s_waitcnt lgkmcnt(0)" ::: "memory");
    if (lane == 0) __hip_atomic_fetch_add((LAS unsigned*)p, 1u, __ATOMIC_RELAXED, __HIP_MEMORY_SCOPE_WORKGROUP);
}
DI void set_flag(char* ws, unsigned off, u64 tag) { __hip_atomic_store((GAS u64*)(ws + off), tag, RLX_AGENT); }
template <int FIRST, int GAP> DI void wait_flags(char* ws, unsigned off, int n, u64 tag, int lane) {
    GAS u64* p = (GAS u64*)(ws + off) + (lane < n ? lane : 0);
    if (FIRST) __builtin_amdgcn_s_sleep(FIRST);
#pragma unroll 1
    for (unsigned spins = 0; spins < SPIN_CAP; ++spins) {
        const u64 v = __hip_atomic_load(p, RLX_AGENT);
        if (__all(v == tag)) break;
        __builtin_amdgcn_s_sleep(GAP);
    }
    __builtin_amdgcn_fence(__ATOMIC_ACQUIRE, "wavefront");
    asm volatile("" ::: "memory");
}

__global__ __launch_bounds__(768) void fused_kernel(const uint16_t* __restrict__ x, const uint16_t* __restrict__ WA, const uint16_t* __restrict__ WB,
                                                    const uint16_t* __restrict__ wphi, const uint16_t* __restrict__ gatew,
                                                    const uint16_t* __restrict__ woA, const uint16_t* __restrict__ woB,
                                                    uint16_t* __restrict__ out, char* ws) {
    __shared__ __attribute__((aligned(16))) char lds[LDS_BYTES];
    LAS char* L = (LAS char*)lds;
    u64 pc0; asm volatile("s_getpc_b64 %0" : "=s"(pc0));
    const unsigned lds0 = (unsigned)(uintptr_t)lds;
    const int tid = threadIdx.x, lane = tid & 63, w = __builtin_amdgcn_readfirstlane(tid >> 6);
    const int r32 = lane & 31, h = lane >> 5;
    const int xcd = blockIdx.x & 7, idx = blockIdx.x >> 3;
    const int qd = 2 * (xcd & 3) + (idx & 1), tl = 2 * (idx >> 1) + (xcd >> 2);
    const int s0 = ((tl >> 1) + 4 * (qd & 1)) & 7;
    const int otl = 4 * xcd + (idx >> 3), ocg = idx & 7;
    const rsrc_t rs = __builtin_amdgcn_make_buffer_rsrc(ws, 0, WS_END, 0x00020000);
    u64 tag;
#define EPOCH_BAR(N) do { asm volatile("s_load_dwordx2 %0, %1, 0x0\n\ts_waitcnt vmcnt(" #N ") lgkmcnt(0)\n\ts_barrier" : "=s"(tag) : "s"(ws + WS_EP) : "memory"); tag += 1; } while (0)
    const int q4 = (lane & 15) >> 2;
    const int trl = ((lane >> 4) & 1) * 32 + (lane & 3) * 8;
    STAMP(ws, 0, 0);
    TS_DECL; TS(0); TS(16);

    asm volatile("" :: "s"(x), "s"(WA), "s"(WB), "s"(wphi), "s"(gatew), "s"(ws));
    if (__builtin_expect(w >= 8, 1)) {
        const int j = w - 8;
        const unsigned vsw = (unsigned)(((lane & 7) ^ (((lane >> 4) & 1) << 2)) << 4);
        unsigned vw[4];
#pragma unroll
        for (int u = 0; u < 4; ++u) vw[u] = (unsigned)((lane >> 3) * 3072) + vsw + u * (8 * 3072 - 1024);
        const unsigned vx0 = (unsigned)((lane >> 3) * 1024 + (((lane & 7) ^ (lane >> 4)) << 4)), vx1 = (vx0 ^ 64u) + 8192 - 1024;
        const char* xb = (const char*)x + (size_t)(64 * tl + 16 * j) * 1024;
        const size_t wo_ = (size_t)((j & 1) * 32) * 3072 + qd * 128;
        const char* wb[3] = {(const char*)WA + wo_ + 1024 + (j >> 1) * 1024, (const char*)WB + wo_ + (j >> 1) * 1024, (j >> 1) ? (const char*)WB + wo_ + 2048 : (const char*)WA + wo_};
        auto issue_x = [&](int i) { const int kc = (s0 + i) & 7; glds2(vx0, vx1, xb + kc * 128, lds0 + L_X + i * 8192 + j * 2048); };
        auto issue_w = [&](int i) { glds4(vw, wb[i >> 3] + ((s0 + i) & 7) * (64 * 3072), lds0 + L_RING + ((i + 4) % 6) * 16384 + j * 4096); };
        __builtin_amdgcn_s_setprio(3);
        TS(4);
        issue_w(0); issue_x(0); issue_x(1); issue_x(2); issue_x(3);
        bf16x8 gw; unsigned warm;
        asm volatile("global_load_dwordx4 %0, %1, %2" : "=v"(gw) : "v"((lane & 15) * 64 + (lane >> 4) * 16), "s"(gatew) : "memory");
        glds2(lane * 16, lane * 16, wphi + qd * 1024, lds0 + L_WPHI);
        issue_w(1);
        TS(5);
        VM_BAR(13); issue_w(2); issue_w(3);
        VM_BAR(8); issue_x(4); issue_w(4);
        EPOCH_BAR(10); issue_x(5); issue_w(5);
        VM_BAR(12); issue_x(6); issue_w(6);
        VM_BAR(12); issue_x(7); issue_w(7);
        VM_BAR(12); issue_w(8); issue_w(9);
        asm volatile("global_load_dword %0, %1, %2" : "=v"(warm) : "v"(((tl >> 1) * 32 + (lane & 15)) * 3072), "s"(wb[2] - (j & 1) * 16 * 3072) : "memory");
        VM_BAR(15);
        VM_BAR(9); issue_w(10); issue_w(11);
        f32x4 gl = {};
        const int grow = 16 * j + (lane & 15);
        int gxa = L_X + grow * 128 + (((lane >> 4) ^ ((grow >> 1) & 7)) << 4);
        u64 f1early = 0;
#pragma unroll
        for (int i = 8; i < 24; i += 2) {
            if (i < 22) VM_BAR(8); else VM_BAR(0);
            if (i == 8) asm volatile("" : "+v"(gw), "+v"(warm));
            bf16x8 xg[4], ga[4];
            if (i < 16) {
#pragma unroll
                for (int u = 0; u < 4; ++u) {
                    xg[u] = ldsfrag(L + (gxa ^ ((u & 1) << 6)) + (i - 8 + (u >> 1)) * 8192);
                    ga[u] = (lane & 15) == 2 * ((s0 + i + (u >> 1)) & 7) + (u & 1) ? gw : bf16x8{};
                }
            }
            SBAR();
            if (i < 20) { issue_w(i + 4); issue_w(i + 5); }
            SBAR();
            if (i < 16) {
#pragma unroll
                for (int u = 0; u < 4; ++u) gl = __builtin_amdgcn_mfma_f32_16x16x32_bf16(ga[u], xg[u], gl, 0, 0, 0);
            }
            if (j == 0 && i == 20) {
                f1early = __hip_atomic_load((GAS u64*)(ws + WS_F1 + qd * 256) + (lane & 31), RLX_AGENT);
                const bool seen = __all(f1early == tag);
                if (lane < 16) *(LAS unsigned*)(L + L_SYNC + lane * 4) = (lane == 0 && seen) ? 1u : 0u;
            }
            if (j == 0 && i == 22 && !__all(f1early == tag)) {
                TS(1);
                wait_flags<0, 2>(ws, WS_F1 + qd * 256, 32, tag, lane);
                *(LAS volatile unsigned*)(L + L_SEEN) = 1;
                TS(2);
            }
            SBAR();
        }
        {
            float z = (gl[0] + gl[1]) + (gl[2] + gl[3]);
            z += __shfl_xor(z, 16); z += __shfl_xor(z, 32);
            if (lane < 16) *(LAS float*)(L + L_G + grow * 4) = __builtin_amdgcn_rcpf(1.f + EXP2(-z * LOG2E));
        }
        BAR();
        TS(3);
        const u64 tp2 = __builtin_amdgcn_s_memtime();
        __builtin_amdgcn_s_sleep(6);
        const int head = 4 * qd + j;
        bf16x8 phq[2][2]; float g2[2];
        {
            f32x16 kv = {};
            const bool keep = (r32 >> 4) == (j & 1);
            bf16x8 pf[2][2], ve[2][2];
#pragma unroll
            for (int tt = 0; tt < 2; ++tt) {
#pragma unroll
                for (int s = 0; s < 2; ++s) {
                    { LAS const char* pp = L + L_PHK + (j * 2 + tt) * 2048 + (16 * s + 4 * h + q4) * 64; const int sw = trl ^ ((2 * h + (q4 >> 1)) * 8);
                      pf[tt][s] = __builtin_shufflevector(trread(pp + sw), trread(pp + 512 + (sw ^ 32)), 0, 1, 2, 3, 4, 5, 6, 7); }
                    ve[tt][s] = ldsfrag(L + L_VB + (((j >> 1) * 2 + tt) * 2 + s) * 1024 + lane * 16);
                    phq[tt][s] = ldsfrag(L + L_PHQ + ((j * 2 + tt) * 2 + s) * 1024 + lane * 16);
                }
                g2[tt] = 1.f - *(LAS const float*)(L + L_G + (tt * 32 + r32) * 4);
            }
            lds_arrive(L + L_HCNT, lane);
            __builtin_amdgcn_s_sleep(PK_DELAY);
#pragma unroll
            for (int tt = 0; tt < 2; ++tt)
#pragma unroll
                for (int s = 0; s < 2; ++s) {
#pragma unroll
                    for (int e = 0; e < 8; ++e) ve[tt][s][e] = keep ? ve[tt][s][e] : (short)0x3F80;
                    kv = MFMA(pf[tt][s], ve[tt][s], kv);
                }
            const int col = keep ? (r32 & 15) : 16;
            const unsigned vo = (keep || (r32 & 15) == 0) ? (unsigned)((col * 2 + h) * 16) : 0x40000000u;
#pragma unroll
            for (int g = 0; g < 4; ++g) st16(rs, __builtin_bit_cast(bf16x8, (f32x4){kv[4 * g], kv[4 * g + 1], kv[4 * g + 2], kv[4 * g + 3]}), vo + g * 544, WS_PK + ((qd * 32 + tl) * 4 + j) * 2176);
            DRAIN();
            if (lane == 0) set_flag(ws, WS_F2 + (head * 32 + tl) * 8, tag);
        }
        unsigned vo[4];
#pragma unroll
        for (int u = 0; u < 4; ++u) vo[u] = (unsigned)((lane >> 3) * 1024) + vsw + u * (8192 - 1024);
        lds_wait(L + L_HCNT, 4);
        int wo_done = 0;
        auto wo_service = [&]() {
            if (wo_done < 2 && __builtin_amdgcn_s_memtime() - tp2 >= (u64)(WO_T0 + WO_DT * (j + 4 * wo_done))) {
                const char* src = (const char*)(wo_done ? woA : woB) + (size_t)(128 * j) * 1024 + ocg * 128;
#pragma unroll
                for (int i = 0; i < 4; ++i) glds4(vo, src + i * 32768, lds0 + (wo_done ? L_WOA : L_WOB) + j * 16384 + i * 4096);
                ++wo_done;
            }
        };
        auto wait_slow = [&](unsigned off, int n) {
            GAS u64* p = (GAS u64*)(ws + off) + (lane < n ? lane : 0);
#pragma unroll 1
            for (unsigned spins = 0; spins < SPIN_CAP; ++spins) {
#pragma unroll 1
                for (int z = 0; z < (spins ? 2 : 6); ++z) { wo_service(); __builtin_amdgcn_s_sleep(6); }
                if (__all(__hip_atomic_load(p, RLX_AGENT) == tag)) break;
            }
            __builtin_amdgcn_fence(__ATOMIC_ACQUIRE, "wavefront");
            asm volatile("" ::: "memory");
        };
        STAMP(ws, 0, 1);
        if (tl < 17) {
            wait_slow(WS_F2 + head * 256, 32);
            const unsigned so = WS_PK + (qd * 128 + j) * 2176 + tl * 128, v1 = (unsigned)((lane >> 3) * 8704 + (lane & 7) * 16);
            f32x4 a = (__builtin_bit_cast(f32x4, ld16(rs, v1, so)) + __builtin_bit_cast(f32x4, ld16(rs, v1, so + 8 * 8704))) +
                      (__builtin_bit_cast(f32x4, ld16(rs, v1, so + 16 * 8704)) + __builtin_bit_cast(f32x4, ld16(rs, v1, so + 24 * 8704)));
#pragma unroll
            for (int o = 8; o < 64; o <<= 1) {
#pragma unroll
                for (int e = 0; e < 4; ++e) a[e] += __shfl_xor(a[e], o);
            }
            st16(rs, __builtin_bit_cast(bf16x8, a), lane < 8 ? (unsigned)(lane * 16) : 0x40000000u, WS_KVF + head * 2176 + tl * 128);
            DRAIN();
            if (lane == 0) set_flag(ws, WS_F3 + (head * 32 + tl) * 8, tag);
        } else {
#pragma unroll 1
            for (int z = 0; z < 10; ++z) { wo_service(); __builtin_amdgcn_s_sleep(6); }
        }
        STAMP(ws, 0, 2);
        wait_slow(WS_F3 + head * 256, 17);
        {
            const unsigned v2 = (unsigned)(((r32 < 16 ? r32 : 16) * 2 + h) * 16);
            f32x16 kv;
#pragma unroll
            for (int g = 0; g < 4; ++g) { const f32x4 t = __builtin_bit_cast(f32x4, ld16(rs, v2 + g * 544, WS_KVF + head * 2176)); kv[4 * g] = t[0]; kv[4 * g + 1] = t[1]; kv[4 * g + 2] = t[2]; kv[4 * g + 3] = t[3]; }
            const bf16x8 k0 = pack8(kv, 0), k1 = pack8(kv, 1);
#pragma unroll
            for (int tt = 0; tt < 2; ++tt) {
                f32x16 ob = MFMA(k0, phq[tt][0], f32x16{});
                ob = MFMA(k1, phq[tt][1], ob);
                const float rd = g2[tt] * __builtin_amdgcn_rcpf(ob[8] + 1e-6f);
#pragma unroll
                for (int i = 0; i < 8; ++i) ob[i] *= rd;
                st16(rs, pack8(ob, 0), lane * 16, WS_O + (2u << 20) + ((2 * tl + tt) * 32 + head) * 1024);
                st16(rs, (bf16x8){-1, -1, -1, -1, -1, -1, -1, -1}, lane * 16, WS_O + ((2 * tl + tt) * 32 + head) * 1024);
            }
            DRAIN();
            if (lane == 0) set_flag(ws, WS_F5 + (tl * 32 + head) * 8, tag);
        }
        STAMP(ws, 0, 3);
        wait_slow(WS_F5 + (otl * 32 + 8 * j) * 8, 8);
        if (lane < 16) touch((lane >> 3) * 32768 + (lane & 7) * 128, ws + WS_O + (2u << 20) + (2 * otl * 32 + 8 * j + ocg) * 1024, lds0 + L_DUMMY);
        while (wo_done < 2) { wo_service(); __builtin_amdgcn_s_sleep(6); }
        DRAIN();
        lds_arrive(L + L_OBS, lane);
        STAMP(ws, 0, 5);
        HTS_DUMP(ws);
        return;
    }

    if (w == 0) {
        const char* a = (const char*)(pc0 & ~127ull) + (idx * 8 + lane) * 128;
        if (lane == 8) {
            a = ws + (size_t)(idx < 21 ? idx : 0) * 524288;
            if (idx >= 22) a = idx < 25 ? (const char*)WB + (idx - 22) * 786368 : idx < 28 ? (const char*)out + (idx - 25) * 1048512 : idx < 30 ? (const char*)woA + (idx - 28) * 524224 : (const char*)woB + (idx - 30) * 524224;
        }
        unsigned keep;
        if (lane < 9) asm volatile("s_mov_b32 %0, m0\n\ts_mov_b32 m0, %2\n\ts_nop 0\n\tglobal_load_lds_dword %1, off\n\ts_mov_b32 m0, %0" : "=&s"(keep) : "v"(a), "s"(lds0 + L_WPHI + 4096) : "memory");
    }
    const int role = w >> 2, p = (w >> 1) & 1, tt = w & 1;
    const int hq = w >> 1, kh = w & 1, head = 4 * qd + hq;
    const unsigned vK = w * 2048 + lane * 16;
    const unsigned vV0 = r32 < 16 ? (unsigned)(w * 2048 + 1024 + h * 256 + r32 * 16) : ((r32 & 11) == 0 ? 16384u : 0x40000000u);
    const unsigned vV1 = r32 < 16 ? vV0 + 512 : vV0;
    const unsigned kvb = WS_KV + qd * 32 * BLK;
#define KVOFF(t) (kvb + (((t) + tl) & 31) * BLK)
    bf16x8 kr[4], v0r[4], v1r[4];
    {
        const int xrow = tt * 32 + r32;
        const int xa0 = L_X + xrow * 128 + ((h ^ ((xrow >> 1) & 7)) << 4);
        const int wa0 = L_RING + (8 * h + q4) * 128 + ((p ^ (q4 >> 1)) * 64) + trl;
        bf16x8 xf[2][4], w0[2][4], own[2];
        f32x16 acc;
        int wsb[3], xsb[4];
#pragma unroll
        for (int i = 0; i < 3; ++i) { wsb[i] = wa0 + i * 32768; asm volatile("" : "+v"(wsb[i])); }
#pragma unroll
        for (int s = 0; s < 4; ++s) { xsb[s] = xa0 ^ (s << 5); asm volatile("" : "+v"(xsb[s])); }
        auto rx = [&](int i, int s) __attribute__((always_inline)) { if (i < 24) xf[i & 1][s] = ldsfrag(L + xsb[s] + (i & 7) * 8192); };
        auto rw = [&](int i, int s, int ROLE) __attribute__((always_inline)) { if (i < 24) w0[i & 1][s] = trfrag(L + wsb[((i + 4) % 6) >> 1] + (i & 1) * 16384 + ROLE * 8192 + s * 2048, 512); };
        auto mm = [&](int i, int s, int ROLE) __attribute__((always_inline)) {
            if (i >= 0) {
                const f32x16 c = ((i & 7) == 0 && s == 0) ? f32x16{} : acc;
                acc = (ROLE && (i >> 3) != 1) ? MFMA(xf[i & 1][s], w0[i & 1][s], c) : MFMA(w0[i & 1][s], xf[i & 1][s], c);
            }
            SBAR();
        };
        auto publish = [&](int ROLE, int part) __attribute__((always_inline)) {
            const unsigned so = kvb + tl * BLK + (4 * p + tt) * 2048;
            if (ROLE == 0) st16kv(rs, own[part], lane * 16, so + part * 4096);
            else st16kv(rs, own[part], (unsigned)((r32 >> 4) * 4096 + 1024 + h * 256 + (r32 & 15) * 16) + part * 512, so);
            const bf16x8 ones = {0x3F80, 0x3F80, 0x3F80, 0x3F80, 0x3F80, 0x3F80, 0x3F80, 0x3F80};
            if (ROLE == 0 && part == 0) st16kv(rs, ones, tid < 8 ? (unsigned)(16384 + lane * 16) : 0x40000000u, so);
        };
        auto gemm = [&](int ROLE) __attribute__((always_inline)) {
            bf16x8 wf[2], ph[2][2];
            f32x16 f[2]; float nm[2];
#define PINF(a) asm volatile("" : "+v"(f[a]))
            auto phi = [&](int i, int s) __attribute__((always_inline)) {
                constexpr unsigned char T[32] = {0, 0, 0x10, 0x11,  0x20, 0x21, 0, 0x22,  0x23, 0x30, 0, 0x40,  0x31, 0x32, 0, 0x33,  0x50, 0x41, 0, 0x51,  0x52, 0x60, 0, 0x53,  0x61, 0x62, 0, 0x63,  0, 0, 0, 0};
                const int op = i >= 17 ? T[4 * (i - 17) + s] : 0, k = op & 15;
                if ((op >> 4) == 1) { nm[k] = -lmax16(f[k]) * LOG2E; asm volatile("" : "+v"(nm[k])); }
                if ((op >> 4) == 2 || (op >> 4) == 3) {
                    const int a = (op >> 4) - 2;
#pragma unroll
                    for (int e = 4 * k; e < 4 * k + 4; e += 2) {
                        const f32x2 t = __builtin_elementwise_fma((f32x2){f[a][e], f[a][e + 1]}, (f32x2){LOG2E, LOG2E}, (f32x2){nm[a], nm[a]});
                        f[a][e] = EXP2(t[0]); f[a][e + 1] = EXP2(t[1]);
                    }
                    PINF(a);
                }
                if ((op >> 4) == 4) {
                    const f32x16& g = f[k];
#define G2(e) (f32x2){g[2 * e], g[2 * e + 1]}
                    const f32x2 t = ((G2(0) + G2(1)) + (G2(2) + G2(3))) + ((G2(4) + G2(5)) + (G2(6) + G2(7)));
                    nm[k] = __builtin_amdgcn_rcpf(t[0] + t[1]); asm volatile("" : "+v"(nm[k]));
                }
                if ((op >> 4) == 5) {
                    const int b = k >> 1, hs = k & 1;
#pragma unroll
                    for (int e = 8 * hs; e < 8 * hs + 8; e += 2) { const f32x2 t = (f32x2){f[b][e], f[b][e + 1]} * nm[b]; f[b][e] = t[0]; f[b][e + 1] = t[1]; }
                    ph[b][hs] = pack8(f[b], hs); asm volatile("" : "+v"(ph[b][hs]));
                }
                if ((op >> 4) == 6) {
                    const int b = k >> 1, hs = k & 1;
                    if (ROLE == 0) *(LAS bf16x8*)(L + L_PHQ + (((2 * p + b) * 2 + tt) * 2 + hs) * 1024 + lane * 16) = ph[b][hs];
                    else {
                        const u32x4 u = __builtin_bit_cast(u32x4, ph[b][hs]);
#pragma unroll
                        for (int g = 0; g < 2; ++g)
                            *(LAS u32x2*)(L + L_PHK + ((2 * p + b) * 2 + tt) * 2048 + r32 * 64 + 8 * ((2 * (2 * hs + g) + h) ^ ((r32 >> 1) & 7))) = (u32x2){u[2 * g], u[2 * g + 1]};
                    }
                }
                SBAR();
            };
            auto first_loads = [&](int t) __attribute__((always_inline)) {
                kr[t] = ldkv(rs, vK, KVOFF(t)); v0r[t] = ldkv(rs, vV0, KVOFF(t)); v1r[t] = ldkv(rs, vV1, KVOFF(t));
                SBAR();
            };
            static_for<0, 25>([&](auto ic) __attribute__((always_inline)) {
                constexpr int i = ic.value;
                if (i == F1_STEP) { VM_BAR(0); if (tid == 0) set_flag(ws, WS_F1 + (qd * 32 + tl) * 8, tag); }
                else if (i == 0) EPOCH_BAR(63);
                else if (i == 2) { __builtin_amdgcn_s_waitcnt(0x0F70); BAR(); }
                else if (i < 8 || (i < 24 && !(i & 1))) BAR();
                if (i == 0 || i == 1 || i == 8) TS(i + 1); else if (i == 22) TS(17);
                SBAR();
                if (i == 9) {
                    if (ROLE == 0) {
#pragma unroll
                        for (int e = 0; e < 16; ++e) acc[e] *= 0.25f * LOG2E;
                    }
                    own[0] = pack8(acc, 0); own[1] = pack8(acc, 1);
                    publish(ROLE, 0); publish(ROLE, 1);
                    SBAR();
                }
                if (i == 17) {
                    f[0] = MFMA(wf[0], pack8(acc, 0), f32x16{}); f[1] = MFMA(wf[1], pack8(acc, 1), f32x16{});
                    SBAR();
                }
                mm(i - 1, 0, ROLE); rx(i, 0); rw(i, 0, ROLE); rx(i, 1); phi(i, 0);
                mm(i - 1, 1, ROLE); rw(i, 1, ROLE); rx(i, 2); rw(i, 2, ROLE); phi(i, 1);
                mm(i - 1, 2, ROLE); rx(i, 3); rw(i, 3, ROLE); phi(i, 2);
                mm(i - 1, 3, ROLE);
                if (i == 1) {
#pragma unroll
                    for (int a = 0; a < 2; ++a) wf[a] = trfrag(L + L_WPHI + (2 * p + a) * 512 + (4 * h + q4) * 32 + (((lane >> 4) & 1) * 2 + ((lane >> 1) & 1)) * 8, 256);
                }
                if (i == 3) {
                    const short sg = (lane & 4) ? (short)0x8000 : (short)0;
#pragma unroll
                    for (int a = 0; a < 2; ++a) {
#pragma unroll
                        for (int e = 0; e < 8; ++e) wf[a][e] ^= sg;
                        asm volatile("" : "+v"(wf[a]));
                    }
                }
                if (i == 18) {
#pragma unroll
                    for (int a = 0; a < 2; ++a) *(LAS bf16x8*)(L + (ROLE ? L_VA + ((p * 2 + tt) * 2 + a) * 1024 : L_KA + ((2 * p + a) * 2 + tt) * 1024) + lane * 16) = own[a];
                }
                if (i == 20) {
                    kr[0] = ldsfrag(L + L_KA + (hq * 2 + kh) * 1024 + lane * 16);
                    v0r[0] = ldsfrag(L + L_VA + (((hq >> 1) * 2 + kh) * 2) * 1024 + (h * 32 + (hq & 1) * 16 + (r32 & 15)) * 16);
                    v1r[0] = ldsfrag(L + L_VA + (((hq >> 1) * 2 + kh) * 2 + 1) * 1024 + (h * 32 + (hq & 1) * 16 + (r32 & 15)) * 16);
                }
                if (i == 24) {
                    const short cf = (r32 & 11) == 0 ? (short)0x3F80 : (short)0;
#pragma unroll
                    for (int e = 0; e < 8; ++e) { v0r[0][e] = r32 < 16 ? v0r[0][e] : cf; v1r[0][e] = r32 < 16 ? v1r[0][e] : cf; }
                    asm volatile("" : "+v"(v0r[0]), "+v"(v1r[0]));
                }
                phi(i, 3);
            });
            TS(18);
            if (ROLE == 1) { lds_wait(L + L_SEEN, 1); first_loads(1); first_loads(2); }
            if (ROLE == 0) {
                const bf16x8 q0 = pack8(acc, 0), q1 = pack8(acc, 1);
                const f32x16 s0_ = MFMA(own[0], q0, f32x16{}), s1_ = MFMA(own[1], q1, f32x16{});
                *(LAS bf16x8*)(L + L_QA + ((2 * p) * 2 + tt) * 1024 + lane * 16) = q0;
                *(LAS bf16x8*)(L + L_QA + ((2 * p + 1) * 2 + tt) * 1024 + lane * 16) = q1;
                const float ma = max16(s0_), mb = max16(s1_);
                *(LAS float*)(L + L_M + (((2 * p + h) * 2 + tt) * 32 + r32) * 4) = h ? mb : ma;
                SBAR();
                lds_wait(L + L_SEEN, 1); first_loads(1); first_loads(2);
            } else {
#pragma unroll
                for (int s = 0; s < 2; ++s) *(LAS bf16x8*)(L + L_VB + ((p * 2 + tt) * 2 + s) * 1024 + lane * 16) = pack8(acc, s);
            }
        };
        if (role == 0) gemm(0); else gemm(1);
    }
    TS(20);
    BAR();
    TS(21);
    const bf16x8 qa = ldsfrag(L + L_QA + (hq * 2 + (kh ^ 1)) * 1024 + lane * 16), qb = ldsfrag(L + L_QA + (hq * 2 + kh) * 1024 + lane * 16);
    const float m0 = *(LAS const float*)(L + L_M + (hq * 64 + r32) * 4), m1 = *(LAS const float*)(L + L_M + (hq * 64 + 32 + r32) * 4);
    TS(23);
    const int ot = w & 1, kq = w >> 1;
    f32x16 oa, ob;
    {
        f32x16 negm;
        const float nm = -fmx(m0, m1);
#pragma unroll
        for (int i = 0; i < 16; ++i) negm[i] = nm;
        f32x16 sa = MFMA(kr[0], qa, negm), sb;
        kr[3] = ldkv(rs, vK, KVOFF(3)); v0r[3] = ldkv(rs, vV0, KVOFF(3));
        TS(24);
        STAMP(ws, 0, 4);
        u32x4 p1, p2;
#define E(S, i) S[i] = EXP2(S[i])
#define C(P, S, i) P[(i) & 3] = pk2(S[2 * (i)], S[2 * (i) + 1])
#define PHASE(S, M1, M2, M3, LD1, LD2, LD3)                                                       \
    do {                                                                                          \
        M1; LD1; SBAR();                                                                          \
        E(S, 0); E(S, 1); E(S, 2); E(S, 3); E(S, 4); SBAR();                                      \
        M2; LD2; SBAR();                                                                          \
        E(S, 5); E(S, 6); E(S, 7); SBAR(); C(p1, S, 0); C(p1, S, 1); C(p1, S, 2); C(p1, S, 3); SBAR(); E(S, 8); E(S, 9); SBAR(); \
        M3; LD3; SBAR();                                                                          \
        E(S, 10); E(S, 11); E(S, 12); E(S, 13); E(S, 14); E(S, 15); SBAR();                       \
        C(p2, S, 4); C(p2, S, 5); C(p2, S, 6); C(p2, S, 7); SBAR();                               \
    } while (0)
#define P1 __builtin_bit_cast(bf16x8, p1)
#define P2 __builtin_bit_cast(bf16x8, p2)
        const unsigned obo = WS_O + (2u << 20) + ((2 * otl + ot) * 32 + 8 * kq) * 1024 - w * 2048;
        auto body = [&](int t0, bool FIRST, bool LAST) {
#pragma unroll
            for (int u = 0; u < 4; ++u) {
                const int t = t0 + u;
                const bool Z = FIRST && u == 0;
                PHASE(sa, sb = MFMA(kr[u], qb, negm), if (!Z) ob = MFMA(v1r[(u + 3) & 3], P2, ob), oa = MFMA(v0r[u], P1, Z ? f32x16{} : oa),
                      kr[u] = ldkv(rs, vK, LAST ? obo + u * 1024 : KVOFF(t + 4)), if (!LAST || u == 0) v1r[(u + 3) & 3] = ldkv(rs, vV1, KVOFF(t + 3)), (void)0);
                PHASE(sb, if (!LAST || u < 3) sa = MFMA(kr[(u + 1) & 3], qa, negm), oa = MFMA(v1r[u], P2, oa), ob = MFMA(v0r[u], P1, Z ? f32x16{} : ob),
                      (void)0, (void)0, v0r[u] = LAST ? ldkv(rs, vK, obo + (4 + u) * 1024) : ldkv(rs, vV0, KVOFF(t + 4)));
            }
        };
        if (w < 4) __builtin_amdgcn_s_setprio(2); else __builtin_amdgcn_s_setprio(1);
        STAMP(ws, 3, 0);
        body(0, true, false);
        for (int t0 = 4; t0 < 28; t0 += 4) {
            if (w < 4 && t0 == PRIO_SWITCH) __builtin_amdgcn_s_setprio(0);
            STAMP(ws, 3, t0 >> 2);
            body(t0, false, false);
        }
        STAMP(ws, 3, 7);
        lds_wait(L + L_OBS, 4);
        body(28, false, true);
        STAMP(ws, 3, 8);
        ob = MFMA(v1r[3], P2, ob);
#undef PHASE
#undef E
#undef C
    }
    __builtin_amdgcn_s_setprio(0);
    STAMP(ws, 0, 5);
    {
        LAS char* EXW = L + L_EX + w * 2304 + lane * 16;
        *(LAS f32x4*)EXW = (f32x4){oa[0], oa[1], oa[2], oa[3]};
        *(LAS f32x4*)(EXW + 1024) = (f32x4){oa[4], oa[5], oa[6], oa[7]};
        *(LAS float*)(L + L_EX + w * 2304 + 2048 + lane * 4) = oa[8];
        lds_arrive(L + L_PAIR + w * 4, lane);
    }
    const int wpo = kq * 16384 + (4 * h + q4) * 128 + trl;
    bf16x8 wv[8][2];
    auto wreads = [&](int base, int k0, int k1) {
#pragma unroll
        for (int k = k0; k < k1; ++k)
#pragma unroll
            for (int nt = 0; nt < 2; ++nt) wv[k][nt] = trfrag(L + base + wpo + k * 2048 + ((nt ^ (q4 >> 1)) * 64), 1024);
    };
    const float gate = *(LAS const float*)(L + L_G + (kh * 32 + r32) * 4);
    wreads(L_WOB, 0, 4);
    lds_wait(L + L_PAIR + (w ^ 1) * 4, 1);
    STAMP(ws, 0, 12);
    {
        LAS const char* EXR = L + L_EX + (w ^ 1) * 2304 + lane * 16;
        const f32x4 e0 = *(LAS const f32x4*)EXR, e1 = *(LAS const f32x4*)(EXR + 1024);
        ob[8] += *(LAS const float*)(L + L_EX + (w ^ 1) * 2304 + 2048 + lane * 4);
#pragma unroll
        for (int i = 0; i < 4; ++i) { ob[i] += e0[i]; ob[4 + i] += e1[i]; }
        float t = fmx(ob[8] * 1.0e19f, __builtin_amdgcn_rcpf(ob[8]) * 1.0e19f);
#pragma unroll
        for (int i = 0; i < 8; i += 2) t = fmx(fmx(t, __builtin_fabsf(ob[i])), __builtin_fabsf(ob[i + 1]));
        if (__builtin_expect(__any(!(t < 1.0e38f)), 0)) {
            ob = f32x16{};
            float m = -INFINITY;
            for (int t2 = 0; t2 < 64; ++t2) {
                const unsigned so = kvb + (t2 >> 1) * BLK, fl = (t2 & 1) * 2048, flv = r32 < 16 ? fl : 0;
                const bf16x8 kf = ld16(rs, vK ^ fl, so), v0 = ld16(rs, vV0 ^ flv, so), v1 = ld16(rs, vV1 ^ flv, so);
                f32x16 s = MFMA(kf, qb, f32x16{});
                const float mn = fmx(max16(s), m), al = EXP2(m - mn);
                m = mn;
#pragma unroll
                for (int i = 0; i < 16; ++i) { ob[i] *= al; s[i] = EXP2(s[i] - mn); }
                ob = MFMA(v0, pack8(s, 0), ob);
                ob = MFMA(v1, pack8(s, 1), ob);
            }
        }
        const float rl = gate * __builtin_amdgcn_rcpf(ob[8]);
#pragma unroll
        for (int i = 0; i < 8; ++i) ob[i] *= rl;
        st16(rs, __builtin_bit_cast(bf16x8, __builtin_elementwise_min(__builtin_bit_cast(u32x4, pack8(ob, 0)), (u32x4){0xFFFFFFFEu, 0xFFFFFFFEu, 0xFFFFFFFEu, 0xFFFFFFFEu})),
             lane * 16, WS_O + ((2 * tl + kh) * 32 + head) * 1024);
        SBAR();
    }
    STAMP(ws, 0, 6);
    f32x16 acc[2];
    {
        const unsigned oao = WS_O + ((2 * otl + ot) * 32 + 8 * kq) * 1024;
        bf16x8 of[8];
#pragma unroll
        for (int k = 0; k < 8; ++k) of[k] = ld16(rs, lane * 16 + (k & 3) * 1024, oao + (k & 4) * 1024);
        SBAR();
#pragma unroll
        for (int k = 0; k < 8; ++k) {
#pragma unroll
            for (int nt = 0; nt < 2; ++nt) acc[nt] = MFMA(wv[k][nt], k < 4 ? kr[k & 3] : v0r[k & 3], k ? acc[nt] : f32x16{});
            SBAR();
            if (k < 4) wreads(L_WOB, k + 4, k + 5); else wreads(L_WOA, k - 4, k - 3);
            SBAR();
        }
        wreads(L_WOA, 4, 8);
        lds_arrive(L + L_PAIR + w * 4, lane);
        SBAR();
#pragma unroll
        for (int g = 0; g < 8; g += GRP) {
#pragma unroll 1
            for (unsigned spins = 0; spins < SPIN_CAP; ++spins) {
                u32x4 v = __builtin_bit_cast(u32x4, of[g]);
#pragma unroll
                for (int k = g + 1; k < g + GRP; ++k) v = __builtin_elementwise_max(v, __builtin_bit_cast(u32x4, of[k]));
                if (!__any(__builtin_elementwise_max(__builtin_elementwise_max(v[0], v[1]), __builtin_elementwise_max(v[2], v[3])) == 0xFFFFFFFFu)) break;
                __builtin_amdgcn_s_sleep(RETRY_SLEEP);
                asm volatile("" ::: "memory");
#pragma unroll
                for (int k = g; k < 8; ++k) if (k < g + GRP || spins == 0 || RELOAD_ALL) of[k] = ld16(rs, lane * 16 + (k & 3) * 1024, oao + (k & 4) * 1024);
            }
#pragma unroll
            for (int k = g; k < g + GRP; ++k) { acc[0] = MFMA(wv[k][0], of[k], acc[0]); acc[1] = MFMA(wv[k][1], of[k], acc[1]); }
        }
        STAMP(ws, 0, 7);
    }
    lds_wait(L + L_PAIR + (w ^ 1) * 4, 2);
    LAS char* PR = L + L_WOB + kq * 16384 + ot * 8192;
#pragma unroll
    for (int z = 0; z < 4; ++z)
#pragma unroll
        for (int g = 0; g < 2; ++g) {
            const int b = 8 * (z & 1) + 4 * g;
            *(LAS f32x4*)(PR + (z * 2 + g) * 1024 + lane * 16) = (f32x4){acc[z >> 1][b], acc[z >> 1][b + 1], acc[z >> 1][b + 2], acc[z >> 1][b + 3]};
        }
    STAMP(ws, 0, 8);
    BAR();
    {
        f32x4 r0 = {}, r1 = {};
#pragma unroll
        for (int z = 0; z < 4; ++z) {
            LAS const char* src = L + L_WOB + z * 16384 + ot * 8192 + kq * 2048 + lane * 16;
            r0 += *(LAS const f32x4*)src; r1 += *(LAS const f32x4*)(src + 1024);
        }
        unsigned a0 = pk2(r0[0], r0[1]), a1 = pk2(r0[2], r0[3]), b0 = pk2(r1[0], r1[1]), b1 = pk2(r1[2], r1[3]);
        auto s0_ = __builtin_amdgcn_permlane32_swap(a0, b0, false, false);
        auto s1_ = __builtin_amdgcn_permlane32_swap(a1, b1, false, false);
        *(u32x4*)(out + (size_t)(64 * otl + 32 * ot + r32) * 512 + 64 * ocg + 16 * kq + 8 * h) = (u32x4){s0_[0], s1_[0], s0_[1], s1_[1]};
    }
    if (blockIdx.x == 0 && tid == 0) set_flag(ws, WS_EP, tag);
    STAMP(ws, 0, 9);
    TS_DUMP(ws);
}

extern "C" void kernel_launch(void* const* d_in, const int* in_sizes, int n_in, void* d_out, int out_size, void* d_ws, size_t ws_size, hipStream_t stream) {
    fused_kernel<<<256, 768, 0, stream>>>((const uint16_t*)d_in[0], (const uint16_t*)d_in[1], (const uint16_t*)d_in[4], (const uint16_t*)d_in[5], (const uint16_t*)d_in[7],
                                          (const uint16_t*)d_in[2], (const uint16_t*)d_in[6], (uint16_t*)d_out, (char*)d_ws + (128u << 20));
}
```

```cpp
#include <hip/hip_runtime.h>
#include <hip/hip_bf16.h>
#include <stdint.h>
#define HTS_DUMP(ws)
#define STAMP(ws, k, i)
#define TS_DECL
#define TS(i)
#define TS_DUMP(ws)

typedef short bf16x8 __attribute__((ext_vector_type(8)));
typedef short s16x4 __attribute__((ext_vector_type(4)));
typedef float f32x16 __attribute__((ext_vector_type(16)));
typedef float f32x4 __attribute__((ext_vector_type(4)));
typedef float f32x2 __attribute__((ext_vector_type(2)));
typedef unsigned u32x4 __attribute__((ext_vector_type(4)));
typedef unsigned u32x8 __attribute__((ext_vector_type(8)));
typedef unsigned u32x2 __attribute__((ext_vector_type(2)));
typedef __bf16 bf16x2_t __attribute__((ext_vector_type(2)));
typedef unsigned long long u64;
#define LAS __attribute__((address_space(3)))
#define GAS __attribute__((address_space(1)))
#define DI __device__ __forceinline__
#define MFMA(a, b, c) __builtin_amdgcn_mfma_f32_32x32x16_bf16((a), (b), (c), 0, 0, 0)
typedef __bf16 bfx8 __attribute__((ext_vector_type(8)));
typedef __bf16 bfx16 __attribute__((ext_vector_type(16)));
#define SM32(a, b, c, i) __builtin_amdgcn_smfmac_f32_32x32x32_bf16(__builtin_bit_cast(bfx8, a), __builtin_bit_cast(bfx16, b), (c), (i), 0, 0)
#define SM16(a, b, c, i) __builtin_amdgcn_smfmac_f32_16x16x64_bf16(__builtin_bit_cast(bfx8, a), __builtin_bit_cast(bfx16, b), (c), (i), 0, 0)
#define SBAR() __builtin_amdgcn_sched_barrier(0)
#define RLX_AGENT __ATOMIC_RELAXED, __HIP_MEMORY_SCOPE_AGENT
#define EXP2(x) __builtin_amdgcn_exp2f(x)

constexpr float LOG2E = 1.4426950408889634f;
constexpr unsigned BLK = 16384;
constexpr unsigned WS_KV = 0;
constexpr unsigned WS_O = WS_KV + 256 * BLK;
constexpr unsigned WS_PK = WS_O + (4u << 20);
constexpr unsigned WS_KVF = WS_PK + 32 * 32 * 2176;
constexpr unsigned WS_F1 = WS_KVF + 32 * 2176;
constexpr unsigned WS_F2 = WS_F1 + 2048;
constexpr unsigned WS_F3 = WS_F2 + 8192;
constexpr unsigned WS_F5 = WS_F3 + 8192;
constexpr unsigned WS_EP = WS_F5 + 8192;
constexpr unsigned WS_END = WS_EP + 128;
constexpr int L_X = 0, L_RING = 65536;
constexpr int L_PHK = 65536, L_PHQ = 81920, L_KA = 0, L_VA = 8192, L_VB = 16384;
constexpr int L_WOA = 0, L_WOB = 65536;
constexpr int L_WPHI = L_RING + 3 * 16384;
constexpr int L_G = 131072, L_SYNC = L_G + 256, L_SEEN = L_SYNC, L_STORED = L_SYNC + 4, L_HCNT = L_SYNC + 8, L_OBS = L_SYNC + 12, L_PAIR = L_SYNC + 16, L_DUMMY = L_SYNC + 256;
constexpr int L_QA = L_G + 1024, L_EX = L_QA + 8192;
constexpr int LDS_BYTES = 163840;
#define KVL_AUX 16
#define RETRY_SLEEP 2
#define WO_T0 4500
#define WO_DT 1700
#define PK_DELAY 24
#define KV_AUX 16
#define YPRIO 1
#define F1_STEP 14
#define PRIO_SWITCH 25
constexpr unsigned SPIN_CAP = 1u << 20;

template <int N> struct IC { static constexpr int value = N; };
template <int I, int N, class F> DI void static_for(F&& f) { if constexpr (I < N) { f(IC<I>{}); static_for<I + 1, N>(f); } }
DI unsigned pk2(float a, float b) { f32x2 v = {a, b}; return __builtin_bit_cast(unsigned, __builtin_convertvector(v, bf16x2_t)); }
DI bf16x8 pack8(const f32x16& x, int s) {
    u32x4 p = {pk2(x[8 * s], x[8 * s + 1]), pk2(x[8 * s + 2], x[8 * s + 3]), pk2(x[8 * s + 4], x[8 * s + 5]), pk2(x[8 * s + 6], x[8 * s + 7])};
    return __builtin_bit_cast(bf16x8, p);
}
DI float fmx(float a, float b) { return __builtin_elementwise_maximum(a, b); }
DI float max32(float v) { auto r = __builtin_amdgcn_permlane32_swap(__float_as_uint(v), __float_as_uint(v), false, false); return fmx(__uint_as_float(r[0]), __uint_as_float(r[1])); }
DI float lmax16(const f32x16& s) {
    const float a = fmx(fmx(s[0], s[1]), s[2]), b = fmx(fmx(s[3], s[4]), s[5]), c = fmx(fmx(s[6], s[7]), s[8]), d = fmx(fmx(s[9], s[10]), s[11]), e = fmx(fmx(s[12], s[13]), s[14]);
    return fmx(fmx(fmx(a, b), c), fmx(fmx(d, e), s[15]));
}
DI float max16(const f32x16& s) { return max32(lmax16(s)); }
DI void glds2(unsigned v0, unsigned v1, const void* sbase, unsigned ldsdst) {
    unsigned keep;
    asm volatile("s_mov_b32 %0, m0\n\ts_mov_b32 m0, %4\n\ts_nop 0\n\tglobal_load_lds_dwordx4 %1, %3\n\tglobal_load_lds_dwordx4 %2, %3 offset:1024\n\ts_mov_b32 m0, %0"
                 : "=&s"(keep) : "v"(v0), "v"(v1), "s"(sbase), "s"(ldsdst) : "memory");
}
DI void glds4(const unsigned (&v)[4], const void* sbase, unsigned ldsdst) {
    unsigned keep;
    asm volatile("s_mov_b32 %0, m0\n\ts_mov_b32 m0, %6\n\ts_nop 0\n\tglobal_load_lds_dwordx4 %1, %5\n\tglobal_load_lds_dwordx4 %2, %5 offset:1024\n\t"
                 "global_load_lds_dwordx4 %3, %5 offset:2048\n\tglobal_load_lds_dwordx4 %4, %5 offset:3072\n\ts_mov_b32 m0, %0"
                 : "=&s"(keep) : "v"(v[0]), "v"(v[1]), "v"(v[2]), "v"(v[3]), "s"(sbase), "s"(ldsdst) : "memory");
}
DI void touch(unsigned voff, const void* sbase, unsigned ldsdst) {
    unsigned keep;
    asm volatile("s_mov_b32 %0, m0\n\ts_mov_b32 m0, %3\n\ts_nop 0\n\tglobal_load_lds_dword %1, %2 sc1\n\ts_mov_b32 m0, %0"
                 : "=&s"(keep) : "v"(voff), "s"(sbase), "s"(ldsdst) : "memory");
}
typedef short v4i16_t __attribute__((ext_vector_type(4)));
DI s16x4 trread(LAS const char* p) { return __builtin_bit_cast(s16x4, __builtin_amdgcn_ds_read_tr16_b64_v4i16((LAS v4i16_t*)p)); }
DI bf16x8 trfrag(LAS const char* p, int d2) { s16x4 lo = trread(p), hi = trread(p + d2); return __builtin_shufflevector(lo, hi, 0, 1, 2, 3, 4, 5, 6, 7); }
DI bf16x8 ldsfrag(LAS const char* p) { return *(LAS const bf16x8*)p; }
#define BAR() asm volatile("s_waitcnt lgkmcnt(0)\n\ts_barrier" ::: "memory")
#define VM_BAR(N) asm volatile("s_waitcnt vmcnt(" #N ") lgkmcnt(0)\n\ts_barrier" ::: "memory")
#define DRAIN() asm volatile("s_waitcnt vmcnt(0)" ::: "memory")
typedef __amdgpu_buffer_rsrc_t rsrc_t;
DI bf16x8 ld16(rsrc_t r, unsigned voff, unsigned soff) { return __builtin_bit_cast(bf16x8, __builtin_amdgcn_raw_buffer_load_b128(r, voff, soff, 16)); }
DI bf16x8 ldkv(rsrc_t r, unsigned voff, unsigned soff) { return __builtin_bit_cast(bf16x8, __builtin_amdgcn_raw_buffer_load_b128(r, voff, soff, KVL_AUX)); }
DI void st16(rsrc_t r, bf16x8 v, unsigned voff, unsigned soff) { __builtin_amdgcn_raw_buffer_store_b128(__builtin_bit_cast(u32x4, v), r, voff, soff, 16); }
DI void st16kv(rsrc_t r, bf16x8 v, unsigned voff, unsigned soff) { __builtin_amdgcn_raw_buffer_store_b128(__builtin_bit_cast(u32x4, v), r, voff, soff, KV_AUX); }
DI unsigned lds_word(LAS char* p) { return __builtin_amdgcn_readfirstlane(*(LAS volatile unsigned*)p); }
DI void lds_wait(LAS char* p, unsigned want) {
#pragma unroll 1
    for (unsigned spins = 0; spins < (SPIN_CAP << 6) && lds_word(p) < want; ++spins) __builtin_amdgcn_s_sleep(1);
    asm volatile("" ::: "memory");
}
DI void lds_arrive(LAS char* p, int lane) {
    asm volatile("s_waitcnt lgkmcnt(0)" ::: "memory");
    if (lane == 0) __hip_atomic_fetch_add((LAS unsigned*)p, 1u, __ATOMIC_RELAXED, __HIP_MEMORY_SCOPE_WORKGROUP);
}
DI void set_flag(char* ws, unsigned off, u64 tag) { __hip_atomic_store((GAS u64*)(ws + off), tag, RLX_AGENT); }
template <int FIRST, int GAP> DI void wait_flags(char* ws, unsigned off, int n, u64 tag, int lane) {
    GAS u64* p = (GAS u64*)(ws + off) + (lane < n ? lane : 0);
    if (FIRST) __builtin_amdgcn_s_sleep(FIRST);
#pragma unroll 1
    for (unsigned spins = 0; spins < SPIN_CAP; ++spins) {
        const u64 v = __hip_atomic_load(p, RLX_AGENT);
        if (__all(v == tag)) break;
        __builtin_amdgcn_s_sleep(GAP);
    }
    __builtin_amdgcn_fence(__ATOMIC_ACQUIRE, "wavefront");
    asm volatile("" ::: "memory");
}

DI void softmax16(const f32x16& p, bf16x8 (&out)[2]) {
#define KEEP1(x) asm("" : "+v"(x))
    const float nm = -lmax16(p) * LOG2E;
    float e[16], t[8];
#pragma unroll
    for (int i = 0; i < 16; ++i) { e[i] = __builtin_fmaf(p[i], LOG2E, nm); KEEP1(e[i]); e[i] = EXP2(e[i]); }
#pragma unroll
    for (int i = 0; i < 8; ++i) { t[i] = e[2 * i] + e[2 * i + 1]; KEEP1(t[i]); }
#pragma unroll
    for (int i = 0; i < 4; ++i) { t[i] = t[2 * i] + t[2 * i + 1]; KEEP1(t[i]); }
    t[0] += t[1]; KEEP1(t[0]); t[2] += t[3]; KEEP1(t[2]);
    const float rs = __builtin_amdgcn_rcpf(t[0] + t[2]);
    u32x4 u[2];
#pragma unroll
    for (int i = 0; i < 16; ++i) { e[i] *= rs; KEEP1(e[i]); }
#pragma unroll
    for (int i = 0; i < 8; ++i) u[i >> 2][i & 3] = pk2(e[2 * i], e[2 * i + 1]);
    out[0] = __builtin_bit_cast(bf16x8, u[0]); out[1] = __builtin_bit_cast(bf16x8, u[1]);
}

__global__ __launch_bounds__(768) void fused_kernel(const uint16_t* __restrict__ x, const uint16_t* __restrict__ WA, const uint16_t* __restrict__ WB,
                                                    const uint16_t* __restrict__ wphi, const uint16_t* __restrict__ gatew,
                                                    const uint16_t* __restrict__ woA, const uint16_t* __restrict__ woB,
                                                    uint16_t* __restrict__ out, char* ws) {
    __shared__ __attribute__((aligned(16))) char lds[LDS_BYTES];
    LAS char* L = (LAS char*)lds;
    u64 pc0; asm volatile("s_getpc_b64 %0" : "=s"(pc0));
    const unsigned lds0 = (unsigned)(uintptr_t)lds;
    const int tid = threadIdx.x, lane = tid & 63, w = __builtin_amdgcn_readfirstlane(tid >> 6);
    const int r32 = lane & 31, h = lane >> 5;
    const int xcd = blockIdx.x & 7, idx = blockIdx.x >> 3;
    const int qd = 2 * (xcd & 3) + (idx & 1), tl = 2 * (idx >> 1) + (xcd >> 2);
    const int s0 = ((tl >> 1) + 4 * (qd & 1)) & 7;
    const int otl = 4 * xcd + (idx >> 3), ocg = idx & 7;
    const rsrc_t rs = __builtin_amdgcn_make_buffer_rsrc(ws, 0, WS_END, 0x00020000);
    u64 tag;
#define EPOCH_BAR(N) do { asm volatile("s_load_dwordx2 %0, %1, 0x0\n\ts_waitcnt vmcnt(" #N ") lgkmcnt(0)\n\ts_barrier" : "=s"(tag) : "s"(ws + WS_EP) : "memory"); tag += 1; } while (0)
    const int q4 = (lane & 15) >> 2;
    const int trl = ((lane >> 4) & 1) * 32 + (lane & 3) * 8;
    STAMP(ws, 0, 0);
    TS_DECL; TS(0); TS(16);

    asm volatile("" :: "s"(x), "s"(WA), "s"(WB), "s"(wphi), "s"(gatew), "s"(ws));
    if (__builtin_expect(w >= 8, 1)) {
        const int j = w - 8;
        const unsigned vsw = (unsigned)(((lane & 7) ^ (((lane >> 4) & 1) << 2)) << 4);
        unsigned vw[4];
#pragma unroll
        for (int u = 0; u < 4; ++u) vw[u] = (unsigned)((lane >> 3) * 3072) + vsw + u * (8 * 3072 - 1024);
        const unsigned vx0 = (unsigned)((lane >> 3) * 1024 + (((lane & 7) ^ (lane >> 4)) << 4)), vx1 = (vx0 ^ 64u) + 8192 - 1024;
        const char* xb = (const char*)x + (size_t)(64 * tl + 16 * j) * 1024;
        const size_t wo_ = (size_t)((j & 1) * 32) * 3072 + qd * 128;
        const char* wb[3] = {(const char*)WA + wo_ + 1024 + (j >> 1) * 1024, (const char*)WB + wo_ + (j >> 1) * 1024, (j >> 1) ? (const char*)WB + wo_ + 2048 : (const char*)WA + wo_};
        auto issue_x = [&](int i) { const int kc = (s0 + i) & 7; glds2(vx0, vx1, xb + kc * 128, lds0 + L_X + i * 8192 + j * 2048); };
        auto issue_w = [&](int i) { glds4(vw, wb[i >> 3] + ((s0 + i) & 7) * (64 * 3072), lds0 + L_RING + ((i + 4) % 6) * 16384 + j * 4096); };
        __builtin_amdgcn_s_setprio(3);
        TS(4);
        issue_w(0); issue_x(0); issue_x(1); issue_x(2); issue_x(3);
        bf16x8 gw; unsigned warm;
        asm volatile("global_load_dwordx4 %0, %1, %2" : "=v"(gw) : "v"((lane & 15) * 64 + (lane >> 4) * 16), "s"(gatew) : "memory");
        glds2(lane * 16, lane * 16, wphi + qd * 1024, lds0 + L_WPHI);
        issue_w(1);
        TS(5);
        VM_BAR(13); issue_w(2); issue_w(3);
        VM_BAR(8); issue_x(4); issue_w(4);
        EPOCH_BAR(10); issue_x(5); issue_w(5);
        VM_BAR(12); issue_x(6); issue_w(6);
        VM_BAR(12); issue_x(7); issue_w(7);
        VM_BAR(12); issue_w(8); issue_w(9);
        asm volatile("global_load_dword %0, %1, %2" : "=v"(warm) : "v"(((tl >> 1) * 32 + (lane & 15)) * 3072), "s"(wb[2] - (j & 1) * 16 * 3072) : "memory");
        VM_BAR(15);
        VM_BAR(9); issue_w(10); issue_w(11);
        f32x4 gl = {};
        const int grow = 16 * j + (lane & 15);
        int gxa = L_X + grow * 128 + (((lane >> 4) ^ ((grow >> 1) & 7)) << 4);
        u64 f1early = 0;
#pragma unroll
        for (int i = 8; i < 24; i += 2) {
            if (i < 22) VM_BAR(8); else VM_BAR(0);
            if (i == 8) asm volatile("" : "+v"(gw), "+v"(warm));
            bf16x8 xg[4], ga[4];
            if (i < 16) {
#pragma unroll
                for (int u = 0; u < 4; ++u) {
                    xg[u] = ldsfrag(L + (gxa ^ ((u & 1) << 6)) + (i - 8 + (u >> 1)) * 8192);
                    ga[u] = (lane & 15) == 2 * ((s0 + i + (u >> 1)) & 7) + (u & 1) ? gw : bf16x8{};
                }
            }
            SBAR();
            if (i < 20) { issue_w(i + 4); issue_w(i + 5); }
            SBAR();
            if (i < 16) {
#pragma unroll
                for (int u = 0; u < 4; ++u) gl = __builtin_amdgcn_mfma_f32_16x16x32_bf16(ga[u], xg[u], gl, 0, 0, 0);
            }
            if (j == 0 && i == 20) {
                f1early = __hip_atomic_load((GAS u64*)(ws + WS_F1 + qd * 256) + (lane & 31), RLX_AGENT);
                const bool seen = __all(f1early == tag);
                if (lane < 16) *(LAS unsigned*)(L + L_SYNC + lane * 4) = (lane == 0 && seen) ? 1u : 0u;
            }
            if (j == 0 && i == 22 && !__all(f1early == tag)) {
                TS(1);
                wait_flags<0, 2>(ws, WS_F1 + qd * 256, 32, tag, lane);
                *(LAS volatile unsigned*)(L + L_SEEN) = 1;
                TS(2);
            }
            SBAR();
        }
        {
            float z = (gl[0] + gl[1]) + (gl[2] + gl[3]);
            z += __shfl_xor(z, 16); z += __shfl_xor(z, 32);
            if (lane < 16) *(LAS float*)(L + L_G + grow * 4) = __builtin_amdgcn_rcpf(1.f + EXP2(-z * LOG2E));
        }
        BAR();
        TS(3);
        const u64 tp2 = __builtin_amdgcn_s_memtime();
        __builtin_amdgcn_s_sleep(6);
        const int head = 4 * qd + j;
        bf16x8 phq[2][2]; float g2[2];
        {
            f32x16 kv = {};
            const bool keep = (r32 >> 4) == (j & 1);
            bf16x8 pf[2][2], ve[2][2];
#pragma unroll
            for (int tt = 0; tt < 2; ++tt) {
#pragma unroll
                for (int s = 0; s < 2; ++s) {
                    { LAS const char* pp = L + L_PHK + (j * 2 + tt) * 2048 + (16 * s + 4 * h + q4) * 64; const int sw = trl ^ ((2 * h + (q4 >> 1)) * 8);
                      pf[tt][s] = __builtin_shufflevector(trread(pp + sw), trread(pp + 512 + (sw ^ 32)), 0, 1, 2, 3, 4, 5, 6, 7); }
                    ve[tt][s] = ldsfrag(L + L_VB + (((j >> 1) * 2 + tt) * 2 + s) * 1024 + lane * 16);
                    phq[tt][s] = ldsfrag(L + L_PHQ + ((j * 2 + tt) * 2 + s) * 1024 + lane * 16);
                }
                g2[tt] = 1.f - *(LAS const float*)(L + L_G + (tt * 32 + r32) * 4);
            }
            lds_arrive(L + L_HCNT, lane);
            __builtin_amdgcn_s_sleep(PK_DELAY);
#pragma unroll
            for (int tt = 0; tt < 2; ++tt)
#pragma unroll
                for (int s = 0; s < 2; ++s) {
#pragma unroll
                    for (int e = 0; e < 8; ++e) ve[tt][s][e] = keep ? ve[tt][s][e] : (short)0x3F80;
                    kv = MFMA(pf[tt][s], ve[tt][s], kv);
                }
            const int col = keep ? (r32 & 15) : 16;
            const unsigned vo = (keep || (r32 & 15) == 0) ? (unsigned)((col * 2 + h) * 16) : 0x40000000u;
#pragma unroll
            for (int g = 0; g < 4; ++g) st16(rs, __builtin_bit_cast(bf16x8, (f32x4){kv[4 * g], kv[4 * g + 1], kv[4 * g + 2], kv[4 * g + 3]}), vo + g * 544, WS_PK + ((qd * 32 + tl) * 4 + j) * 2176);
            DRAIN();
            if (lane == 0) set_flag(ws, WS_F2 + (head * 32 + tl) * 8, tag);
        }
        unsigned vo[4];
#pragma unroll
        for (int u = 0; u < 4; ++u) vo[u] = (unsigned)((lane >> 3) * 1024) + vsw + u * (8192 - 1024);
        lds_wait(L + L_HCNT, 4);
        int wo_done = 0;
        auto wo_service = [&]() {
            if (wo_done < 2 && __builtin_amdgcn_s_memtime() - tp2 >= (u64)(WO_T0 + WO_DT * (j + 4 * wo_done))) {
                const char* src = (const char*)(wo_done ? woA : woB) + (size_t)(128 * j) * 1024 + ocg * 128;
#pragma unroll
                for (int i = 0; i < 4; ++i) glds4(vo, src + i * 32768, lds0 + (wo_done ? L_WOA : L_WOB) + j * 16384 + i * 4096);
                ++wo_done;
            }
        };
        auto wait_slow = [&](unsigned off, int n) {
            GAS u64* p = (GAS u64*)(ws + off) + (lane < n ? lane : 0);
#pragma unroll 1
            for (unsigned spins = 0; spins < SPIN_CAP; ++spins) {
#pragma unroll 1
                for (int z = 0; z < (spins ? 2 : 6); ++z) { wo_service(); __builtin_amdgcn_s_sleep(6); }
                if (__all(__hip_atomic_load(p, RLX_AGENT) == tag)) break;
            }
            __builtin_amdgcn_fence(__ATOMIC_ACQUIRE, "wavefront");
            asm volatile("" ::: "memory");
        };
        STAMP(ws, 0, 1);
        if (tl < 17) {
            wait_slow(WS_F2 + head * 256, 32);
            const unsigned so = WS_PK + (qd * 128 + j) * 2176 + tl * 128, v1 = (unsigned)((lane >> 3) * 8704 + (lane & 7) * 16);
            f32x4 a = (__builtin_bit_cast(f32x4, ld16(rs, v1, so)) + __builtin_bit_cast(f32x4, ld16(rs, v1, so + 8 * 8704))) +
                      (__builtin_bit_cast(f32x4, ld16(rs, v1, so + 16 * 8704)) + __builtin_bit_cast(f32x4, ld16(rs, v1, so + 24 * 8704)));
#pragma unroll
            for (int o = 8; o < 64; o <<= 1) {
#pragma unroll
                for (int e = 0; e < 4; ++e) a[e] += __shfl_xor(a[e], o);
            }
            st16(rs, __builtin_bit_cast(bf16x8, a), lane < 8 ? (unsigned)(lane * 16) : 0x40000000u, WS_KVF + head * 2176 + tl * 128);
            DRAIN();
            if (lane == 0) set_flag(ws, WS_F3 + (head * 32 + tl) * 8, tag);
        } else {
#pragma unroll 1
            for (int z = 0; z < 10; ++z) { wo_service(); __builtin_amdgcn_s_sleep(6); }
        }
        STAMP(ws, 0, 2);
        wait_slow(WS_F3 + head * 256, 17);
        {
            const unsigned v2 = (unsigned)(((r32 < 16 ? r32 : 16) * 2 + h) * 16);
            f32x16 kv;
#pragma unroll
            for (int g = 0; g < 4; ++g) { const f32x4 t = __builtin_bit_cast(f32x4, ld16(rs, v2 + g * 544, WS_KVF + head * 2176)); kv[4 * g] = t[0]; kv[4 * g + 1] = t[1]; kv[4 * g + 2] = t[2]; kv[4 * g + 3] = t[3]; }
            const bf16x8 k0 = pack8(kv, 0), k1 = pack8(kv, 1);
#pragma unroll
            for (int tt = 0; tt < 2; ++tt) {
                f32x16 ob = MFMA(k0, phq[tt][0], f32x16{});
                ob = MFMA(k1, phq[tt][1], ob);
                const float rd = g2[tt] * __builtin_amdgcn_rcpf(ob[8] + 1e-6f);
#pragma unroll
                for (int i = 0; i < 8; ++i) ob[i] *= rd;
                st16(rs, pack8(ob, 0), lane * 16, WS_O + (2u << 20) + ((2 * tl + tt) * 32 + head) * 1024);
                st16(rs, (bf16x8){-1, -1, -1, -1, -1, -1, -1, -1}, lane * 16, WS_O + ((2 * tl + tt) * 32 + head) * 1024);
            }
            DRAIN();
            if (lane == 0) set_flag(ws, WS_F5 + (tl * 32 + head) * 8, tag);
        }
        STAMP(ws, 0, 3);
        wait_slow(WS_F5 + (otl * 32 + 8 * j) * 8, 8);
        if (lane < 16) touch((lane >> 3) * 32768 + (lane & 7) * 128, ws + WS_O + (2u << 20) + (2 * otl * 32 + 8 * j + ocg) * 1024, lds0 + L_DUMMY);
        while (wo_done < 2) { wo_service(); __builtin_amdgcn_s_sleep(6); }
        DRAIN();
        lds_arrive(L + L_OBS, lane);
        STAMP(ws, 0, 5);
        HTS_DUMP(ws);
        return;
    }

    if (w == 0) {
        const char* a = (const char*)(pc0 & ~127ull) + (idx * 9 + lane) * 128;
        if (lane == 9) {
            a = ws + (size_t)(idx < 21 ? idx : 0) * 524288;
            if (idx >= 22) a = idx < 25 ? (const char*)WB + (idx - 22) * 786368 : idx < 28 ? (const char*)out + (idx - 25) * 1048512 : idx < 30 ? (const char*)woA + (idx - 28) * 524224 : (const char*)woB + (idx - 30) * 524224;
        }
        unsigned keep;
        if (lane < 10) asm volatile("s_mov_b32 %0, m0\n\ts_mov_b32 m0, %2\n\ts_nop 0\n\tglobal_load_lds_dword %1, off\n\ts_mov_b32 m0, %0" : "=&s"(keep) : "v"(a), "s"(lds0 + L_WPHI + 4096) : "memory");
    }
    const int p = w & 1;
    const int hq = w >> 1, kh = w & 1, head = 4 * qd + hq;
    const unsigned vK = w * 2048 + lane * 16;
    const unsigned kvb = WS_KV + qd * 32 * BLK;
#define KVOFF(t) (kvb + (((t) + tl) & 31) * BLK)
    bf16x8 kr[4], vr[4];
    const int kpos = ((lane & 0x31) | ((lane & 2) << 2) | ((lane & 12) >> 1)) * 16;
    bf16x8 kref;
    {
        const int xa0 = L_X + r32 * 128 + ((h ^ ((r32 >> 1) & 7)) << 4);
        const int wa0 = L_RING + ((w >> 1) & 1) * 8192 + (8 * h + q4) * 128 + ((p ^ (q4 >> 1)) * 64) + trl;
        bf16x8 xf[2][4][2], w0[2][4];
        f32x16 acc[2];
        int wsb[3], xsb[4];
#pragma unroll
        for (int i = 0; i < 3; ++i) { wsb[i] = wa0 + i * 32768; asm volatile("" : "+v"(wsb[i])); }
#pragma unroll
        for (int s = 0; s < 4; ++s) { xsb[s] = xa0 ^ (s << 5); asm volatile("" : "+v"(xsb[s])); }
        auto gemm = [&](int TEAM, int TYPE) __attribute__((always_inline)) {
            auto mine = [&](int i) __attribute__((always_inline)) { return i >= 0 && i < 24 && ((i >> 3) & 1) == TEAM; };
            auto rx = [&](int i, int s, int t) __attribute__((always_inline)) { if (mine(i)) xf[i & 1][s][t] = ldsfrag(L + xsb[s] + (i & 7) * 8192 + t * 4096); };
            auto rw = [&](int i, int s) __attribute__((always_inline)) { if (mine(i)) w0[i & 1][s] = trfrag(L + wsb[((i + 4) % 6) >> 1] + (i & 1) * 16384 + s * 2048, 512); };
            auto mm = [&](int i, int s, int t) __attribute__((always_inline)) {
                SBAR();
                if (mine(i)) {
                    const f32x16 c = ((i & 7) == 0 && s == 0) ? f32x16{} : acc[t];
                    acc[t] = (TEAM == 0 && TYPE) ? MFMA(xf[i & 1][s][t], w0[i & 1][s], c) : MFMA(w0[i & 1][s], xf[i & 1][s][t], c);
                }
                SBAR();
            };
            auto first_loads = [&](int t) __attribute__((always_inline)) {
                kr[t] = ldkv(rs, vK, KVOFF(t)); vr[t] = ldkv(rs, vK + 1024, KVOFF(t));
                SBAR();
            };
            bf16x8 own[2][2], wf[2], ph[4][2];
            f32x16 f[4];
            static_for<0, 25>([&](auto ic) __attribute__((always_inline)) {
                constexpr int i = ic.value;
                if (i == F1_STEP) { VM_BAR(0); if (tid == 0) set_flag(ws, WS_F1 + (qd * 32 + tl) * 8, tag); }
                else if (i == 0) EPOCH_BAR(63);
                else if (i == 2) { __builtin_amdgcn_s_waitcnt(0x0F70); BAR(); }
                else if (i < 8 || (i < 24 && !(i & 1))) BAR();
                if (i == 0 || i == 1 || i == 8) TS(i + 1); else if (i == 22) TS(17);
                mm(i - 1, 0, 0); rw(i, 0); rx(i, 0, 0);
                mm(i - 1, 0, 1); rx(i, 0, 1); rw(i, 1);
                mm(i - 1, 1, 0); rx(i, 1, 0); rx(i, 1, 1);
                mm(i - 1, 1, 1); rw(i, 2); rx(i, 2, 0);
                mm(i - 1, 2, 0); rx(i, 2, 1); rw(i, 3);
                mm(i - 1, 2, 1); rx(i, 3, 0); rx(i, 3, 1);
                mm(i - 1, 3, 0);
                if (TEAM == 0 && (i == 18 || i == 19)) {
#pragma unroll
                    for (int a = 0; a < 2; ++a)
                        *(LAS bf16x8*)(L + (TYPE ? L_VA + ((p * 2 + (i & 1)) * 2 + a) * 1024 + lane * 16 : L_KA + ((2 * p + a) * 2 + (i & 1)) * 1024 + kpos)) = own[i & 1][a];
                }
                mm(i - 1, 3, 1);
                if (TEAM == 0 && i == 8) {
#pragma unroll
                    for (int t = 0; t < 2; ++t) {
                        if (TYPE == 0) {
#pragma unroll
                            for (int e = 0; e < 16; ++e) { float z = acc[t][e] * (0.25f * LOG2E); KEEP1(z); acc[t][e] = z; }
                        }
                        const unsigned so = kvb + tl * BLK + (4 * p + t) * 2048;
#pragma unroll
                        for (int a = 0; a < 2; ++a) {
                            own[t][a] = pack8(acc[t], a);
                            if (TYPE == 0) st16kv(rs, own[t][a], kpos, so + a * 4096);
                            else st16kv(rs, own[t][a], (unsigned)((r32 >> 4) * 4096 + 1024 + h * 256 + (r32 & 15) * 16) + a * 512, so);
                        }
                    }
                }
                if (TEAM == 1) {
                    if (i == 1) {
                        const short sg = (lane & 4) ? (short)0x8000 : (short)0;
#pragma unroll
                        for (int a = 0; a < 2; ++a) {
                            wf[a] = trfrag(L + L_WPHI + (2 * p + a) * 512 + (4 * h + q4) * 32 + (((lane >> 4) & 1) * 2 + ((lane >> 1) & 1)) * 8, 256);
#pragma unroll
                            for (int e = 0; e < 8; ++e) wf[a][e] ^= sg;
                            asm volatile("" : "+v"(wf[a]));
                        }
                    }
                    if (i == 16) {
                        __builtin_amdgcn_s_setprio(YPRIO);
#pragma unroll
                        for (int m = 0; m < 4; ++m) f[m] = MFMA(wf[m >> 1], pack8(acc[m & 1], m >> 1), f32x16{});
                    }
                    if (i == 22) { lds_wait(L + L_SEEN, 1); first_loads(1); first_loads(2); }
                    if (i >= 16 && i < 24 && !(i & 1)) { softmax16(f[(i - 16) / 2], ph[(i - 16) / 2]); asm volatile("" : "+v"(ph[(i - 16) / 2][0]), "+v"(ph[(i - 16) / 2][1])); }
                }
                if (i == 20 + (1 - TEAM) * 4) {
                    kref = ldsfrag(L + L_KA + (hq * 2) * 1024 + lane * 16);
                    kr[0] = ldsfrag(L + L_KA + (hq * 2 + kh) * 1024 + lane * 16);
                    vr[0] = ldsfrag(L + L_VA + (((hq >> 1) * 2 + kh) * 2 + h) * 1024 + ((r32 >> 4) * 32 + (hq & 1) * 16 + (r32 & 15)) * 16);
                }
                SBAR();
            });
            TS(18);
            if (TEAM == 0) {
                lds_wait(L + L_SEEN, 1); first_loads(1);
#pragma unroll
                for (int t = 0; t < 2; ++t)
#pragma unroll
                    for (int a = 0; a < 2; ++a)
                        *(LAS bf16x8*)(L + (TYPE ? L_VB + ((p * 2 + t) * 2 + a) * 1024 : L_QA + ((2 * p + a) * 2 + t) * 1024) + lane * 16) = pack8(acc[t], a);
                SBAR();
                first_loads(2);
            } else if (w < 6) {
#pragma unroll
                for (int m = 0; m < 8; ++m) *(LAS bf16x8*)(L + L_PHQ + (8 * p + m) * 1024 + lane * 16) = ph[m >> 1][m & 1];
            } else {
#pragma unroll
                for (int m = 0; m < 8; ++m) {
                    const u32x4 u = __builtin_bit_cast(u32x4, ph[m >> 1][m & 1]);
#pragma unroll
                    for (int g = 0; g < 2; ++g)
                        *(LAS u32x2*)(L + L_PHK + (4 * p + (m >> 1)) * 2048 + r32 * 64 + 8 * ((2 * (2 * (m & 1) + g) + h) ^ ((r32 >> 1) & 7))) = (u32x2){u[2 * g], u[2 * g + 1]};
                }
            }
        };
        if (w < 2) gemm(0, 0); else if (w < 4) gemm(0, 1); else gemm(1, 0);
    }
    TS(20);
    BAR();
    TS(21);
    const bf16x8 qa = ldsfrag(L + L_QA + (hq * 2 + (kh ^ 1)) * 1024 + lane * 16), qb = ldsfrag(L + L_QA + (hq * 2 + kh) * 1024 + lane * 16);
    const float m0 = lmax16(MFMA(kref, qa, f32x16{})), m1 = lmax16(MFMA(kref, qb, f32x16{}));
    TS(23);
    const int ot = w & 1, kq = w >> 1;
    f32x16 oa = {}, ob = {};
    f32x4 la = {}, lb = {};
    bf16x8 ofb[8];
    const int ixv = (lane & 16) ? 0xEEEE : 0x4444, ixs = (lane & 1) ? 0xEEEE : 0x4444;
    const unsigned one2 = (lane & 2) ? 0u : 0x3F803F80u, olo = (lane & 4) ? 0u : one2, ohi = (lane & 4) ? one2 : 0u;
    const bf16x8 ones = __builtin_bit_cast(bf16x8, (u32x4){olo, olo, ohi, ohi});
    {
        f32x16 negm;
        const float nm = -max32(fmx(m0, m1));
#pragma unroll
        for (int i = 0; i < 16; ++i) negm[i] = nm;
        f32x16 sa = MFMA(kr[0], qa, negm), sb;
        kr[3] = ldkv(rs, vK, KVOFF(3));
        TS(24);
        STAMP(ws, 0, 4);
        u32x8 pa, pb;
#define E(S, i) S[i] = EXP2(S[i])
#define C(P, S, i) P[i] = pk2(S[2 * (i)], S[2 * (i) + 1])
#define PHASE(S, P, M1, M2, M3, LD1, LD2, LD3)                                                    \
    do {                                                                                          \
        M1; LD1; SBAR();                                                                          \
        E(S, 0); E(S, 1); E(S, 2); E(S, 3); E(S, 4); SBAR();                                      \
        M2; LD2; SBAR();                                                                          \
        E(S, 5); E(S, 6); E(S, 7); E(S, 8); E(S, 9); SBAR();                                      \
        M3; LD3; SBAR();                                                                          \
        E(S, 10); E(S, 11); SBAR(); C(P, S, 0); C(P, S, 1); C(P, S, 2); C(P, S, 3); SBAR();       \
        E(S, 12); E(S, 13); E(S, 14); E(S, 15); SBAR();                                           \
        C(P, S, 4); C(P, S, 5); C(P, S, 6); C(P, S, 7); SBAR();                                   \
    } while (0)
        const unsigned obo = WS_O + (2u << 20) + ((2 * otl + ot) * 32 + 8 * kq) * 1024 - w * 2048;
        auto handoff = [&]() {
            LAS char* EXW = L + L_EX + w * 2304 + lane * 16;
            *(LAS f32x4*)EXW = (f32x4){oa[0] + oa[8], oa[1] + oa[9], oa[2] + oa[10], oa[3] + oa[11]};
            *(LAS f32x4*)(EXW + 1024) = (f32x4){oa[4] + oa[12], oa[5] + oa[13], oa[6] + oa[14], oa[7] + oa[15]};
            *(LAS float*)(L + L_EX + w * 2304 + 2048 + lane * 4) = la[0] + la[1];
            asm volatile("" ::: "memory");
            *(LAS volatile unsigned*)(L + L_PAIR + w * 4) = 1;
        };
        auto body = [&](int t0, bool FIRST, bool LAST, bool STAT) __attribute__((always_inline)) {
#pragma unroll
            for (int u = 0; u < 4; ++u) {
                const int t = t0 + u;
                const bool Z = FIRST && u == 0;
                if (STAT && t == PRIO_SWITCH && w < 4) __builtin_amdgcn_s_setprio(0);
                PHASE(sa, pa, sb = MFMA(kr[u], qb, negm), if (!Z) ob = SM32(vr[(u + 3) & 3], pb, ob, ixv), if (!Z) lb = SM16(ones, pb, lb, ixs),
                      if (LAST) ofb[u] = ldkv(rs, vK, obo + u * 1024); else kr[u] = ldkv(rs, vK, KVOFF(t + 4)),
                      if (!LAST || u == 0) vr[(u + 3) & 3] = ldkv(rs, vK + 1024, KVOFF(t + 3)); else ofb[3 + u] = ldkv(rs, vK, obo + (3 + u) * 1024), (void)0);
                PHASE(sb, pb, if (!LAST || u < 3) sa = MFMA(kr[(u + 1) & 3], qa, negm); else la = SM16(ones, pa, la, ixs), oa = SM32(vr[u], pa, oa, ixv),
                      if (!LAST || u < 3) la = SM16(ones, pa, la, ixs); else handoff(),
                      if (LAST && u == 3) ofb[7] = ldkv(rs, vK, obo + 7 * 1024), (void)0, (void)0);
            }
        };
        if (w < 4) __builtin_amdgcn_s_setprio(2); else __builtin_amdgcn_s_setprio(1);
        STAMP(ws, 3, 0);
        body(0, true, false, false);
#pragma unroll 1
        for (int t0 = 4; t0 < 24; t0 += 4) {
            STAMP(ws, 3, t0 >> 2);
            body(t0, false, false, false);
        }
        STAMP(ws, 3, 6);
        body(24, false, false, true);
        STAMP(ws, 3, 7);
        lds_wait(L + L_OBS, 4);
        body(28, false, true, true);
        STAMP(ws, 3, 8);
        ob = SM32(vr[3], pb, ob, ixv);
        lb = SM16(ones, pb, lb, ixs);
#undef PHASE
#undef E
#undef C
    }
    __builtin_amdgcn_s_setprio(0);
    STAMP(ws, 0, 5);
    int lane_ = __builtin_amdgcn_mbcnt_hi(~0u, __builtin_amdgcn_mbcnt_lo(~0u, 0u));
    asm volatile("" : "+v"(lane_));
    const int r32_ = lane_ & 31, h_ = lane_ >> 5, q4_ = (lane_ & 15) >> 2, trl_ = ((lane_ >> 4) & 1) * 32 + (lane_ & 3) * 8;
    const unsigned oao = WS_O + ((2 * otl + ot) * 32 + 8 * kq) * 1024, vp = lane_ < 8 ? (unsigned)(lane_ * 1024 + 1020) : 0x40000000u;
    auto probe = [&]() { return (unsigned)__ballot(__builtin_amdgcn_raw_buffer_load_b32(rs, vp, oao, 16) != 0xFFFFFFFFu); };
    const unsigned pr0 = __builtin_amdgcn_raw_buffer_load_b32(rs, vp, oao, 16);
    SBAR();
    const int wpo = kq * 16384 + (4 * h_ + q4_) * 128 + trl_;
    bf16x8 wv[8][2];
    auto wreads = [&](int base, int k0, int k1) {
#pragma unroll
        for (int k = k0; k < k1; ++k)
#pragma unroll
            for (int nt = 0; nt < 2; ++nt) wv[k][nt] = trfrag(L + base + wpo + k * 2048 + ((nt ^ (q4_ >> 1)) * 64), 1024);
    };
    const float gate = *(LAS const float*)(L + L_G + (kh * 32 + r32_) * 4);
    lds_wait(L + L_PAIR + (w ^ 1) * 4, 1);
    STAMP(ws, 0, 12);
    {
        LAS const char* EXR = L + L_EX + (w ^ 1) * 2304 + lane_ * 16;
        const f32x4 e0 = *(LAS const f32x4*)EXR, e1 = *(LAS const f32x4*)(EXR + 1024);
        const float e2 = *(LAS const float*)(L + L_EX + (w ^ 1) * 2304 + 2048 + lane_ * 4);
        SBAR();
        wreads(L_WOB, 0, 4);
        SBAR();
        float l = (lb[0] + lb[1]) + e2;
#pragma unroll
        for (int i = 0; i < 4; ++i) { ob[i] = (ob[i] + ob[8 + i]) + e0[i]; ob[4 + i] = (ob[4 + i] + ob[12 + i]) + e1[i]; }
        float rl = __builtin_amdgcn_rcpf(l), t = fmx(l, rl) * 1.0e19f;
#pragma unroll
        for (int i = 0; i < 8; i += 2) t = fmx(fmx(t, __builtin_fabsf(ob[i])), __builtin_fabsf(ob[i + 1]));
        if (__builtin_expect(__any(!(t < 1.0e38f)), 0)) {
            ob = f32x16{}; lb = f32x4{};
            float m = -INFINITY;
            for (int t2 = 0; t2 < 64; ++t2) {
                const unsigned so = kvb + (t2 >> 1) * BLK, vo = vK ^ ((t2 & 1) * 2048);
                const bf16x8 kf = ld16(rs, vo, so), vf = ld16(rs, vo + 1024, so);
                f32x16 s = MFMA(kf, qb, f32x16{});
                const float mn = fmx(max16(s), m), al = EXP2(m - mn);
                m = mn;
                u32x8 pp;
#pragma unroll
                for (int i = 0; i < 16; ++i) { ob[i] *= al; s[i] = EXP2(s[i] - mn); }
#pragma unroll
                for (int i = 0; i < 8; ++i) pp[i] = pk2(s[2 * i], s[2 * i + 1]);
                lb *= al;
                ob = SM32(vf, pp, ob, ixv);
                lb = SM16(ones, pp, lb, ixs);
            }
#pragma unroll
            for (int i = 0; i < 8; ++i) ob[i] += ob[8 + i];
            rl = __builtin_amdgcn_rcpf(lb[0] + lb[1]);
        }
        rl *= gate;
#pragma unroll
        for (int i = 0; i < 8; ++i) ob[i] *= rl;
        st16(rs, __builtin_bit_cast(bf16x8, __builtin_elementwise_min(__builtin_bit_cast(u32x4, pack8(ob, 0)), (u32x4){0xFFFFFFFEu, 0xFFFFFFFEu, 0xFFFFFFFEu, 0xFFFFFFFEu})),
             lane_ * 16, WS_O + ((2 * tl + kh) * 32 + head) * 1024);
        SBAR();
    }
    STAMP(ws, 0, 6);
    if (lane_ == 0) __hip_atomic_fetch_add((LAS unsigned*)(L + L_STORED), 1u, __ATOMIC_RELAXED, __HIP_MEMORY_SCOPE_WORKGROUP);
    f32x16 acc[2];
    f32x4 keep0 = {}, keep1 = {};
    {
        bf16x8 of[8];
        auto loads = [&](int g) {
#pragma unroll
            for (int k = 4 * g; k < 4 * g + 4; ++k) of[k] = ld16(rs, lane_ * 16 + (k & 3) * 1024, oao + (k & 4) * 1024);
        };
        const bf16x8 none = {-1, -1, -1, -1, -1, -1, -1, -1};
        unsigned rdy = (unsigned)__ballot(pr0 != 0xFFFFFFFFu);
#pragma unroll
        for (int g = 0; g < 2; ++g) { if (((rdy >> (4 * g)) & 15) == 15) loads(g); else of[4 * g] = none; }
        SBAR();
#pragma unroll
        for (int k = 0; k < 8; ++k) {
#pragma unroll
            for (int nt = 0; nt < 2; ++nt) acc[nt] = MFMA(wv[k][nt], ofb[k], k ? acc[nt] : f32x16{});
            SBAR();
            if (k < 4) wreads(L_WOB, k + 4, k + 5); else wreads(L_WOA, k - 4, k - 3);
            SBAR();
        }
        wreads(L_WOA, 4, 8);
        lds_arrive(L + L_PAIR + w * 4, lane_);
        SBAR();
        auto missing = [&](int g) {
            const u32x4 v = __builtin_elementwise_max(__builtin_elementwise_max(__builtin_bit_cast(u32x4, of[4 * g]), __builtin_bit_cast(u32x4, of[4 * g + 1])),
                                                      __builtin_elementwise_max(__builtin_bit_cast(u32x4, of[4 * g + 2]), __builtin_bit_cast(u32x4, of[4 * g + 3])));
            return (bool)__any(__builtin_elementwise_max(__builtin_elementwise_max(v[0], v[1]), __builtin_elementwise_max(v[2], v[3])) == 0xFFFFFFFFu);
        };
        bool have1 = ((rdy >> 4) & 15) == 15;
#pragma unroll
        for (int g = 0; g < 2; ++g) {
#pragma unroll 1
            for (unsigned spins = 0; spins < SPIN_CAP && missing(g); ++spins) {
                rdy = lds_word(L + L_STORED) >= 8 ? 0xFFu : probe();
                asm volatile("" ::: "memory");
                if (g == 0 && !have1 && ((rdy >> 4) & 15) == 15) { loads(1); have1 = true; }
                if (((rdy >> (4 * g)) & 15) == 15) loads(g); else __builtin_amdgcn_s_sleep(RETRY_SLEEP);
            }
            if (g == 0) {
#pragma unroll
                for (int k = 0; k < 4; ++k) { acc[0] = MFMA(wv[k][0], of[k], acc[0]); acc[1] = MFMA(wv[k][1], of[k], acc[1]); }
                lds_wait(L + L_PAIR + (w ^ 1) * 4, 2);
            }
        }
        LAS char* PR = L + L_WOB + kq * 16384 + ot * 8192 + lane_ * 16;
#pragma unroll
        for (int nt = 0; nt < 2; ++nt)
#pragma unroll
            for (int k = 4; k < 8; ++k) acc[nt] = MFMA(wv[k][nt], of[k], acc[nt]);
        SBAR();
        STAMP(ws, 0, 7);
#pragma unroll
        for (int z = 0; z < 4; ++z) {
            const int b = 8 * (z & 1);
            const f32x4 u0 = {acc[z >> 1][b], acc[z >> 1][b + 1], acc[z >> 1][b + 2], acc[z >> 1][b + 3]}, u1 = {acc[z >> 1][b + 4], acc[z >> 1][b + 5], acc[z >> 1][b + 6], acc[z >> 1][b + 7]};
            if (z != kq) { *(LAS f32x4*)(PR + z * 2048) = u0; *(LAS f32x4*)(PR + z * 2048 + 1024) = u1; } else { keep0 = u0; keep1 = u1; }
        }
    }
    STAMP(ws, 0, 8);
    BAR();
    {
        f32x4 r0 = keep0, r1 = keep1;
#pragma unroll
        for (int z = 1; z < 4; ++z) {
            LAS const char* src = L + L_WOB + ((kq + z) & 3) * 16384 + ot * 8192 + kq * 2048 + lane_ * 16;
            r0 += *(LAS const f32x4*)src; r1 += *(LAS const f32x4*)(src + 1024);
        }
        unsigned a0 = pk2(r0[0], r0[1]), a1 = pk2(r0[2], r0[3]), b0 = pk2(r1[0], r1[1]), b1 = pk2(r1[2], r1[3]);
        auto s0_ = __builtin_amdgcn_permlane32_swap(a0, b0, false, false);
        auto s1_ = __builtin_amdgcn_permlane32_swap(a1, b1, false, false);
        *(u32x4*)(out + (size_t)(64 * otl + 32 * ot + r32_) * 512 + 64 * ocg + 16 * kq + 8 * h_) = (u32x4){s0_[0], s1_[0], s0_[1], s1_[1]};
    }
    if (blockIdx.x == 0 && w == 0 && lane_ == 0) set_flag(ws, WS_EP, tag);
    STAMP(ws, 0, 9);
    TS_DUMP(ws);
}

extern "C" void kernel_launch(void* const* d_in, const int* in_sizes, int n_in, void* d_out, int out_size, void* d_ws, size_t ws_size, hipStream_t stream) {
    fused_kernel<<<256, 768, 0, stream>>>((const uint16_t*)d_in[0], (const uint16_t*)d_in[1], (const uint16_t*)d_in[4], (const uint16_t*)d_in[5], (const uint16_t*)d_in[7],
                                          (const uint16_t*)d_in[2], (const uint16_t*)d_in[6], (uint16_t*)d_out, (char*)d_ws);
}
```

```cpp
#include <hip/hip_runtime.h>
#include <hip/hip_bf16.h>
#include <stdint.h>
#define HTS_DUMP(ws)
#define STAMP(ws, k, i)
#define TS_DECL
#define TS(i)
#define TS_DUMP(ws)

typedef short bf16x8 __attribute__((ext_vector_type(8)));
typedef short s16x4 __attribute__((ext_vector_type(4)));
typedef float f32x16 __attribute__((ext_vector_type(16)));
typedef float f32x4 __attribute__((ext_vector_type(4)));
typedef float f32x2 __attribute__((ext_vector_type(2)));
typedef unsigned u32x4 __attribute__((ext_vector_type(4)));
typedef unsigned u32x8 __attribute__((ext_vector_type(8)));
typedef unsigned u32x2 __attribute__((ext_vector_type(2)));
typedef __bf16 bf16x2_t __attribute__((ext_vector_type(2)));
typedef unsigned long long u64;
#define LAS __attribute__((address_space(3)))
#define GAS __attribute__((address_space(1)))
#define DI __device__ __forceinline__
#define MFMA(a, b, c) __builtin_amdgcn_mfma_f32_32x32x16_bf16((a), (b), (c), 0, 0, 0)
typedef __bf16 bfx8 __attribute__((ext_vector_type(8)));
typedef __bf16 bfx16 __attribute__((ext_vector_type(16)));
#define PV(a, S, half, c) __builtin_amdgcn_smfmac_f32_32x32x32_bf16(__builtin_bit_cast(bfx8, a), \
    __builtin_bit_cast(bfx16, __builtin_shufflevector(S, S, 8 * half, 8 * half + 1, 8 * half + 2, 8 * half + 3, 8 * half + 4, 8 * half + 5, 8 * half + 6, 8 * half + 7)), (c), 0xDDDD, 0, 0)
#define SBAR() __builtin_amdgcn_sched_barrier(0)
#define RLX_AGENT __ATOMIC_RELAXED, __HIP_MEMORY_SCOPE_AGENT
#define EXP2(x) __builtin_amdgcn_exp2f(x)

constexpr float LOG2E = 1.4426950408889634f;
constexpr unsigned BLK = 16512;
constexpr unsigned WS_KV = 0;
constexpr unsigned WS_O = WS_KV + 256 * BLK;
constexpr unsigned WS_PK = WS_O + (4u << 20);
constexpr unsigned WS_KVF = WS_PK + 32 * 32 * 2176;
constexpr unsigned WS_F1 = WS_KVF + 32 * 2176;
constexpr unsigned WS_F2 = WS_F1 + 2048;
constexpr unsigned WS_F3 = WS_F2 + 8192;
constexpr unsigned WS_F5 = WS_F3 + 8192;
constexpr unsigned WS_EP = WS_F5 + 8192;
constexpr unsigned WS_END = WS_EP + 128;
constexpr int L_X = 0, L_RING = 65536;
constexpr int L_PHK = 65536, L_PHQ = 81920, L_KA = 0, L_VA = 8192, L_VB = 16384;
constexpr int L_WOA = 0, L_WOB = 65536;
constexpr int L_WPHI = L_RING + 3 * 16384;
constexpr int L_G = 131072, L_SYNC = L_G + 256, L_SEEN = L_SYNC, L_STORED = L_SYNC + 4, L_HCNT = L_SYNC + 8, L_OBS = L_SYNC + 12, L_PAIR = L_SYNC + 16, L_ONES = L_SYNC + 128, L_DUMMY = L_SYNC + 256, L_G2 = L_SYNC + 512;
constexpr int L_QA = L_G + 1024, L_EX = L_QA + 8192;
constexpr int LDS_BYTES = 163840;
#define KVL_AUX 16
#define RETRY_SLEEP 2
#define WO_T0 4500
#define WO_DT 1700
#define PK_DELAY 24
#define KV_AUX 16
#define POLL_GAP 2
#define POLL_FIRST 6
#define HPRIO 3
#define WO_MODE 0
#define OBS_U 2
#define GATE_STEP 22
#define PUB_DELAY 4
#define YPRIO 1
#define F1_STEP 14
#define PRIO_SWITCH 23
constexpr int CODE_LINES = 270;
constexpr unsigned SPIN_CAP = 1u << 20;

template <int N> struct IC { static constexpr int value = N; };
template <int I, int N, class F> DI void static_for(F&& f) { if constexpr (I < N) { f(IC<I>{}); static_for<I + 1, N>(f); } }
DI unsigned pk2(float a, float b) { f32x2 v = {a, b}; return __builtin_bit_cast(unsigned, __builtin_convertvector(v, bf16x2_t)); }
DI bf16x8 pack8(const f32x16& x, int s) {
    u32x4 p = {pk2(x[8 * s], x[8 * s + 1]), pk2(x[8 * s + 2], x[8 * s + 3]), pk2(x[8 * s + 4], x[8 * s + 5]), pk2(x[8 * s + 6], x[8 * s + 7])};
    return __builtin_bit_cast(bf16x8, p);
}
DI float fmx(float a, float b) { return __builtin_elementwise_maximum(a, b); }
DI float max32(float v) { auto r = __builtin_amdgcn_permlane32_swap(__float_as_uint(v), __float_as_uint(v), false, false); return fmx(__uint_as_float(r[0]), __uint_as_float(r[1])); }
DI float lmax16(const f32x16& s) {
    const float a = fmx(fmx(s[0], s[1]), s[2]), b = fmx(fmx(s[3], s[4]), s[5]), c = fmx(fmx(s[6], s[7]), s[8]), d = fmx(fmx(s[9], s[10]), s[11]), e = fmx(fmx(s[12], s[13]), s[14]);
    return fmx(fmx(fmx(a, b), c), fmx(fmx(d, e), s[15]));
}
DI float max16(const f32x16& s) { return max32(lmax16(s)); }
DI void glds2(unsigned v0, unsigned v1, const void* sbase, unsigned ldsdst) {
    unsigned keep;
    asm volatile("s_mov_b32 %0, m0\n\ts_mov_b32 m0, %4\n\ts_nop 0\n\tglobal_load_lds_dwordx4 %1, %3\n\tglobal_load_lds_dwordx4 %2, %3 offset:1024\n\ts_mov_b32 m0, %0"
                 : "=&s"(keep) : "v"(v0), "v"(v1), "s"(sbase), "s"(ldsdst) : "memory");
}
DI void glds4(const unsigned (&v)[4], const void* sbase, unsigned ldsdst) {
    unsigned keep;
    asm volatile("s_mov_b32 %0, m0\n\ts_mov_b32 m0, %6\n\ts_nop 0\n\tglobal_load_lds_dwordx4 %1, %5\n\tglobal_load_lds_dwordx4 %2, %5 offset:1024\n\t"
                 "global_load_lds_dwordx4 %3, %5 offset:2048\n\tglobal_load_lds_dwordx4 %4, %5 offset:3072\n\ts_mov_b32 m0, %0"
                 : "=&s"(keep) : "v"(v[0]), "v"(v[1]), "v"(v[2]), "v"(v[3]), "s"(sbase), "s"(ldsdst) : "memory");
}
DI void touch(unsigned voff, const void* sbase, unsigned ldsdst) {
    unsigned keep;
    asm volatile("s_mov_b32 %0, m0\n\ts_mov_b32 m0, %3\n\ts_nop 0\n\tglobal_load_lds_dword %1, %2 sc1\n\ts_mov_b32 m0, %0"
                 : "=&s"(keep) : "v"(voff), "s"(sbase), "s"(ldsdst) : "memory");
}
typedef short v4i16_t __attribute__((ext_vector_type(4)));
DI s16x4 trread(LAS const char* p) { return __builtin_bit_cast(s16x4, __builtin_amdgcn_ds_read_tr16_b64_v4i16((LAS v4i16_t*)p)); }
DI bf16x8 trfrag(LAS const char* p, int d2) { s16x4 lo = trread(p), hi = trread(p + d2); return __builtin_shufflevector(lo, hi, 0, 1, 2, 3, 4, 5, 6, 7); }
DI bf16x8 ldsfrag(LAS const char* p) { return *(LAS const bf16x8*)p; }
#define BAR() asm volatile("s_waitcnt lgkmcnt(0)\n\ts_barrier" ::: "memory")
#define VM_BAR(N) asm volatile("s_waitcnt vmcnt(" #N ") lgkmcnt(0)\n\ts_barrier" ::: "memory")
#define DRAIN() asm volatile("s_waitcnt vmcnt(0)" ::: "memory")
typedef __amdgpu_buffer_rsrc_t rsrc_t;
DI bf16x8 ld16(rsrc_t r, unsigned voff, unsigned soff) { return __builtin_bit_cast(bf16x8, __builtin_amdgcn_raw_buffer_load_b128(r, voff, soff, 16)); }
DI bf16x8 ldkv(rsrc_t r, unsigned voff, unsigned soff) { return __builtin_bit_cast(bf16x8, __builtin_amdgcn_raw_buffer_load_b128(r, voff, soff, KVL_AUX)); }
DI void st16(rsrc_t r, bf16x8 v, unsigned voff, unsigned soff) { __builtin_amdgcn_raw_buffer_store_b128(__builtin_bit_cast(u32x4, v), r, voff, soff, 16); }
DI void st16kv(rsrc_t r, bf16x8 v, unsigned voff, unsigned soff) { __builtin_amdgcn_raw_buffer_store_b128(__builtin_bit_cast(u32x4, v), r, voff, soff, KV_AUX); }
DI unsigned lds_word(LAS char* p) { return __builtin_amdgcn_readfirstlane(*(LAS volatile unsigned*)p); }
DI void lds_wait(LAS char* p, unsigned want) {
#pragma unroll 1
    for (unsigned spins = 0; spins < (SPIN_CAP << 6) && lds_word(p) < want; ++spins) __builtin_amdgcn_s_sleep(1);
    asm volatile("" ::: "memory");
}
DI void lds_arrive(LAS char* p, int lane) {
    asm volatile("s_waitcnt lgkmcnt(0)" ::: "memory");
    if (lane == 0) __hip_atomic_fetch_add((LAS unsigned*)p, 1u, __ATOMIC_RELAXED, __HIP_MEMORY_SCOPE_WORKGROUP);
}
DI void set_flag(char* ws, unsigned off, u64 tag) { __hip_atomic_store((GAS u64*)(ws + off), tag, RLX_AGENT); }
template <int FIRST, int GAP> DI void wait_flags(char* ws, unsigned off, int n, u64 tag, int lane) {
    GAS u64* p = (GAS u64*)(ws + off) + (lane < n ? lane : 0);
    if (FIRST) __builtin_amdgcn_s_sleep(FIRST);
#pragma unroll 1
    for (unsigned spins = 0; spins < SPIN_CAP; ++spins) {
        const u64 v = __hip_atomic_load(p, RLX_AGENT);
        if (__all(v == tag)) break;
        __builtin_amdgcn_s_sleep(GAP);
    }
    __builtin_amdgcn_fence(__ATOMIC_ACQUIRE, "wavefront");
    asm volatile("" ::: "memory");
}

DI void softmax16(const f32x16& p, bf16x8 (&out)[2]) {
#define KEEP1(x) asm("" : "+v"(x))
    const float nm = -lmax16(p) * LOG2E;
    float e[16], t[8];
#pragma unroll
    for (int i = 0; i < 16; ++i) { e[i] = __builtin_fmaf(p[i], LOG2E, nm); KEEP1(e[i]); e[i] = EXP2(e[i]); }
#pragma unroll
    for (int i = 0; i < 8; ++i) { t[i] = e[2 * i] + e[2 * i + 1]; KEEP1(t[i]); }
#pragma unroll
    for (int i = 0; i < 4; ++i) { t[i] = t[2 * i] + t[2 * i + 1]; KEEP1(t[i]); }
    t[0] += t[1]; KEEP1(t[0]); t[2] += t[3]; KEEP1(t[2]);
    const float rs = __builtin_amdgcn_rcpf(t[0] + t[2]);
    u32x4 u[2];
#pragma unroll
    for (int i = 0; i < 16; ++i) { e[i] *= rs; KEEP1(e[i]); }
#pragma unroll
    for (int i = 0; i < 8; ++i) u[i >> 2][i & 3] = pk2(e[2 * i], e[2 * i + 1]);
    out[0] = __builtin_bit_cast(bf16x8, u[0]); out[1] = __builtin_bit_cast(bf16x8, u[1]);
}

__global__ __launch_bounds__(768, 3) void fused_kernel(const uint16_t* __restrict__ x, const uint16_t* __restrict__ WA, const uint16_t* __restrict__ WB,
                                                    const uint16_t* __restrict__ wphi, const uint16_t* __restrict__ gatew,
                                                    const uint16_t* __restrict__ woA, const uint16_t* __restrict__ woB,
                                                    uint16_t* __restrict__ out, char* ws) {
    __shared__ __attribute__((aligned(16))) char lds[LDS_BYTES];
    LAS char* L = (LAS char*)lds;
    u64 pc0; asm volatile("s_getpc_b64 %0" : "=s"(pc0));
    const unsigned lds0 = (unsigned)(uintptr_t)lds;
    const int tid = threadIdx.x, lane = tid & 63, w = __builtin_amdgcn_readfirstlane(tid >> 6);
    const int r32 = lane & 31, h = lane >> 5;
    const int xcd = blockIdx.x & 7, idx = blockIdx.x >> 3;
    const int qd = 2 * (xcd & 3) + (idx & 1), tl = 2 * (idx >> 1) + (xcd >> 2);
    const int s0 = ((tl >> 1) + 4 * (qd & 1)) & 7;
    const int otl = 4 * xcd + (idx >> 3), ocg = idx & 7;
    const rsrc_t rs = __builtin_amdgcn_make_buffer_rsrc(ws, 0, WS_END, 0x00020000);
    u64 tag;
#define EPOCH_BAR(N) do { asm volatile("s_load_dwordx2 %0, %1, 0x0\n\ts_waitcnt vmcnt(" #N ") lgkmcnt(0)\n\ts_barrier" : "=s"(tag) : "s"(ws + WS_EP) : "memory"); tag += 1; } while (0)
    const int q4 = (lane & 15) >> 2;
    const int trl = ((lane >> 4) & 1) * 32 + (lane & 3) * 8;
    STAMP(ws, 0, 0);
    TS_DECL; TS(0); TS(16);

    asm volatile("" :: "s"(x), "s"(WA), "s"(WB), "s"(wphi), "s"(gatew), "s"(ws));
    if (__builtin_expect(w >= 8, 1)) {
        const int j = w - 8;
        const unsigned vsw = (unsigned)(((lane & 7) ^ (((lane >> 4) & 1) << 2)) << 4);
        unsigned vw[4];
#pragma unroll
        for (int u = 0; u < 4; ++u) vw[u] = (unsigned)((lane >> 3) * 3072) + vsw + u * (8 * 3072 - 1024);
        const unsigned vx0 = (unsigned)((lane >> 3) * 1024 + (((lane & 7) ^ (lane >> 4)) << 4)), vx1 = (vx0 ^ 64u) + 8192 - 1024;
        const char* xb = (const char*)x + (size_t)(64 * tl + 16 * j) * 1024;
        const size_t wo_ = (size_t)((j & 1) * 32) * 3072 + qd * 128;
        const char* wb[3] = {(const char*)WA + wo_ + 1024 + (j >> 1) * 1024, (const char*)WB + wo_ + (j >> 1) * 1024, (j >> 1) ? (const char*)WB + wo_ + 2048 : (const char*)WA + wo_};
        auto issue_x = [&](int i) { const int kc = (s0 + i) & 7; glds2(vx0, vx1, xb + kc * 128, lds0 + L_X + i * 8192 + j * 2048); };
        auto issue_w = [&](int i) { glds4(vw, wb[i >> 3] + ((s0 + i) & 7) * (64 * 3072), lds0 + L_RING + ((i + 4) % 6) * 16384 + j * 4096); };
        __builtin_amdgcn_s_setprio(3);
        TS(4);
        issue_w(0); issue_x(0); issue_x(1); issue_x(2); issue_x(3);
        bf16x8 gw; unsigned warm;
        asm volatile("global_load_dwordx4 %0, %1, %2" : "=v"(gw) : "v"((lane & 15) * 64 + (lane >> 4) * 16), "s"(gatew) : "memory");
        glds2(lane * 16, lane * 16, wphi + qd * 1024, lds0 + L_WPHI);
        issue_w(1);
        int laneh = lane; asm volatile("" : "+v"(laneh));
        const int r32h = laneh & 31, hh = laneh >> 5, q4h = (laneh & 15) >> 2, trlh = ((laneh >> 4) & 1) * 32 + (laneh & 3) * 8;
        const unsigned vswh = (unsigned)(((laneh & 7) ^ (((laneh >> 4) & 1) << 2)) << 4);
        const int head = 4 * qd + j;
        const bool keep = (r32h >> 4) == (j & 1);
        constexpr unsigned OOB = 0x40000000u;
        unsigned a_ve[4], a_pf[2], a_kf[4], vo[4];
#pragma unroll
        for (int u = 0; u < 4; ++u) {
            a_ve[u] = keep ? L_VB + (j >> 1) * 4096 + u * 1024 + laneh * 16 : L_ONES;
            const unsigned n = u * 136 + ((r32h < 16 ? r32h : 16) * 2 + hh) * 4;
            a_kf[u] = (n >> 5) * 128 + (n & 31) * 2;
            vo[u] = (unsigned)((laneh >> 3) * 1024) + vswh + u * (8192 - 1024);
            asm volatile("" : "+v"(a_ve[u]), "+v"(a_kf[u]), "+v"(vo[u]));
        }
        a_pf[0] = L_PHK + j * 4096 + (4 * hh + q4h) * 64 + (trlh ^ ((2 * hh + (q4h >> 1)) * 8)); a_pf[1] = (a_pf[0] ^ 32) + 512;
        unsigned a_l16 = laneh * 16, a_q = L_PHQ + j * 4096 + laneh * 16, a_g2 = L_G2 + r32h * 4;
        unsigned v_pk = (keep || (r32h & 15) == 0) ? (unsigned)(((keep ? (r32h & 15) : 16) * 2 + hh) * 16) : OOB;
        unsigned v_one = laneh == 0 ? 0u : OOB, v_p32 = r32h * 8, v_p17 = (laneh < 17 ? laneh : 0) * 8, v_p8 = (laneh & 7) * 8;
        unsigned v_h1 = hh * 8704 + r32h * 4, v_h1s = (laneh < 32 && !(laneh & 1)) ? laneh * 2 : OOB, v_tch = ((laneh >> 3) & 1) * 32768 + (laneh & 7) * 128;
        bf16x8 none = {-1, -1, -1, -1, -1, -1, -1, -1};
        asm volatile("" : "+v"(a_pf[0]), "+v"(a_pf[1]), "+v"(a_l16), "+v"(a_q), "+v"(a_g2), "+v"(v_pk), "+v"(v_one), "+v"(v_p32), "+v"(v_p17), "+v"(v_p8));
        asm volatile("" : "+v"(v_h1), "+v"(v_h1s), "+v"(v_tch), "+v"(none));
        TS(5);
        VM_BAR(13); issue_w(2); issue_w(3);
        VM_BAR(8); issue_x(4); issue_w(4);
        EPOCH_BAR(10); issue_x(5); issue_w(5);
        VM_BAR(12); issue_x(6); issue_w(6);
        VM_BAR(12); issue_x(7); issue_w(7);
        VM_BAR(12); issue_w(8); issue_w(9);
        asm volatile("global_load_dword %0, %1, %2" : "=v"(warm) : "v"(((tl >> 1) * 32 + (lane & 15)) * 3072), "s"(wb[2] - (j & 1) * 16 * 3072) : "memory");
        VM_BAR(15);
        VM_BAR(9); issue_w(10); issue_w(11);
        u32x2 tagv = {(unsigned)tag, (unsigned)(tag >> 32)};
        asm volatile("" : "+v"(tagv));
        f32x4 gl = {};
        const int grow = 16 * j + (lane & 15);
        int gxa = L_X + grow * 128 + (((lane >> 4) ^ ((grow >> 1) & 7)) << 4);
        u64 f1early = 0;
        float gt = 0.f;
        unsigned a_gt = lane < 16 ? L_G + grow * 4 : lane < 32 ? L_G2 + grow * 4 : L_ONES + (lane & 3) * 4;
        asm volatile("" : "+v"(a_gt));
#pragma unroll
        for (int i = 8; i < 24; i += 2) {
            if (i < 22) VM_BAR(8); else VM_BAR(0);
            if (i == 8) asm volatile("" : "+v"(gw), "+v"(warm));
            bf16x8 xg[4], ga[4];
            if (i < 16) {
#pragma unroll
                for (int u = 0; u < 4; ++u) {
                    xg[u] = ldsfrag(L + (gxa ^ ((u & 1) << 6)) + (i - 8 + (u >> 1)) * 8192);
                    ga[u] = (lane & 15) == 2 * ((s0 + i + (u >> 1)) & 7) + (u & 1) ? gw : bf16x8{};
                }
            }
            SBAR();
            if (i < 20) { issue_w(i + 4); issue_w(i + 5); }
            SBAR();
            if (i < 16) {
#pragma unroll
                for (int u = 0; u < 4; ++u) gl = __builtin_amdgcn_mfma_f32_16x16x32_bf16(ga[u], xg[u], gl, 0, 0, 0);
            }
            if (i == GATE_STEP) {
                float z = (gl[0] + gl[1]) + (gl[2] + gl[3]);
                z += __shfl_xor(z, 16); z += __shfl_xor(z, 32);
                gt = __builtin_amdgcn_rcpf(1.f + EXP2(-z * LOG2E));
                gt = lane < 16 ? gt : lane < 32 ? 1.f - gt : __uint_as_float(0x3F803F80u);
                asm volatile("" : "+v"(gt));
            }
            if (i == 22) *(LAS float*)(L + a_gt) = gt;
            if (j == 0 && i == 20) {
                f1early = __hip_atomic_load((GAS u64*)(ws + WS_F1 + qd * 256) + (lane & 31), RLX_AGENT);
                const bool seen = __all(f1early == tag);
                if (lane < 36) *(LAS unsigned*)(L + L_SYNC + lane * 4) = lane >= 32 ? 0x3F803F80u : (lane == 0 && seen) ? 1u : 0u;
            }
            if (j == 0 && i == 22 && !__all(f1early == tag)) {
                TS(1);
                wait_flags<0, 2>(ws, WS_F1 + qd * 256, 32, tag, lane);
                *(LAS volatile unsigned*)(L + L_SEEN) = 1;
                TS(2);
            }
            SBAR();
        }
        auto publish_flag = [&](unsigned off) { DRAIN(); __builtin_amdgcn_raw_buffer_store_b64(tagv, rs, v_one, off, 16); };
        BAR();
        TS(3);
        const u64 tp2 = __builtin_amdgcn_s_memtime();
        __builtin_amdgcn_s_sleep(6);
        bf16x8 phq[2][2]; float g2[2];
        {
            bf16x8 pf[4], ve[4];
#pragma unroll
            for (int u = 0; u < 4; ++u) {
                pf[u] = __builtin_shufflevector(trread(L + a_pf[0] + u * 1024), trread(L + a_pf[1] + u * 1024), 0, 1, 2, 3, 4, 5, 6, 7);
                ve[u] = ldsfrag(L + a_ve[u]);
                phq[u >> 1][u & 1] = ldsfrag(L + a_q + u * 1024);
            }
            g2[0] = *(LAS const float*)(L + a_g2); g2[1] = *(LAS const float*)(L + a_g2 + 128);
            lds_arrive(L + L_HCNT, lane);
            __builtin_amdgcn_s_sleep(PK_DELAY);
            f32x16 kv = MFMA(pf[0], ve[0], f32x16{});
#pragma unroll
            for (int u = 1; u < 4; ++u) kv = MFMA(pf[u], ve[u], kv);
#pragma unroll
            for (int g = 0; g < 4; ++g) st16(rs, __builtin_bit_cast(bf16x8, (f32x4){kv[4 * g], kv[4 * g + 1], kv[4 * g + 2], kv[4 * g + 3]}), v_pk, WS_PK + ((qd * 32 + tl) * 4 + j) * 2176 + g * 544);
            publish_flag(WS_F2 + (head * 32 + tl) * 8);
        }
        lds_wait(L + L_HCNT, 4);
        int wo_done = 0;
        auto wo_service = [&]() {
            if (wo_done < 2 && __builtin_amdgcn_s_memtime() - tp2 >= (u64)(WO_T0 + WO_DT * (j + 4 * wo_done))) {
                const char* src = (const char*)(wo_done ? woA : woB) + (size_t)(128 * j) * 1024 + ocg * 128;
#pragma unroll
                for (int i = 0; i < 4; ++i) glds4(vo, src + i * 32768, lds0 + (wo_done ? L_WOA : L_WOB) + j * 16384 + i * 4096);
                ++wo_done;
            }
        };
        auto wait_slow = [&](unsigned off, unsigned v, int first) {
#pragma unroll 1
            for (unsigned spins = 0; spins < SPIN_CAP; ++spins) {
#pragma unroll 1
                for (int z = 0; z < (spins ? POLL_GAP : first); ++z) { wo_service(); __builtin_amdgcn_s_sleep(6); }
                asm volatile("" ::: "memory");
                if (__all(__builtin_bit_cast(u64, __builtin_amdgcn_raw_buffer_load_b64(rs, v, off, 16)) == tag)) break;
            }
            __builtin_amdgcn_fence(__ATOMIC_ACQUIRE, "wavefront");
            asm volatile("" ::: "memory");
        };
        STAMP(ws, 0, 1);
        if (tl < 17) {
            wait_slow(WS_F2 + head * 256, v_p32, POLL_FIRST);
            float t[16];
#pragma unroll
            for (int i = 0; i < 16; ++i) t[i] = __uint_as_float(__builtin_amdgcn_raw_buffer_load_b32(rs, v_h1, WS_PK + (qd * 128 + j) * 2176 + tl * 128 + i * 17408, 16));
#pragma unroll
            for (int n = 8; n >= 1; n >>= 1)
#pragma unroll
                for (int i = 0; i < n; ++i) { t[i] += t[i + n]; KEEP1(t[i]); }
            auto r = __builtin_amdgcn_permlane32_swap(__float_as_uint(t[0]), __float_as_uint(t[0]), false, false);
            const float x0 = __uint_as_float(r[0]) + __uint_as_float(r[1]);
            const float x1 = __uint_as_float(__builtin_amdgcn_update_dpp(0, __float_as_uint(x0), 0xF5, 0xF, 0xF, true));
            __builtin_amdgcn_raw_buffer_store_b32(pk2(x0, x1), rs, v_h1s, WS_KVF + head * 2176 + tl * 128, 16);
            publish_flag(WS_F3 + (head * 32 + tl) * 8);
        }
        STAMP(ws, 0, 2);
        wait_slow(WS_F3 + head * 256, v_p17, tl < 17 ? POLL_FIRST : 2 * POLL_FIRST + 4);
        {
            u32x2 kq_[4];
#pragma unroll
            for (int g = 0; g < 4; ++g) kq_[g] = __builtin_amdgcn_raw_buffer_load_b64(rs, a_kf[g], WS_KVF + head * 2176, 16);
            const bf16x8 k0 = __builtin_bit_cast(bf16x8, __builtin_shufflevector(kq_[0], kq_[1], 0, 1, 2, 3)), k1 = __builtin_bit_cast(bf16x8, __builtin_shufflevector(kq_[2], kq_[3], 0, 1, 2, 3));
#pragma unroll
            for (int tt = 0; tt < 2; ++tt) {
                f32x16 ob = MFMA(k0, phq[tt][0], f32x16{});
                ob = MFMA(k1, phq[tt][1], ob);
                const float rd = g2[tt] * __builtin_amdgcn_rcpf(ob[8] + 1e-6f);
#pragma unroll
                for (int i = 0; i < 8; ++i) { float z = ob[i] * rd; KEEP1(z); ob[i] = z; }
                st16(rs, pack8(ob, 0), a_l16, WS_O + (2u << 20) + ((2 * tl + tt) * 32 + head) * 1024);
                st16(rs, none, a_l16, WS_O + ((2 * tl + tt) * 32 + head) * 1024);
            }
            publish_flag(WS_F5 + (tl * 32 + head) * 8);
        }
        STAMP(ws, 0, 3);
        wait_slow(WS_F5 + (otl * 32 + 8 * j) * 8, v_p8, POLL_FIRST);
        touch(v_tch, ws + WS_O + (2u << 20) + (2 * otl * 32 + 8 * j + ocg) * 1024, lds0 + L_DUMMY);
        while (wo_done < 2) { wo_service(); __builtin_amdgcn_s_sleep(6); }
        DRAIN();
        lds_arrive(L + L_OBS, lane);
        STAMP(ws, 0, 5);
        HTS_DUMP(ws);
        return;
    }

    if (w == 0) {
        const char* a = (const char*)(pc0 & ~127ull) + (idx * 9 + lane < CODE_LINES ? idx * 9 + lane : 0) * 128;
        if (lane == 9) {
            a = ws + (size_t)(idx < 21 ? idx : 0) * 524288;
            if (idx >= 22) a = idx < 25 ? (const char*)WB + (idx - 22) * 786368 : idx < 28 ? (const char*)out + (idx - 25) * 1048512 : idx < 30 ? (const char*)woA + (idx - 28) * 524224 : (const char*)woB + (idx - 30) * 524224;
        }
        unsigned keep;
        if (lane < 10) asm volatile("s_mov_b32 %0, m0\n\ts_mov_b32 m0, %2\n\ts_nop 0\n\tglobal_load_lds_dword %1, off\n\ts_mov_b32 m0, %0" : "=&s"(keep) : "v"(a), "s"(lds0 + L_WPHI + 4096) : "memory");
    }
    const int p = w & 1;
    const int hq = w >> 1, kh = w & 1, head = 4 * qd + hq;
    const unsigned vK = w * 2048 + lane * 16;
    const unsigned vV0 = r32 < 16 ? (unsigned)(w * 2048 + 1024 + h * 256 + r32 * 16) : ((r32 & 11) == 0 ? 16384u : 0x40000000u);
    const unsigned vV1 = r32 < 16 ? vV0 + 512 : vV0;
    const unsigned kvb = WS_KV + qd * 32 * BLK;
#define KVOFF(t) (kvb + (((t) + tl) & 31) * BLK)
    bf16x8 kr[4], v0r[4], v1r[4];
    const int kpos = ((lane & 0x33) | ((lane & 4) << 1) | ((lane & 8) >> 1)) * 16;
    bf16x8 kref;
    {
        const int xa0 = L_X + r32 * 128 + ((h ^ ((r32 >> 1) & 7)) << 4);
        const int wa0 = L_RING + ((w >> 1) & 1) * 8192 + (8 * h + q4) * 128 + ((p ^ (q4 >> 1)) * 64) + trl;
        bf16x8 xf[2][4][2], w0[2][4];
        f32x16 acc[2];
        int wsb[3], xsb[4];
#pragma unroll
        for (int i = 0; i < 3; ++i) { wsb[i] = wa0 + i * 32768; asm volatile("" : "+v"(wsb[i])); }
#pragma unroll
        for (int s = 0; s < 4; ++s) { xsb[s] = xa0 ^ (s << 5); asm volatile("" : "+v"(xsb[s])); }
        auto gemm = [&](int TEAM, int TYPE) __attribute__((always_inline)) {
            auto mine = [&](int i) __attribute__((always_inline)) { return i >= 0 && i < 24 && ((i >> 3) & 1) == TEAM; };
            auto rx = [&](int i, int s, int t) __attribute__((always_inline)) { if (mine(i)) xf[i & 1][s][t] = ldsfrag(L + xsb[s] + (i & 7) * 8192 + t * 4096); };
            auto rw = [&](int i, int s) __attribute__((always_inline)) { if (mine(i)) w0[i & 1][s] = trfrag(L + wsb[((i + 4) % 6) >> 1] + (i & 1) * 16384 + s * 2048, 512); };
            auto mm = [&](int i, int s, int t, bool now = false) __attribute__((always_inline)) {
                SBAR();
                if (mine(i) && (i != 7 || now)) {
                    const f32x16 c = ((i & 7) == 0 && s == 0) ? f32x16{} : acc[t];
                    acc[t] = (TEAM == 0 && TYPE) ? MFMA(xf[i & 1][s][t], w0[i & 1][s], c) : MFMA(w0[i & 1][s], xf[i & 1][s][t], c);
                }
                SBAR();
            };
            auto first_loads = [&](int t) __attribute__((always_inline)) {
                kr[t] = ldkv(rs, vK, KVOFF(t)); v0r[t] = ldkv(rs, vV0, KVOFF(t)); v1r[t] = ldkv(rs, vV1, KVOFF(t));
                SBAR();
            };
            bf16x8 own[2][2], wf[2], ph[4][2];
            f32x16 f[4];
            static_for<0, 25>([&](auto ic) __attribute__((always_inline)) {
                constexpr int i = ic.value;
                if (i == F1_STEP) { VM_BAR(0); if (tid == 0) set_flag(ws, WS_F1 + (qd * 32 + tl) * 8, tag); }
                else if (i == 0) EPOCH_BAR(63);
                else if (i == 2) { __builtin_amdgcn_s_waitcnt(0x0F70); BAR(); }
                else if (i < 8 || (i < 24 && !(i & 1))) BAR();
                if (i == 0 || i == 1 || i == 8) TS(i + 1); else if (i == 22) TS(17);
                mm(i - 1, 0, 0); rw(i, 0); rx(i, 0, 0);
                mm(i - 1, 0, 1); rx(i, 0, 1); rw(i, 1);
                mm(i - 1, 1, 0); rx(i, 1, 0); rx(i, 1, 1);
                mm(i - 1, 1, 1); rw(i, 2); rx(i, 2, 0);
                mm(i - 1, 2, 0); rx(i, 2, 1); rw(i, 3);
                mm(i - 1, 2, 1); rx(i, 3, 0); rx(i, 3, 1);
                mm(i - 1, 3, 0);
                if (TEAM == 0 && (i == 18 || i == 19)) {
#pragma unroll
                    for (int a = 0; a < 2; ++a)
                        *(LAS bf16x8*)(L + (TYPE ? L_VA + ((p * 2 + (i & 1)) * 2 + a) * 1024 + lane * 16 : L_KA + ((2 * p + a) * 2 + (i & 1)) * 1024 + kpos)) = own[i & 1][a];
                }
                mm(i - 1, 3, 1);
                if (TEAM == 0 && i == 7) {
#pragma unroll
                    for (int z = 0; z < 8; ++z) mm(7, z >> 1, z & 1, true);
#pragma unroll
                    for (int t = 0; t < 2; ++t) {
                        if (TYPE == 0) {
#pragma unroll
                            for (int e = 0; e < 16; ++e) acc[t][e] *= 0.25f * LOG2E;
                        }
                        own[t][0] = pack8(acc[t], 0); own[t][1] = pack8(acc[t], 1);
                        asm volatile("" : "+v"(own[t][0]), "+v"(own[t][1]));
                    }
                }
                if (TEAM == 0 && i == 8) {
                    __builtin_amdgcn_s_sleep(PUB_DELAY);
                    if (TYPE == 0) st16kv(rs, (bf16x8){0x3F80, 0x3F80, 0x3F80, 0x3F80, 0x3F80, 0x3F80, 0x3F80, 0x3F80}, tid < 8 ? (unsigned)(16384 + lane * 16) : 0x40000000u, kvb + tl * BLK);
#pragma unroll
                    for (int t = 0; t < 2; ++t) {
                        const unsigned so = kvb + tl * BLK + (4 * p + t) * 2048;
#pragma unroll
                        for (int a = 0; a < 2; ++a) {
                            if (TYPE == 0) st16kv(rs, own[t][a], kpos, so + a * 4096);
                            else st16kv(rs, own[t][a], (unsigned)((r32 >> 4) * 4096 + 1024 + h * 256 + (r32 & 15) * 16) + a * 512, so);
                        }
                    }
                }
                if (TEAM == 1) {
                    if (i == 1) {
                        const short sg = (lane & 4) ? (short)0x8000 : (short)0;
#pragma unroll
                        for (int a = 0; a < 2; ++a) {
                            wf[a] = trfrag(L + L_WPHI + (2 * p + a) * 512 + (4 * h + q4) * 32 + (((lane >> 4) & 1) * 2 + ((lane >> 1) & 1)) * 8, 256);
#pragma unroll
                            for (int e = 0; e < 8; ++e) wf[a][e] ^= sg;
                            asm volatile("" : "+v"(wf[a]));
                        }
                    }
                    if (i == 16) {
                        __builtin_amdgcn_s_setprio(YPRIO);
#pragma unroll
                        for (int m = 0; m < 4; ++m) f[m] = MFMA(wf[m >> 1], pack8(acc[m & 1], m >> 1), f32x16{});
                    }
                    if (i == 22) { lds_wait(L + L_SEEN, 1); first_loads(1); first_loads(2); }
                    if (i >= 16 && i < 24 && !(i & 1)) { softmax16(f[(i - 16) / 2], ph[(i - 16) / 2]); asm volatile("" : "+v"(ph[(i - 16) / 2][0]), "+v"(ph[(i - 16) / 2][1])); }
                }
                if (i == 24) {
                    kref = ldsfrag(L + L_KA + (hq * 2) * 1024 + lane * 16);
                    kr[0] = ldsfrag(L + L_KA + (hq * 2 + kh) * 1024 + lane * 16);
                    const int va = L_VA + ((hq >> 1) * 2 + kh) * 2048 + (h * 32 + (hq & 1) * 16 + r32) * 16;
                    v0r[0] = ldsfrag(L + (r32 < 16 ? va : L_ONES)); v1r[0] = ldsfrag(L + (r32 < 16 ? va + 1024 : L_ONES));
                }
                SBAR();
            });
            TS(18);
            if (TEAM == 0) {
                lds_wait(L + L_SEEN, 1); first_loads(1);
#pragma unroll
                for (int t = 0; t < 2; ++t)
#pragma unroll
                    for (int a = 0; a < 2; ++a)
                        *(LAS bf16x8*)(L + (TYPE ? L_VB + ((p * 2 + t) * 2 + a) * 1024 : L_QA + ((2 * p + a) * 2 + t) * 1024) + lane * 16) = pack8(acc[t], a);
                SBAR();
                first_loads(2);
            } else if (w < 6) {
#pragma unroll
                for (int m = 0; m < 8; ++m) *(LAS bf16x8*)(L + L_PHQ + (8 * p + m) * 1024 + lane * 16) = ph[m >> 1][m & 1];
            } else {
#pragma unroll
                for (int m = 0; m < 8; ++m) {
                    const u32x4 u = __builtin_bit_cast(u32x4, ph[m >> 1][m & 1]);
#pragma unroll
                    for (int g = 0; g < 2; ++g)
                        *(LAS u32x2*)(L + L_PHK + (4 * p + (m >> 1)) * 2048 + r32 * 64 + 8 * ((2 * (2 * (m & 1) + g) + h) ^ ((r32 >> 1) & 7))) = (u32x2){u[2 * g], u[2 * g + 1]};
                }
            }
        };
        if (w < 2) gemm(0, 0); else if (w < 4) gemm(0, 1); else gemm(1, 0);
    }
    f32x16 oa = {}, ob = {};
    bf16x8 ofb[8];
    TS(20);
    BAR();
    TS(21);
    const bf16x8 qa = ldsfrag(L + L_QA + (hq * 2 + (kh ^ 1)) * 1024 + lane * 16), qb = ldsfrag(L + L_QA + (hq * 2 + kh) * 1024 + lane * 16);
    const float m0 = lmax16(MFMA(kref, qa, f32x16{})), m1 = lmax16(MFMA(kref, qb, f32x16{}));
    TS(23);
    const int ot = w & 1, kq = w >> 1;
    {
        f32x16 negm;
        const float nm = -max32(fmx(m0, m1));
#pragma unroll
        for (int i = 0; i < 16; ++i) negm[i] = nm;
        f32x16 sa = MFMA(kr[0], qa, negm), sb;
        kr[3] = ldkv(rs, vK, KVOFF(3)); v0r[3] = ldkv(rs, vV0, KVOFF(3));
        TS(24);
        STAMP(ws, 0, 4);
#define E(S, i) S[i] = EXP2(S[i])
#define PHASE(S, M1, M2, M3, LD1, LD2, LD3)                                                       \
    do {                                                                                          \
        E(S, 0); E(S, 1); SBAR();                                                                 \
        M1; LD1; SBAR();                                                                          \
        E(S, 2); E(S, 3); E(S, 4); E(S, 5); SBAR();                                               \
        M2; LD2; SBAR();                                                                          \
        E(S, 6); E(S, 7); E(S, 8); E(S, 9); SBAR();                                               \
        M3; LD3; SBAR();                                                                          \
        E(S, 10); E(S, 11); E(S, 12); E(S, 13); E(S, 14); E(S, 15); SBAR();                       \
    } while (0)
        const unsigned obo = WS_O + (2u << 20) + ((2 * otl + ot) * 32 + 8 * kq) * 1024 - w * 2048;
        auto handoff = [&]() {
            LAS char* EXW = L + L_EX + w * 2304 + lane * 16;
            *(LAS f32x4*)EXW = (f32x4){oa[0], oa[1], oa[2], oa[3]};
            *(LAS f32x4*)(EXW + 1024) = (f32x4){oa[4], oa[5], oa[6], oa[7]};
            *(LAS float*)(L + L_EX + w * 2304 + 2048 + lane * 4) = oa[8];
            asm volatile("" ::: "memory");
            *(LAS volatile unsigned*)(L + L_PAIR + w * 4) = 1;
        };
        const int psw = w < 4 ? PRIO_SWITCH : -1;
        auto body = [&](int t0, bool FIRST, bool LAST) __attribute__((always_inline)) {
#pragma unroll
            for (int u = 0; u < 4; ++u) {
                const int t = t0 + u;
                const bool Z = FIRST && u == 0;
                if (!FIRST && !LAST && u == (PRIO_SWITCH & 3)) asm volatile("s_cmp_lg_u32 %0, %1\n\ts_cbranch_scc1 1f\n\ts_setprio 0\n1:" :: "s"(t), "s"(psw) : "scc");
#define OFB(k) ofb[k] = ldkv(rs, vK, obo + (k) * 1024)
                if (LAST && u == OBS_U) lds_wait(L + L_OBS, 4);
                const bool B2 = LAST && u >= 2;
                PHASE(sa, if (!Z) ob = PV(v1r[(u + 3) & 3], sb, 1, ob), sb = MFMA(kr[u], qb, negm), oa = PV(v0r[u], sa, 0, oa),
                      if (!LAST || u == 0) v1r[(u + 3) & 3] = ldkv(rs, vV1, KVOFF(t + 3)); else if (B2) OFB(4 * u - 8),
                      if (!LAST) kr[u] = ldkv(rs, vK, KVOFF(t + 4)); else if (B2) OFB(4 * u - 7), if (B2) OFB(4 * u - 6));
                PHASE(sb, oa = PV(v1r[u], sa, 1, oa), if (!LAST || u < 3) sa = MFMA(kr[(u + 1) & 3], qa, negm), ob = PV(v0r[u], sb, 0, ob),
                      if (B2) OFB(4 * u - 5), (void)0, if (!LAST) v0r[u] = ldkv(rs, vV0, KVOFF(t + 4)); else if (u == 3) handoff());
            }
        };
        if (w < 4) __builtin_amdgcn_s_setprio(2); else __builtin_amdgcn_s_setprio(1);
        STAMP(ws, 3, 0);
        body(0, true, false);
#pragma unroll 1
        for (int t0 = 4; t0 < 28; t0 += 4) {
            STAMP(ws, 3, t0 >> 2);
            body(t0, false, false);
        }
        STAMP(ws, 3, 7);
        body(28, false, true);
        STAMP(ws, 3, 8);
        ob = PV(v1r[3], sb, 1, ob);
#undef PHASE
#undef E
    }
    __builtin_amdgcn_s_setprio(0);
    STAMP(ws, 0, 5);
    int lane_ = __builtin_amdgcn_mbcnt_hi(~0u, __builtin_amdgcn_mbcnt_lo(~0u, 0u));
    asm volatile("" : "+v"(lane_));
    const int r32_ = lane_ & 31, h_ = lane_ >> 5, q4_ = (lane_ & 15) >> 2, trl_ = ((lane_ >> 4) & 1) * 32 + (lane_ & 3) * 8;
    const unsigned oao = WS_O + ((2 * otl + ot) * 32 + 8 * kq) * 1024, vp = lane_ < 8 ? (unsigned)(lane_ * 1024 + 1020) : 0x40000000u;
    auto probe = [&]() { return (unsigned)__ballot(__builtin_amdgcn_raw_buffer_load_b32(rs, vp, oao, 16) != 0xFFFFFFFFu); };
    const unsigned pr0 = __builtin_amdgcn_raw_buffer_load_b32(rs, vp, oao, 16);
    SBAR();
    const int wpo = kq * 16384 + (4 * h_ + q4_) * 128 + trl_;
    bf16x8 wv[8][2];
    auto wreads = [&](int base, int k0, int k1) {
#pragma unroll
        for (int k = k0; k < k1; ++k)
#pragma unroll
            for (int nt = 0; nt < 2; ++nt) wv[k][nt] = trfrag(L + base + wpo + k * 2048 + ((nt ^ (q4_ >> 1)) * 64), 1024);
    };
    const float gate = *(LAS const float*)(L + L_G + (kh * 32 + r32_) * 4);
    lds_wait(L + L_PAIR + (w ^ 1) * 4, 1);
    STAMP(ws, 0, 12);
    {
        LAS const char* EXR = L + L_EX + (w ^ 1) * 2304 + lane_ * 16;
        const f32x4 e0 = *(LAS const f32x4*)EXR, e1 = *(LAS const f32x4*)(EXR + 1024);
        const float e2 = *(LAS const float*)(L + L_EX + (w ^ 1) * 2304 + 2048 + lane_ * 4);
        SBAR();
        wreads(L_WOB, 0, 4);
        SBAR();
        const float l = ob[8] + e2;
#pragma unroll
        for (int i = 0; i < 4; ++i) { ob[i] += e0[i]; ob[4 + i] += e1[i]; }
        float rl = __builtin_amdgcn_rcpf(l), t = fmx(l, rl) * 1.0e19f;
#pragma unroll
        for (int i = 0; i < 8; i += 2) t = fmx(fmx(t, __builtin_fabsf(ob[i])), __builtin_fabsf(ob[i + 1]));
        if (__builtin_expect(__any(!(t < 1.0e38f)), 0)) {
            ob = f32x16{};
            float m = -INFINITY;
            for (int t2 = 0; t2 < 64; ++t2) {
                const unsigned so = kvb + (t2 >> 1) * BLK, fl = (t2 & 1) * 2048, flv = r32_ < 16 ? fl : 0;
                const bf16x8 kf = ld16(rs, vK ^ fl, so), v0 = ld16(rs, vV0 ^ flv, so), v1 = ld16(rs, vV1 ^ flv, so);
                f32x16 s = MFMA(kf, qb, f32x16{});
                const float mn = fmx(max16(s), m), al = EXP2(m - mn);
                m = mn;
#pragma unroll
                for (int i = 0; i < 16; ++i) { ob[i] *= al; s[i] = EXP2(s[i] - mn); }
                ob = PV(v0, s, 0, ob);
                ob = PV(v1, s, 1, ob);
            }
            rl = __builtin_amdgcn_rcpf(ob[8]);
        }
        rl *= gate;
#pragma unroll
        for (int i = 0; i < 8; ++i) ob[i] *= rl;
        st16(rs, __builtin_bit_cast(bf16x8, __builtin_elementwise_min(__builtin_bit_cast(u32x4, pack8(ob, 0)), (u32x4){0xFFFFFFFEu, 0xFFFFFFFEu, 0xFFFFFFFEu, 0xFFFFFFFEu})),
             lane_ * 16, WS_O + ((2 * tl + kh) * 32 + head) * 1024);
        SBAR();
    }
    STAMP(ws, 0, 6);
    if (lane_ == 0) __hip_atomic_fetch_add((LAS unsigned*)(L + L_STORED), 1u, __ATOMIC_RELAXED, __HIP_MEMORY_SCOPE_WORKGROUP);
    f32x16 acc[2];
    f32x4 keep0 = {}, keep1 = {};
    {
        bf16x8 of[8];
        auto loads = [&](int g) {
#pragma unroll
            for (int k = 4 * g; k < 4 * g + 4; ++k) of[k] = ld16(rs, lane_ * 16 + (k & 3) * 1024, oao + (k & 4) * 1024);
        };
        const bf16x8 none = {-1, -1, -1, -1, -1, -1, -1, -1};
        unsigned rdy = (unsigned)__ballot(pr0 != 0xFFFFFFFFu);
#pragma unroll
        for (int g = 0; g < 2; ++g) { if (((rdy >> (4 * g)) & 15) == 15) loads(g); else of[4 * g] = none; }
        SBAR();
#pragma unroll
        for (int k = 0; k < 8; ++k) {
#pragma unroll
            for (int nt = 0; nt < 2; ++nt) acc[nt] = MFMA(wv[k][nt], ofb[k], k ? acc[nt] : f32x16{});
            SBAR();
            if (k < 4) wreads(L_WOB, k + 4, k + 5); else wreads(L_WOA, k - 4, k - 3);
            SBAR();
        }
        wreads(L_WOA, 4, 8);
        lds_arrive(L + L_PAIR + w * 4, lane_);
        SBAR();
        auto missing = [&](int g) {
            const u32x4 v = __builtin_elementwise_max(__builtin_elementwise_max(__builtin_bit_cast(u32x4, of[4 * g]), __builtin_bit_cast(u32x4, of[4 * g + 1])),
                                                      __builtin_elementwise_max(__builtin_bit_cast(u32x4, of[4 * g + 2]), __builtin_bit_cast(u32x4, of[4 * g + 3])));
            return (bool)__any(__builtin_elementwise_max(__builtin_elementwise_max(v[0], v[1]), __builtin_elementwise_max(v[2], v[3])) == 0xFFFFFFFFu);
        };
        bool have1 = ((rdy >> 4) & 15) == 15;
#pragma unroll
        for (int g = 0; g < 2; ++g) {
#pragma unroll 1
            for (unsigned spins = 0; spins < SPIN_CAP && missing(g); ++spins) {
                rdy = lds_word(L + L_STORED) >= 8 ? 0xFFu : probe();
                asm volatile("" ::: "memory");
                if (g == 0 && !have1 && ((rdy >> 4) & 15) == 15) { loads(1); have1 = true; }
                if (((rdy >> (4 * g)) & 15) == 15) loads(g); else __builtin_amdgcn_s_sleep(RETRY_SLEEP);
            }
            if (g == 0) {
#pragma unroll
                for (int k = 0; k < 4; ++k) { acc[0] = MFMA(wv[k][0], of[k], acc[0]); acc[1] = MFMA(wv[k][1], of[k], acc[1]); }
                lds_wait(L + L_PAIR + (w ^ 1) * 4, 2);
            }
        }
        LAS char* PR = L + L_WOB + kq * 16384 + ot * 8192 + lane_ * 16;
#pragma unroll
        for (int nt = 0; nt < 2; ++nt)
#pragma unroll
            for (int k = 4; k < 8; ++k) acc[nt] = MFMA(wv[k][nt], of[k], acc[nt]);
        SBAR();
        STAMP(ws, 0, 7);
#pragma unroll
        for (int z = 0; z < 4; ++z) {
            const int b = 8 * (z & 1);
            const f32x4 u0 = {acc[z >> 1][b], acc[z >> 1][b + 1], acc[z >> 1][b + 2], acc[z >> 1][b + 3]}, u1 = {acc[z >> 1][b + 4], acc[z >> 1][b + 5], acc[z >> 1][b + 6], acc[z >> 1][b + 7]};
            if (z != kq) { *(LAS f32x4*)(PR + z * 2048) = u0; *(LAS f32x4*)(PR + z * 2048 + 1024) = u1; } else { keep0 = u0; keep1 = u1; }
        }
    }
    STAMP(ws, 0, 8);
    BAR();
    {
        f32x4 r0 = keep0, r1 = keep1;
#pragma unroll
        for (int z = 1; z < 4; ++z) {
            LAS const char* src = L + L_WOB + ((kq + z) & 3) * 16384 + ot * 8192 + kq * 2048 + lane_ * 16;
            r0 += *(LAS const f32x4*)src; r1 += *(LAS const f32x4*)(src + 1024);
        }
        unsigned a0 = pk2(r0[0], r0[1]), a1 = pk2(r0[2], r0[3]), b0 = pk2(r1[0], r1[1]), b1 = pk2(r1[2], r1[3]);
        auto s0_ = __builtin_amdgcn_permlane32_swap(a0, b0, false, false);
        auto s1_ = __builtin_amdgcn_permlane32_swap(a1, b1, false, false);
        *(u32x4*)(out + (size_t)(64 * otl + 32 * ot + r32_) * 512 + 64 * ocg + 16 * kq + 8 * h_) = (u32x4){s0_[0], s1_[0], s0_[1], s1_[1]};
    }
    if (blockIdx.x == 0 && w == 0 && lane_ == 0) set_flag(ws, WS_EP, tag);
    STAMP(ws, 0, 9);
    TS_DUMP(ws);
}

extern "C" void kernel_launch(void* const* d_in, const int* in_sizes, int n_in, void* d_out, int out_size, void* d_ws, size_t ws_size, hipStream_t stream) {
    fused_kernel<<<256, 768, 0, stream>>>((const uint16_t*)d_in[0], (const uint16_t*)d_in[1], (const uint16_t*)d_in[4], (const uint16_t*)d_in[5], (const uint16_t*)d_in[7],
                                          (const uint16_t*)d_in[2], (const uint16_t*)d_in[6], (uint16_t*)d_out, (char*)d_ws);
}
```

```cpp
#include <hip/hip_runtime.h>
#include <hip/hip_bf16.h>
#include <stdint.h>
#define HTS_DUMP(ws)
#define STAMP(ws, k, i)
#define TS_DECL
#define TS(i)
#define TS_DUMP(ws)

typedef short bf16x8 __attribute__((ext_vector_type(8)));
typedef short s16x4 __attribute__((ext_vector_type(4)));
typedef float f32x16 __attribute__((ext_vector_type(16)));
typedef float f32x4 __attribute__((ext_vector_type(4)));
typedef float f32x2 __attribute__((ext_vector_type(2)));
typedef unsigned u32x4 __attribute__((ext_vector_type(4)));
typedef unsigned u32x8 __attribute__((ext_vector_type(8)));
typedef unsigned u32x2 __attribute__((ext_vector_type(2)));
typedef __bf16 bf16x2_t __attribute__((ext_vector_type(2)));
typedef unsigned long long u64;
#define LAS __attribute__((address_space(3)))
#define GAS __attribute__((address_space(1)))
#define DI __device__ __forceinline__
#define MFMA(a, b, c) __builtin_amdgcn_mfma_f32_32x32x16_bf16((a), (b), (c), 0, 0, 0)
typedef __bf16 bfx8 __attribute__((ext_vector_type(8)));
typedef __bf16 bfx16 __attribute__((ext_vector_type(16)));
#define PV(a, S, half, c) __builtin_amdgcn_smfmac_f32_32x32x32_bf16(__builtin_bit_cast(bfx8, a), \
    __builtin_bit_cast(bfx16, __builtin_shufflevector(S, S, 8 * half, 8 * half + 1, 8 * half + 2, 8 * half + 3, 8 * half + 4, 8 * half + 5, 8 * half + 6, 8 * half + 7)), (c), 0xDDDD, 0, 0)
#define SBAR() __builtin_amdgcn_sched_barrier(0)
#define RLX_AGENT __ATOMIC_RELAXED, __HIP_MEMORY_SCOPE_AGENT
#define EXP2(x) __builtin_amdgcn_exp2f(x)

constexpr float LOG2E = 1.4426950408889634f;
constexpr unsigned BLK = 16384;
constexpr unsigned WS_KV = 0;
constexpr unsigned WS_O = WS_KV + 256 * BLK;
constexpr unsigned WS_PK = WS_O + (4u << 20);
constexpr unsigned WS_KVF = WS_PK + 32 * 32 * 2176;
constexpr unsigned WS_F1 = WS_KVF + 32 * 2176;
constexpr unsigned WS_F2 = WS_F1 + 2048;
constexpr unsigned WS_F3 = WS_F2 + 8192;
constexpr unsigned WS_F5 = WS_F3 + 8192;
constexpr unsigned WS_EP = WS_F5 + 8192;
constexpr unsigned WS_END = WS_EP + 128;
constexpr int L_X = 0, L_RING = 65536;
constexpr int L_PHK = 65536, L_PHQ = 81920, L_KA = 0, L_VA = 8192, L_VB = 16384;
constexpr int L_WOA = 0, L_WOB = 65536;
constexpr int L_WPHI = L_RING + 3 * 16384;
constexpr int L_G = 131072, L_SYNC = L_G + 256, L_SEEN = L_SYNC, L_STORED = L_SYNC + 4, L_HCNT = L_SYNC + 8, L_OBS = L_SYNC + 12, L_PAIR = L_SYNC + 16, L_ONES = L_SYNC + 128, L_DUMMY = L_SYNC + 256, L_G2 = L_SYNC + 512;
constexpr int L_QA = L_G + 1024, L_EX = L_QA + 8192;
constexpr int LDS_BYTES = 163840;
#define KVL_AUX 16
#define RETRY_SLEEP 2
#define WO_T0 4500
#define WO_DT 1700
#define PK_DELAY 24
#define KV_AUX 16
#define POLL_GAP 2
#define POLL_FIRST 6
#define HPRIO 3
#define WO_MODE 0
#define OBS_U 2
#define GATE_STEP 22
#define PUB_DELAY 4
#define POISON_AT 0
#define ST_GAP 4
#define STAG 0
#define POISON_DELAY 0
#define WO_PRE 0
#define WO_PIECES 1
#define WO_PRE2 0
#define YPRIO 1
#define F1_STEP 14
#define PRIO_SWITCH 23
constexpr int CODE_LINES = 274;
constexpr unsigned SPIN_CAP = 1u << 20;

template <int N> struct IC { static constexpr int value = N; };
template <int I, int N, class F> DI void static_for(F&& f) { if constexpr (I < N) { f(IC<I>{}); static_for<I + 1, N>(f); } }
DI unsigned pk2(float a, float b) { f32x2 v = {a, b}; return __builtin_bit_cast(unsigned, __builtin_convertvector(v, bf16x2_t)); }
DI bf16x8 pack8(const f32x16& x, int s) {
    u32x4 p = {pk2(x[8 * s], x[8 * s + 1]), pk2(x[8 * s + 2], x[8 * s + 3]), pk2(x[8 * s + 4], x[8 * s + 5]), pk2(x[8 * s + 6], x[8 * s + 7])};
    return __builtin_bit_cast(bf16x8, p);
}
DI float fmx(float a, float b) { return __builtin_elementwise_maximum(a, b); }
DI float max32(float v) { auto r = __builtin_amdgcn_permlane32_swap(__float_as_uint(v), __float_as_uint(v), false, false); return fmx(__uint_as_float(r[0]), __uint_as_float(r[1])); }
DI float lmax16(const f32x16& s) {
    const float a = fmx(fmx(s[0], s[1]), s[2]), b = fmx(fmx(s[3], s[4]), s[5]), c = fmx(fmx(s[6], s[7]), s[8]), d = fmx(fmx(s[9], s[10]), s[11]), e = fmx(fmx(s[12], s[13]), s[14]);
    return fmx(fmx(fmx(a, b), c), fmx(fmx(d, e), s[15]));
}
DI float max16(const f32x16& s) { return max32(lmax16(s)); }
DI void glds2(unsigned v0, unsigned v1, const void* sbase, unsigned ldsdst) {
    unsigned keep;
    asm volatile("s_mov_b32 %0, m0\n\ts_mov_b32 m0, %4\n\ts_nop 0\n\tglobal_load_lds_dwordx4 %1, %3\n\tglobal_load_lds_dwordx4 %2, %3 offset:1024\n\ts_mov_b32 m0, %0"
                 : "=&s"(keep) : "v"(v0), "v"(v1), "s"(sbase), "s"(ldsdst) : "memory");
}
DI void glds4(const unsigned (&v)[4], const void* sbase, unsigned ldsdst) {
    unsigned keep;
    asm volatile("s_mov_b32 %0, m0\n\ts_mov_b32 m0, %6\n\ts_nop 0\n\tglobal_load_lds_dwordx4 %1, %5\n\tglobal_load_lds_dwordx4 %2, %5 offset:1024\n\t"
                 "global_load_lds_dwordx4 %3, %5 offset:2048\n\tglobal_load_lds_dwordx4 %4, %5 offset:3072\n\ts_mov_b32 m0, %0"
                 : "=&s"(keep) : "v"(v[0]), "v"(v[1]), "v"(v[2]), "v"(v[3]), "s"(sbase), "s"(ldsdst) : "memory");
}
DI void touch(unsigned voff, const void* sbase, unsigned ldsdst) {
    unsigned keep;
    asm volatile("s_mov_b32 %0, m0\n\ts_mov_b32 m0, %3\n\ts_nop 0\n\tglobal_load_lds_dword %1, %2 sc1\n\ts_mov_b32 m0, %0"
                 : "=&s"(keep) : "v"(voff), "s"(sbase), "s"(ldsdst) : "memory");
}
typedef short v4i16_t __attribute__((ext_vector_type(4)));
DI s16x4 trread(LAS const char* p) { return __builtin_bit_cast(s16x4, __builtin_amdgcn_ds_read_tr16_b64_v4i16((LAS v4i16_t*)p)); }
DI bf16x8 trfrag(LAS const char* p, int d2) { s16x4 lo = trread(p), hi = trread(p + d2); return __builtin_shufflevector(lo, hi, 0, 1, 2, 3, 4, 5, 6, 7); }
DI bf16x8 ldsfrag(LAS const char* p) { return *(LAS const bf16x8*)p; }
#define BAR() asm volatile("s_waitcnt lgkmcnt(0)\n\ts_barrier" ::: "memory")
#define VM_BAR(N) asm volatile("s_waitcnt vmcnt(" #N ") lgkmcnt(0)\n\ts_barrier" ::: "memory")
#define DRAIN() asm volatile("s_waitcnt vmcnt(0)" ::: "memory")
typedef __amdgpu_buffer_rsrc_t rsrc_t;
DI bf16x8 ld16(rsrc_t r, unsigned voff, unsigned soff) { return __builtin_bit_cast(bf16x8, __builtin_amdgcn_raw_buffer_load_b128(r, voff, soff, 16)); }
DI bf16x8 ldkv(rsrc_t r, unsigned voff, unsigned soff) { return __builtin_bit_cast(bf16x8, __builtin_amdgcn_raw_buffer_load_b128(r, voff, soff, KVL_AUX)); }
DI void st16(rsrc_t r, bf16x8 v, unsigned voff, unsigned soff) { __builtin_amdgcn_raw_buffer_store_b128(__builtin_bit_cast(u32x4, v), r, voff, soff, 16); }
DI void st16kv(rsrc_t r, bf16x8 v, unsigned voff, unsigned soff) { __builtin_amdgcn_raw_buffer_store_b128(__builtin_bit_cast(u32x4, v), r, voff, soff, KV_AUX); }
DI unsigned lds_word(LAS char* p) { return __builtin_amdgcn_readfirstlane(*(LAS volatile unsigned*)p); }
DI void lds_wait(LAS char* p, unsigned want) {
#pragma unroll 1
    for (unsigned spins = 0; spins < (SPIN_CAP << 6) && lds_word(p) < want; ++spins) __builtin_amdgcn_s_sleep(1);
    asm volatile("" ::: "memory");
}
DI void lds_arrive(LAS char* p, int lane) {
    asm volatile("s_waitcnt lgkmcnt(0)" ::: "memory");
    if (lane == 0) __hip_atomic_fetch_add((LAS unsigned*)p, 1u, __ATOMIC_RELAXED, __HIP_MEMORY_SCOPE_WORKGROUP);
}
DI void set_flag(char* ws, unsigned off, u64 tag) { __hip_atomic_store((GAS u64*)(ws + off), tag, RLX_AGENT); }
template <int FIRST, int GAP> DI void wait_flags(char* ws, unsigned off, int n, u64 tag, int lane) {
    GAS u64* p = (GAS u64*)(ws + off) + (lane < n ? lane : 0);
    if (FIRST) __builtin_amdgcn_s_sleep(FIRST);
#pragma unroll 1
    for (unsigned spins = 0; spins < SPIN_CAP; ++spins) {
        const u64 v = __hip_atomic_load(p, RLX_AGENT);
        if (__all(v == tag)) break;
        __builtin_amdgcn_s_sleep(GAP);
    }
    __builtin_amdgcn_fence(__ATOMIC_ACQUIRE, "wavefront");
    asm volatile("" ::: "memory");
}

DI void softmax16(const f32x16& p, bf16x8 (&out)[2]) {
#define KEEP1(x) asm("" : "+v"(x))
    const float nm = -lmax16(p) * LOG2E;
    float e[16], t[8];
#pragma unroll
    for (int i = 0; i < 16; ++i) { e[i] = __builtin_fmaf(p[i], LOG2E, nm); KEEP1(e[i]); e[i] = EXP2(e[i]); }
#pragma unroll
    for (int i = 0; i < 8; ++i) { t[i] = e[2 * i] + e[2 * i + 1]; KEEP1(t[i]); }
#pragma unroll
    for (int i = 0; i < 4; ++i) { t[i] = t[2 * i] + t[2 * i + 1]; KEEP1(t[i]); }
    t[0] += t[1]; KEEP1(t[0]); t[2] += t[3]; KEEP1(t[2]);
    const float rs = __builtin_amdgcn_rcpf(t[0] + t[2]);
    u32x4 u[2];
#pragma unroll
    for (int i = 0; i < 16; ++i) { e[i] *= rs; KEEP1(e[i]); }
#pragma unroll
    for (int i = 0; i < 8; ++i) u[i >> 2][i & 3] = pk2(e[2 * i], e[2 * i + 1]);
    out[0] = __builtin_bit_cast(bf16x8, u[0]); out[1] = __builtin_bit_cast(bf16x8, u[1]);
}

__global__ __launch_bounds__(768, 3) void fused_kernel(const uint16_t* __restrict__ x, const uint16_t* __restrict__ WA, const uint16_t* __restrict__ WB,
                                                    const uint16_t* __restrict__ wphi, const uint16_t* __restrict__ gatew,
                                                    const uint16_t* __restrict__ woA, const uint16_t* __restrict__ woB,
                                                    uint16_t* __restrict__ out, char* ws) {
    __shared__ __attribute__((aligned(16))) char lds[LDS_BYTES];
    LAS char* L = (LAS char*)lds;
    u64 pc0; asm volatile("s_getpc_b64 %0" : "=s"(pc0));
    const unsigned lds0 = (unsigned)(uintptr_t)lds;
    const int tid = threadIdx.x, lane = tid & 63, w = __builtin_amdgcn_readfirstlane(tid >> 6);
    const int r32 = lane & 31, h = lane >> 5;
    const int xcd = blockIdx.x & 7, idx = blockIdx.x >> 3;
    const int qd = 2 * (xcd & 3) + (idx & 1), tl = 2 * (idx >> 1) + (xcd >> 2);
    const int s0 = ((tl >> 1) + 4 * (qd & 1)) & 7;
    const int otl = 4 * xcd + (idx >> 3), ocg = idx & 7;
    const rsrc_t rs = __builtin_amdgcn_make_buffer_rsrc(ws, 0, WS_END, 0x00020000);
    u64 tag;
#define EPOCH_BAR(N) do { asm volatile("s_load_dwordx2 %0, %1, 0x0\n\ts_waitcnt vmcnt(" #N ") lgkmcnt(0)\n\ts_barrier" : "=s"(tag) : "s"(ws + WS_EP) : "memory"); tag += 1; } while (0)
    const int q4 = (lane & 15) >> 2;
    const int trl = ((lane >> 4) & 1) * 32 + (lane & 3) * 8;
    STAMP(ws, 0, 0);
    TS_DECL; TS(0); TS(16);

    asm volatile("" :: "s"(x), "s"(WA), "s"(WB), "s"(wphi), "s"(gatew), "s"(ws));
    if (__builtin_expect(w >= 8, 1)) {
        const int j = w - 8;
        const unsigned vsw = (unsigned)(((lane & 7) ^ (((lane >> 4) & 1) << 2)) << 4);
        unsigned vw[4];
#pragma unroll
        for (int u = 0; u < 4; ++u) vw[u] = (unsigned)((lane >> 3) * 3072) + vsw + u * (8 * 3072 - 1024);
        const unsigned vx0 = (unsigned)((lane >> 3) * 1024 + (((lane & 7) ^ (lane >> 4)) << 4)), vx1 = (vx0 ^ 64u) + 8192 - 1024;
        const char* xb = (const char*)x + (size_t)(64 * tl + 16 * j) * 1024;
        const size_t wo_ = (size_t)((j & 1) * 32) * 3072 + qd * 128;
        const char* wb[3] = {(const char*)WA + wo_ + 1024 + (j >> 1) * 1024, (const char*)WB + wo_ + (j >> 1) * 1024, (j >> 1) ? (const char*)WB + wo_ + 2048 : (const char*)WA + wo_};
        auto issue_x = [&](int i) { const int kc = (s0 + i) & 7; glds2(vx0, vx1, xb + kc * 128, lds0 + L_X + i * 8192 + j * 2048); };
        auto issue_w = [&](int i) { glds4(vw, wb[i >> 3] + ((s0 + i) & 7) * (64 * 3072), lds0 + L_RING + ((i + 4) % 6) * 16384 + j * 4096); };
        __builtin_amdgcn_s_setprio(3);
        TS(4);
        issue_w(0); issue_x(0); issue_x(1); issue_x(2); issue_x(3);
        bf16x8 gw; unsigned warm, warm2 = 0;
        asm volatile("global_load_dwordx4 %0, %1, %2" : "=v"(gw) : "v"((lane & 15) * 64 + (lane >> 4) * 16), "s"(gatew) : "memory");
        glds2(lane * 16, lane * 16, wphi + qd * 1024, lds0 + L_WPHI);
        issue_w(1);
        int laneh = lane; asm volatile("" : "+v"(laneh));
        const int r32h = laneh & 31, hh = laneh >> 5, q4h = (laneh & 15) >> 2, trlh = ((laneh >> 4) & 1) * 32 + (laneh & 3) * 8;
        const unsigned vswh = (unsigned)(((laneh & 7) ^ (((laneh >> 4) & 1) << 2)) << 4);
        const int head = 4 * qd + j;
        const bool keep = (r32h >> 4) == (j & 1);
        constexpr unsigned OOB = 0x40000000u;
        unsigned a_ve[4], a_pf[2], a_kf[4], vo[4];
#pragma unroll
        for (int u = 0; u < 4; ++u) {
            a_ve[u] = keep ? L_VB + (j >> 1) * 4096 + u * 1024 + laneh * 16 : L_ONES;
            const unsigned n = u * 136 + ((r32h < 16 ? r32h : 16) * 2 + hh) * 4;
            a_kf[u] = (n >> 5) * 128 + (n & 31) * 2;
            vo[u] = (unsigned)((laneh >> 3) * 1024) + vswh + u * (8192 - 1024);
            asm volatile("" : "+v"(a_ve[u]), "+v"(a_kf[u]), "+v"(vo[u]));
        }
        a_pf[0] = L_PHK + j * 4096 + (4 * hh + q4h) * 64 + (trlh ^ ((2 * hh + (q4h >> 1)) * 8)); a_pf[1] = (a_pf[0] ^ 32) + 512;
        unsigned a_l16 = laneh * 16, a_q = L_PHQ + j * 4096 + laneh * 16, a_g2 = L_G2 + r32h * 4;
        unsigned v_pk = (keep || (r32h & 15) == 0) ? (unsigned)(((keep ? (r32h & 15) : 16) * 2 + hh) * 16) : OOB;
        unsigned v_one = laneh == 0 ? 0u : OOB, v_p32 = r32h * 8, v_p17 = (laneh < 17 ? laneh : 0) * 8, v_p8 = (laneh & 7) * 8;
        unsigned v_h1 = hh * 8704 + r32h * 4, v_h1s = (laneh < 32 && !(laneh & 1)) ? laneh * 2 : OOB, v_tch = ((laneh >> 3) & 1) * 32768 + (laneh & 7) * 128;
        bf16x8 none = {-1, -1, -1, -1, -1, -1, -1, -1};
        asm volatile("" : "+v"(a_pf[0]), "+v"(a_pf[1]), "+v"(a_l16), "+v"(a_q), "+v"(a_g2), "+v"(v_pk), "+v"(v_one), "+v"(v_p32), "+v"(v_p17), "+v"(v_p8));
        asm volatile("" : "+v"(v_h1), "+v"(v_h1s), "+v"(v_tch), "+v"(none));
        TS(5);
        VM_BAR(13); issue_w(2); issue_w(3);
        VM_BAR(8); issue_x(4); issue_w(4);
        EPOCH_BAR(10); issue_x(5); issue_w(5);
        VM_BAR(12); issue_x(6); issue_w(6);
        VM_BAR(12); issue_x(7); issue_w(7);
        VM_BAR(12); issue_w(8); issue_w(9);
        asm volatile("global_load_dword %0, %1, %2" : "=v"(warm) : "v"(((tl >> 1) * 32 + (lane & 15)) * 3072), "s"(wb[2] - (j & 1) * 16 * 3072) : "memory");
        VM_BAR(15);
        VM_BAR(9); issue_w(10); issue_w(11);
        u32x2 tagv = {(unsigned)tag, (unsigned)(tag >> 32)};
        asm volatile("" : "+v"(tagv));
        f32x4 gl = {};
        const int grow = 16 * j + (lane & 15);
        int gxa = L_X + grow * 128 + (((lane >> 4) ^ ((grow >> 1) & 7)) << 4);
        u64 f1early = 0;
        float gt = 0.f;
        unsigned a_gt = lane < 16 ? L_G + grow * 4 : lane < 32 ? L_G2 + grow * 4 : L_ONES + (lane & 3) * 4;
        asm volatile("" : "+v"(a_gt));
#pragma unroll
        for (int i = 8; i < 24; i += 2) {
            if (i < 22) VM_BAR(8); else VM_BAR(0);
            if (i == 8) asm volatile("" : "+v"(gw), "+v"(warm), "+v"(warm2));
            bf16x8 xg[4], ga[4];
            if (i < 16) {
#pragma unroll
                for (int u = 0; u < 4; ++u) {
                    xg[u] = ldsfrag(L + (gxa ^ ((u & 1) << 6)) + (i - 8 + (u >> 1)) * 8192);
                    ga[u] = (lane & 15) == 2 * ((s0 + i + (u >> 1)) & 7) + (u & 1) ? gw : bf16x8{};
                }
            }
            SBAR();
            if (i < 20) { issue_w(i + 4); issue_w(i + 5); }
            SBAR();
            if (i < 16) {
#pragma unroll
                for (int u = 0; u < 4; ++u) gl = __builtin_amdgcn_mfma_f32_16x16x32_bf16(ga[u], xg[u], gl, 0, 0, 0);
            }
            if (i == GATE_STEP) {
                float z = (gl[0] + gl[1]) + (gl[2] + gl[3]);
                z += __shfl_xor(z, 16); z += __shfl_xor(z, 32);
                gt = __builtin_amdgcn_rcpf(1.f + EXP2(-z * LOG2E));
                gt = lane < 16 ? gt : lane < 32 ? 1.f - gt : __uint_as_float(0x3F803F80u);
                asm volatile("" : "+v"(gt));
            }
            if (i == 22) *(LAS float*)(L + a_gt) = gt;
            if (j == 0 && i == 20) {
                f1early = __hip_atomic_load((GAS u64*)(ws + WS_F1 + qd * 256) + (lane & 31), RLX_AGENT);
                const bool seen = __all(f1early == tag);
                if (lane < 36) *(LAS unsigned*)(L + L_SYNC + lane * 4) = lane >= 32 ? 0x3F803F80u : (lane == 0 && seen) ? 1u : 0u;
            }
            if (j == 0 && i == 22 && !__all(f1early == tag)) {
                TS(1);
                wait_flags<0, 2>(ws, WS_F1 + qd * 256, 32, tag, lane);
                *(LAS volatile unsigned*)(L + L_SEEN) = 1;
                TS(2);
            }
            SBAR();
        }
        auto publish_flag = [&](unsigned off) { DRAIN(); __builtin_amdgcn_raw_buffer_store_b64(tagv, rs, v_one, off, 16); };
        BAR();
        TS(3);
        const u64 tp2 = __builtin_amdgcn_s_memtime();
        __builtin_amdgcn_s_sleep(6);
        bf16x8 phq[2][2]; float g2[2];
        {
            bf16x8 pf[4], ve[4];
#pragma unroll
            for (int u = 0; u < 4; ++u) {
                pf[u] = __builtin_shufflevector(trread(L + a_pf[0] + u * 1024), trread(L + a_pf[1] + u * 1024), 0, 1, 2, 3, 4, 5, 6, 7);
                ve[u] = ldsfrag(L + a_ve[u]);
                phq[u >> 1][u & 1] = ldsfrag(L + a_q + u * 1024);
            }
            g2[0] = *(LAS const float*)(L + a_g2); g2[1] = *(LAS const float*)(L + a_g2 + 128);
            lds_arrive(L + L_HCNT, lane);
            __builtin_amdgcn_s_sleep(PK_DELAY);
            if (WO_PRE2) touch(v_p8 * 16, (const char*)(j & 2 ? woA : woB) + blockIdx.x * 2048 + (j & 1) * 1024, lds0 + L_DUMMY);
            f32x16 kv = MFMA(pf[0], ve[0], f32x16{});
#pragma unroll
            for (int u = 1; u < 4; ++u) kv = MFMA(pf[u], ve[u], kv);
#pragma unroll
            for (int g = 0; g < 4; ++g) st16(rs, __builtin_bit_cast(bf16x8, (f32x4){kv[4 * g], kv[4 * g + 1], kv[4 * g + 2], kv[4 * g + 3]}), v_pk, WS_PK + ((qd * 32 + tl) * 4 + j) * 2176 + g * 544);
            publish_flag(WS_F2 + (head * 32 + tl) * 8);
        }
        lds_wait(L + L_HCNT, 4);
        int wo_done = 0;
        constexpr int WO_N = 2 * WO_PIECES;
        auto wo_service = [&]() {
            if (wo_done < WO_N && __builtin_amdgcn_s_memtime() - tp2 >= (u64)(WO_T0 + WO_DT * (j + 4 * wo_done))) {
                const int mtx = wo_done / WO_PIECES, pc = wo_done % WO_PIECES;
                const char* src = (const char*)(mtx ? woA : woB) + (size_t)(128 * j) * 1024 + ocg * 128 + pc * (131072 / WO_PIECES);
#pragma unroll
                for (int i = 0; i < 4 / WO_PIECES; ++i) glds4(vo, src + i * 32768, lds0 + (mtx ? L_WOA : L_WOB) + j * 16384 + pc * (16384 / WO_PIECES) + i * 4096);
                ++wo_done;
            }
        };
        auto wait_slow = [&](unsigned off, unsigned v, int first) {
#pragma unroll 1
            for (unsigned spins = 0; spins < SPIN_CAP; ++spins) {
#pragma unroll 1
                for (int z = 0; z < (spins ? POLL_GAP : first); ++z) { wo_service(); __builtin_amdgcn_s_sleep(6); }
                asm volatile("" ::: "memory");
                if (__all(__builtin_bit_cast(u64, __builtin_amdgcn_raw_buffer_load_b64(rs, v, off, 16)) == tag)) break;
            }
            __builtin_amdgcn_fence(__ATOMIC_ACQUIRE, "wavefront");
            asm volatile("" ::: "memory");
        };
        auto poison = [&]() {
#pragma unroll
            for (int tt = 0; tt < 2; ++tt) { st16(rs, none, a_l16, WS_O + ((2 * tl + tt) * 32 + head) * 1024); __builtin_amdgcn_s_sleep(ST_GAP); }
        };
        if (POISON_AT == 1) { __builtin_amdgcn_s_sleep(8); poison(); }
        STAMP(ws, 0, 1);
        if (tl < 17) {
            wait_slow(WS_F2 + head * 256, v_p32, POLL_FIRST);
            float t[16];
#pragma unroll
            for (int i = 0; i < 16; ++i) t[i] = __uint_as_float(__builtin_amdgcn_raw_buffer_load_b32(rs, v_h1, WS_PK + (qd * 128 + j) * 2176 + tl * 128 + i * 17408, 16));
#pragma unroll
            for (int n = 8; n >= 1; n >>= 1)
#pragma unroll
                for (int i = 0; i < n; ++i) { t[i] += t[i + n]; KEEP1(t[i]); }
            auto r = __builtin_amdgcn_permlane32_swap(__float_as_uint(t[0]), __float_as_uint(t[0]), false, false);
            const float x0 = __uint_as_float(r[0]) + __uint_as_float(r[1]);
            const float x1 = __uint_as_float(__builtin_amdgcn_update_dpp(0, __float_as_uint(x0), 0xF5, 0xF, 0xF, true));
            __builtin_amdgcn_raw_buffer_store_b32(pk2(x0, x1), rs, v_h1s, WS_KVF + head * 2176 + tl * 128, 16);
            publish_flag(WS_F3 + (head * 32 + tl) * 8);
        }
        if (POISON_AT == 2) poison();
        STAMP(ws, 0, 2);
        wait_slow(WS_F3 + head * 256, v_p17, tl < 17 ? POLL_FIRST : 2 * POLL_FIRST + 4);
        {
            u32x2 kq_[4];
#pragma unroll
            for (int g = 0; g < 4; ++g) kq_[g] = __builtin_amdgcn_raw_buffer_load_b64(rs, a_kf[g], WS_KVF + head * 2176, 16);
            const bf16x8 k0 = __builtin_bit_cast(bf16x8, __builtin_shufflevector(kq_[0], kq_[1], 0, 1, 2, 3)), k1 = __builtin_bit_cast(bf16x8, __builtin_shufflevector(kq_[2], kq_[3], 0, 1, 2, 3));
#pragma unroll
            for (int tt = 0; tt < 2; ++tt) {
                f32x16 ob = MFMA(k0, phq[tt][0], f32x16{});
                ob = MFMA(k1, phq[tt][1], ob);
                const float rd = g2[tt] * __builtin_amdgcn_rcpf(ob[8] + 1e-6f);
#pragma unroll
                for (int i = 0; i < 8; ++i) { float z = ob[i] * rd; KEEP1(z); ob[i] = z; }
                if (STAG) { for (int z = 0; z < (tt ? 1 : 2 * j); ++z) __builtin_amdgcn_s_sleep(STAG); }
                st16(rs, pack8(ob, 0), a_l16, WS_O + (2u << 20) + ((2 * tl + tt) * 32 + head) * 1024);
                if (POISON_AT == 0) st16(rs, none, a_l16, WS_O + ((2 * tl + tt) * 32 + head) * 1024);
            }
            publish_flag(WS_F5 + (tl * 32 + head) * 8);
        }
        STAMP(ws, 0, 3);
        wait_slow(WS_F5 + (otl * 32 + 8 * j) * 8, v_p8, POLL_FIRST);
        touch(v_tch, ws + WS_O + (2u << 20) + (2 * otl * 32 + 8 * j + ocg) * 1024, lds0 + L_DUMMY);
        while (wo_done < WO_N) { wo_service(); __builtin_amdgcn_s_sleep(6); }
        DRAIN();
        lds_arrive(L + L_OBS, lane);
        STAMP(ws, 0, 5);
        HTS_DUMP(ws);
        return;
    }

    if (w == 0) {
        const char* a = (const char*)(pc0 & ~127ull) + (idx * 9 + lane < CODE_LINES ? idx * 9 + lane : 0) * 128;
        if (lane == 9) {
            a = ws + (size_t)(idx < 21 ? idx : 0) * 524288;
            if (idx >= 22) a = idx < 25 ? (const char*)WB + (idx - 22) * 786368 : idx < 28 ? (const char*)out + (idx - 25) * 1048512 : idx < 30 ? (const char*)woA + (idx - 28) * 524224 : (const char*)woB + (idx - 30) * 524224;
        }
        unsigned keep;
        if (lane < 10) asm volatile("s_mov_b32 %0, m0\n\ts_mov_b32 m0, %2\n\ts_nop 0\n\tglobal_load_lds_dword %1, off\n\ts_mov_b32 m0, %0" : "=&s"(keep) : "v"(a), "s"(lds0 + L_WPHI + 4096) : "memory");
    }
    const int p = w & 1;
    const int hq = w >> 1, kh = w & 1, head = 4 * qd + hq;
    const unsigned vK = w * 2048 + lane * 16;
    const unsigned vV = (unsigned)(w * 2048 + 1024 + h * 256 + (r32 & 15) * 16);
    const unsigned kvb = WS_KV + qd * 32 * BLK;
#define KVOFF(t) (kvb + (((t) + tl) & 31) * BLK)
    bf16x8 kr[4], v0r[4], v1r[4];
    const int kpos = ((lane & 0x33) | ((lane & 4) << 1) | ((lane & 8) >> 1)) * 16;
    if (POISON_AT == 3) {
        __builtin_amdgcn_s_sleep(POISON_DELAY);
        st16(rs, (bf16x8){-1, -1, -1, -1, -1, -1, -1, -1}, lane * 16, WS_O + ((2 * tl + kh) * 32 + head) * 1024);
    }
    bf16x8 kref;
    {
        const int xa0 = L_X + r32 * 128 + ((h ^ ((r32 >> 1) & 7)) << 4);
        const int wa0 = L_RING + ((w >> 1) & 1) * 8192 + (8 * h + q4) * 128 + ((p ^ (q4 >> 1)) * 64) + trl;
        bf16x8 xf[2][4][2], w0[2][4];
        f32x16 acc[2];
        int wsb[3], xsb[4];
#pragma unroll
        for (int i = 0; i < 3; ++i) { wsb[i] = wa0 + i * 32768; asm volatile("" : "+v"(wsb[i])); }
#pragma unroll
        for (int s = 0; s < 4; ++s) { xsb[s] = xa0 ^ (s << 5); asm volatile("" : "+v"(xsb[s])); }
        auto gemm = [&](int TEAM, int TYPE) __attribute__((always_inline)) {
            auto mine = [&](int i) __attribute__((always_inline)) { return i >= 0 && i < 24 && ((i >> 3) & 1) == TEAM; };
            auto rx = [&](int i, int s, int t) __attribute__((always_inline)) { if (mine(i)) xf[i & 1][s][t] = ldsfrag(L + xsb[s] + (i & 7) * 8192 + t * 4096); };
            auto rw = [&](int i, int s) __attribute__((always_inline)) { if (mine(i)) w0[i & 1][s] = trfrag(L + wsb[((i + 4) % 6) >> 1] + (i & 1) * 16384 + s * 2048, 512); };
            auto mm = [&](int i, int s, int t, bool now = false) __attribute__((always_inline)) {
                SBAR();
                if (mine(i) && (i != 7 || now)) {
                    const f32x16 c = ((i & 7) == 0 && s == 0) ? f32x16{} : acc[t];
                    acc[t] = (TEAM == 0 && TYPE) ? MFMA(xf[i & 1][s][t], w0[i & 1][s], c) : MFMA(w0[i & 1][s], xf[i & 1][s][t], c);
                }
                SBAR();
            };
            bf16x8 own[2][2], wf[2], ph[4][2];
            f32x16 f[4];
            static_for<0, 25>([&](auto ic) __attribute__((always_inline)) {
                constexpr int i = ic.value;
                if (i == F1_STEP) { VM_BAR(0); if (tid == 0) set_flag(ws, WS_F1 + (qd * 32 + tl) * 8, tag); }
                else if (i == 0) EPOCH_BAR(63);
                else if (i == 2) { __builtin_amdgcn_s_waitcnt(0x0F70); BAR(); }
                else if (i < 8 || (i < 24 && !(i & 1))) BAR();
                if (i == 0 || i == 1 || i == 8) TS(i + 1); else if (i == 22) TS(17);
                mm(i - 1, 0, 0); rw(i, 0); rx(i, 0, 0);
                mm(i - 1, 0, 1); rx(i, 0, 1); rw(i, 1);
                mm(i - 1, 1, 0); rx(i, 1, 0); rx(i, 1, 1);
                mm(i - 1, 1, 1); rw(i, 2); rx(i, 2, 0);
                mm(i - 1, 2, 0); rx(i, 2, 1); rw(i, 3);
                mm(i - 1, 2, 1); rx(i, 3, 0); rx(i, 3, 1);
                mm(i - 1, 3, 0);
                if (TEAM == 0 && (i == 18 || i == 19)) {
#pragma unroll
                    for (int a = 0; a < 2; ++a)
                        *(LAS bf16x8*)(L + (TYPE ? L_VA + ((p * 2 + (i & 1)) * 2 + a) * 1024 + lane * 16 : L_KA + ((2 * p + a) * 2 + (i & 1)) * 1024 + kpos)) = own[i & 1][a];
                }
                mm(i - 1, 3, 1);
                if (TEAM == 0 && i == 7) {
#pragma unroll
                    for (int z = 0; z < 8; ++z) mm(7, z >> 1, z & 1, true);
#pragma unroll
                    for (int t = 0; t < 2; ++t) {
                        if (TYPE == 0) {
#pragma unroll
                            for (int e = 0; e < 16; ++e) acc[t][e] *= 0.25f * LOG2E;
                        }
                        own[t][0] = pack8(acc[t], 0); own[t][1] = pack8(acc[t], 1);
                        asm volatile("" : "+v"(own[t][0]), "+v"(own[t][1]));
                    }
                }
                if (TEAM == 0 && i == 8) {
                    __builtin_amdgcn_s_sleep(PUB_DELAY);
#pragma unroll
                    for (int t = 0; t < 2; ++t) {
                        const unsigned so = kvb + tl * BLK + (4 * p + t) * 2048;
#pragma unroll
                        for (int a = 0; a < 2; ++a) {
                            if (TYPE == 0) st16kv(rs, own[t][a], kpos, so + a * 4096);
                            else st16kv(rs, own[t][a], (unsigned)((r32 >> 4) * 4096 + 1024 + h * 256 + (r32 & 15) * 16) + a * 512, so);
                        }
                    }
                }
                if (TEAM == 1) {
                    if (i == 1) {
                        const short sg = (lane & 4) ? (short)0x8000 : (short)0;
#pragma unroll
                        for (int a = 0; a < 2; ++a) {
                            wf[a] = trfrag(L + L_WPHI + (2 * p + a) * 512 + (4 * h + q4) * 32 + (((lane >> 4) & 1) * 2 + ((lane >> 1) & 1)) * 8, 256);
#pragma unroll
                            for (int e = 0; e < 8; ++e) wf[a][e] ^= sg;
                            asm volatile("" : "+v"(wf[a]));
                        }
                    }
                    if (i == 16) {
                        __builtin_amdgcn_s_setprio(YPRIO);
#pragma unroll
                        for (int m = 0; m < 4; ++m) f[m] = MFMA(wf[m >> 1], pack8(acc[m & 1], m >> 1), f32x16{});
                    }
                    if (i == 22) {
                        lds_wait(L + L_SEEN, 1);
                        touch(((tl + 1 + ((lane >> 4) & 1)) & 31) * BLK + (w - 4 + 4 * h) * 2048 + (lane & 15) * 128, ws + kvb, lds0 + L_DUMMY);
                    }
                    if (i >= 16 && i < 24 && !(i & 1)) { softmax16(f[(i - 16) / 2], ph[(i - 16) / 2]); asm volatile("" : "+v"(ph[(i - 16) / 2][0]), "+v"(ph[(i - 16) / 2][1])); }
                }
                if (i == 24) {
                    kref = ldsfrag(L + L_KA + (hq * 2) * 1024 + lane * 16);
                    kr[0] = ldsfrag(L + L_KA + (hq * 2 + kh) * 1024 + lane * 16);
                    const int va = L_VA + ((hq >> 1) * 2 + kh) * 2048 + (h * 32 + (hq & 1) * 16 + r32) * 16;
                    v0r[0] = ldsfrag(L + (r32 < 16 ? va : L_ONES)); v1r[0] = ldsfrag(L + (r32 < 16 ? va + 1024 : L_ONES));
                }
                SBAR();
            });
            TS(18);
            if (TEAM == 0) {
#pragma unroll
                for (int t = 0; t < 2; ++t)
#pragma unroll
                    for (int a = 0; a < 2; ++a)
                        *(LAS bf16x8*)(L + (TYPE ? L_VB + ((p * 2 + t) * 2 + a) * 1024 : L_QA + ((2 * p + a) * 2 + t) * 1024) + lane * 16) = pack8(acc[t], a);
            } else if (w < 6) {
#pragma unroll
                for (int m = 0; m < 8; ++m) *(LAS bf16x8*)(L + L_PHQ + (8 * p + m) * 1024 + lane * 16) = ph[m >> 1][m & 1];
            } else {
#pragma unroll
                for (int m = 0; m < 8; ++m) {
                    const u32x4 u = __builtin_bit_cast(u32x4, ph[m >> 1][m & 1]);
#pragma unroll
                    for (int g = 0; g < 2; ++g)
                        *(LAS u32x2*)(L + L_PHK + (4 * p + (m >> 1)) * 2048 + r32 * 64 + 8 * ((2 * (2 * (m & 1) + g) + h) ^ ((r32 >> 1) & 7))) = (u32x2){u[2 * g], u[2 * g + 1]};
                }
            }
        };
        if (w < 2) gemm(0, 0); else if (w < 4) gemm(0, 1); else gemm(1, 0);
    }
    const u32x4 rs4 = {(unsigned)(uintptr_t)ws, (unsigned)((uintptr_t)ws >> 32) & 0xFFFFu, WS_END, 0x00020000u};
#define LDK(R, t) asm volatile("buffer_load_dwordx4 %0, %1, %2, %3 offen sc1" : "=v"(R) : "v"(vK), "s"(rs4), "s"(KVOFF(t)))
#define LDV(R, t, S) asm volatile("s_mov_b64 exec, %4\n\tbuffer_load_dwordx4 %0, %1, %2, %3 offen offset:" #S " sc1\n\ts_mov_b64 exec, -1" : "+v"(R) : "v"(vV), "s"(rs4), "s"(KVOFF(t)), "s"(0x0000FFFF0000FFFFull))
#define LD2(a, b, t) asm volatile("buffer_load_dwordx4 %0, %6, %8, %9 offen sc1\n\tbuffer_load_dwordx4 %1, %6, %8, %10 offen sc1\n\ts_mov_b64 exec, %11\n\t" \
    "buffer_load_dwordx4 %2, %7, %8, %9 offen sc1\n\tbuffer_load_dwordx4 %3, %7, %8, %9 offen offset:512 sc1\n\t" \
    "buffer_load_dwordx4 %4, %7, %8, %10 offen sc1\n\tbuffer_load_dwordx4 %5, %7, %8, %10 offen offset:512 sc1\n\ts_mov_b64 exec, -1" \
    : "=&v"(kr[a]), "=&v"(kr[b]), "+v"(v0r[a]), "+v"(v1r[a]), "+v"(v0r[b]), "+v"(v1r[b]) : "v"(vK), "v"(vV), "s"(rs4), "s"(KVOFF(t)), "s"(KVOFF((t) + 1)), "s"(0x0000FFFF0000FFFFull))
#define WAITR2(N, R, Q) asm volatile("s_waitcnt vmcnt(" #N ")" : "+v"(R), "+v"(Q))
#define WAITR(N, R) asm volatile("s_waitcnt vmcnt(" #N ")" : "+v"(R))
#pragma unroll
    for (int t = 1; t < 4; ++t) { v0r[t] = ldsfrag(L + L_ONES); v1r[t] = ldsfrag(L + L_ONES); asm volatile("" ::: "memory"); }
    lds_wait(L + L_SEEN, 1);
    LDK(kr[1], 1); LDV(v0r[1], 1, 0); LDV(v1r[1], 1, 512);
    f32x16 oa = {}, ob = {};
    bf16x8 ofb[8];
    TS(20);
    BAR();
    TS(21);
    const bf16x8 qa = ldsfrag(L + L_QA + (hq * 2 + (kh ^ 1)) * 1024 + lane * 16), qb = ldsfrag(L + L_QA + (hq * 2 + kh) * 1024 + lane * 16);
    const float m0 = lmax16(MFMA(kref, qa, f32x16{})), m1 = lmax16(MFMA(kref, qb, f32x16{}));
    TS(23);
    const int ot = w & 1, kq = w >> 1;
    {
        f32x16 negm;
        const float nm = -max32(fmx(m0, m1));
#pragma unroll
        for (int i = 0; i < 16; ++i) negm[i] = nm;
        f32x16 sa = MFMA(kr[0], qa, negm), sb = {};
        TS(24);
        STAMP(ws, 0, 4);
#define E(S, i) S[i] = EXP2(S[i])
#define PHASE(S, M1, M2, M3, LD1, LD2, LD3)                                                       \
    do {                                                                                          \
        E(S, 0); E(S, 1); SBAR();                                                                 \
        M1; LD1; SBAR();                                                                          \
        E(S, 2); E(S, 3); E(S, 4); E(S, 5); SBAR();                                               \
        M2; LD2; SBAR();                                                                          \
        E(S, 6); E(S, 7); E(S, 8); E(S, 9); SBAR();                                               \
        M3; LD3; SBAR();                                                                          \
        E(S, 10); E(S, 11); E(S, 12); E(S, 13); E(S, 14); E(S, 15); SBAR();                       \
    } while (0)
        const unsigned obo = WS_O + (2u << 20) + ((2 * otl + ot) * 32 + 8 * kq) * 1024 - w * 2048;
        auto handoff = [&]() {
            LAS char* EXW = L + L_EX + w * 2304 + lane * 16;
            *(LAS f32x4*)EXW = (f32x4){oa[0], oa[1], oa[2], oa[3]};
            *(LAS f32x4*)(EXW + 1024) = (f32x4){oa[4], oa[5], oa[6], oa[7]};
            *(LAS float*)(L + L_EX + w * 2304 + 2048 + lane * 4) = oa[8];
            asm volatile("" ::: "memory");
            *(LAS volatile unsigned*)(L + L_PAIR + w * 4) = 1;
        };
        const int psw = w < 4 ? PRIO_SWITCH : -1;
        if (w < 4) __builtin_amdgcn_s_setprio(2); else __builtin_amdgcn_s_setprio(1);
#define PSW(t, u) if (u == (PRIO_SWITCH & 3)) asm volatile("s_cmp_lg_u32 %0, %1\n\ts_cbranch_scc1 1f\n\ts_setprio 0\n1:" :: "s"(t), "s"(psw) : "scc")
#define PAIR(t, u)                                                                                                  \
        PSW(t, u);                                                                                                                                                      \
        PHASE(sa, ob = PV(v1r[(u + 3) & 3], sb, 1, ob), sb = MFMA(kr[u], qb, negm), WAITR(9, v0r[u]); oa = PV(v0r[u], sa, 0, oa), SBAR(); LD2(u ^ 2, u ^ 3, (t) + 2), (void)0, (void)0); \
        PHASE(sb, WAITR2(8, v1r[u], kr[u + 1]); oa = PV(v1r[u], sa, 1, oa), sa = MFMA(kr[u + 1], qa, negm), ob = PV(v0r[u], sb, 0, ob), (void)0, (void)0, (void)0);     \
        PSW((t) + 1, u + 1);                                                                                                                                            \
        PHASE(sa, ob = PV(v1r[u], sb, 1, ob), sb = MFMA(kr[u + 1], qb, negm), WAITR(7, v0r[u + 1]); oa = PV(v0r[u + 1], sa, 0, oa), (void)0, (void)0, (void)0);         \
        PHASE(sb, WAITR(6, v1r[u + 1]); oa = PV(v1r[u + 1], sa, 1, oa), WAITR(5, kr[u ^ 2]); sa = MFMA(kr[u ^ 2], qa, negm), ob = PV(v0r[u + 1], sb, 0, ob), (void)0, (void)0, (void)0)
#pragma unroll 1
        for (int t0 = 0; t0 < 28; t0 += 4) {
            STAMP(ws, 3, t0 >> 2);
            PAIR(t0, 0);
            PAIR(t0 + 2, 2);
        }
        STAMP(ws, 3, 7);
        PAIR(28, 0);
#define OFB(k) ofb[k] = ld16(rs, vK, obo + (k) * 1024)
        asm volatile("s_waitcnt vmcnt(0)" : "+v"(v0r[2]), "+v"(v1r[2]), "+v"(kr[3]), "+v"(v0r[3]), "+v"(v1r[3]));
        lds_wait(L + L_OBS, 4);
        PHASE(sa, ob = PV(v1r[1], sb, 1, ob), sb = MFMA(kr[2], qb, negm), oa = PV(v0r[2], sa, 0, oa), OFB(0), OFB(1), OFB(2));
        PHASE(sb, oa = PV(v1r[2], sa, 1, oa), sa = MFMA(kr[3], qa, negm), ob = PV(v0r[2], sb, 0, ob), OFB(3), (void)0, (void)0);
        PHASE(sa, ob = PV(v1r[2], sb, 1, ob), sb = MFMA(kr[3], qb, negm), oa = PV(v0r[3], sa, 0, oa), OFB(4), OFB(5), OFB(6));
        PHASE(sb, oa = PV(v1r[3], sa, 1, oa), (void)0, ob = PV(v0r[3], sb, 0, ob), OFB(7), (void)0, handoff());
        STAMP(ws, 3, 8);
        ob = PV(v1r[3], sb, 1, ob);
#undef PHASE
#undef E
    }
    __builtin_amdgcn_s_setprio(0);
    STAMP(ws, 0, 5);
    int lane_ = __builtin_amdgcn_mbcnt_hi(~0u, __builtin_amdgcn_mbcnt_lo(~0u, 0u));
    asm volatile("" : "+v"(lane_));
    const int r32_ = lane_ & 31, h_ = lane_ >> 5, q4_ = (lane_ & 15) >> 2, trl_ = ((lane_ >> 4) & 1) * 32 + (lane_ & 3) * 8;
    const unsigned oao = WS_O + ((2 * otl + ot) * 32 + 8 * kq) * 1024, vp = lane_ < 8 ? (unsigned)(lane_ * 1024 + 1020) : 0x40000000u;
    auto probe = [&]() { return (unsigned)__ballot(__builtin_amdgcn_raw_buffer_load_b32(rs, vp, oao, 16) != 0xFFFFFFFFu); };
    const unsigned pr0 = __builtin_amdgcn_raw_buffer_load_b32(rs, vp, oao, 16);
    SBAR();
    const int wpo = kq * 16384 + (4 * h_ + q4_) * 128 + trl_;
    bf16x8 wv[8][2];
    auto wreads = [&](int base, int k0, int k1) {
#pragma unroll
        for (int k = k0; k < k1; ++k)
#pragma unroll
            for (int nt = 0; nt < 2; ++nt) wv[k][nt] = trfrag(L + base + wpo + k * 2048 + ((nt ^ (q4_ >> 1)) * 64), 1024);
    };
    const float gate = *(LAS const float*)(L + L_G + (kh * 32 + r32_) * 4);
    lds_wait(L + L_PAIR + (w ^ 1) * 4, 1);
    STAMP(ws, 0, 12);
    {
        LAS const char* EXR = L + L_EX + (w ^ 1) * 2304 + lane_ * 16;
        const f32x4 e0 = *(LAS const f32x4*)EXR, e1 = *(LAS const f32x4*)(EXR + 1024);
        const float e2 = *(LAS const float*)(L + L_EX + (w ^ 1) * 2304 + 2048 + lane_ * 4);
        SBAR();
        wreads(L_WOB, 0, 4);
        SBAR();
        const float l = ob[8] + e2;
#pragma unroll
        for (int i = 0; i < 4; ++i) { ob[i] += e0[i]; ob[4 + i] += e1[i]; }
        float rl = __builtin_amdgcn_rcpf(l), t = fmx(l, rl) * 1.0e19f;
#pragma unroll
        for (int i = 0; i < 8; i += 2) t = fmx(fmx(t, __builtin_fabsf(ob[i])), __builtin_fabsf(ob[i + 1]));
        if (__builtin_expect(__any(!(t < 1.0e38f)), 0)) {
            ob = f32x16{};
            float m = -INFINITY;
            for (int t2 = 0; t2 < 64; ++t2) {
                const unsigned so = kvb + (t2 >> 1) * BLK, fl = (t2 & 1) * 2048;
                const bf16x8 ones = {0x3F80, 0x3F80, 0x3F80, 0x3F80, 0x3F80, 0x3F80, 0x3F80, 0x3F80};
                const bf16x8 kf = ld16(rs, vK ^ fl, so), v0l = ld16(rs, vV ^ fl, so), v1l = ld16(rs, vV ^ fl, so + 512);
                const bf16x8 v0 = r32_ < 16 ? v0l : ones, v1 = r32_ < 16 ? v1l : ones;
                f32x16 s = MFMA(kf, qb, f32x16{});
                const float mn = fmx(max16(s), m), al = EXP2(m - mn);
                m = mn;
#pragma unroll
                for (int i = 0; i < 16; ++i) { ob[i] *= al; s[i] = EXP2(s[i] - mn); }
                ob = PV(v0, s, 0, ob);
                ob = PV(v1, s, 1, ob);
            }
            rl = __builtin_amdgcn_rcpf(ob[8]);
        }
        rl *= gate;
#pragma unroll
        for (int i = 0; i < 8; ++i) ob[i] *= rl;
        st16(rs, __builtin_bit_cast(bf16x8, __builtin_elementwise_min(__builtin_bit_cast(u32x4, pack8(ob, 0)), (u32x4){0xFFFFFFFEu, 0xFFFFFFFEu, 0xFFFFFFFEu, 0xFFFFFFFEu})),
             lane_ * 16, WS_O + ((2 * tl + kh) * 32 + head) * 1024);
        SBAR();
    }
    STAMP(ws, 0, 6);
    if (lane_ == 0) __hip_atomic_fetch_add((LAS unsigned*)(L + L_STORED), 1u, __ATOMIC_RELAXED, __HIP_MEMORY_SCOPE_WORKGROUP);
    f32x16 acc[2];
    f32x4 keep0 = {}, keep1 = {};
    {
        bf16x8 of[8];
        auto loads = [&](int g) {
#pragma unroll
            for (int k = 4 * g; k < 4 * g + 4; ++k) of[k] = ld16(rs, lane_ * 16 + (k & 3) * 1024, oao + (k & 4) * 1024);
        };
        const bf16x8 none = {-1, -1, -1, -1, -1, -1, -1, -1};
        unsigned rdy = (unsigned)__ballot(pr0 != 0xFFFFFFFFu);
#pragma unroll
        for (int g = 0; g < 2; ++g) { if (((rdy >> (4 * g)) & 15) == 15) loads(g); else of[4 * g] = none; }
        SBAR();
#pragma unroll
        for (int k = 0; k < 8; ++k) {
#pragma unroll
            for (int nt = 0; nt < 2; ++nt) acc[nt] = MFMA(wv[k][nt], ofb[k], k ? acc[nt] : f32x16{});
            SBAR();
            if (k < 4) wreads(L_WOB, k + 4, k + 5); else wreads(L_WOA, k - 4, k - 3);
            SBAR();
        }
        wreads(L_WOA, 4, 8);
        lds_arrive(L + L_PAIR + w * 4, lane_);
        SBAR();
        auto missing = [&](int g) {
            const u32x4 v = __builtin_elementwise_max(__builtin_elementwise_max(__builtin_bit_cast(u32x4, of[4 * g]), __builtin_bit_cast(u32x4, of[4 * g + 1])),
                                                      __builtin_elementwise_max(__builtin_bit_cast(u32x4, of[4 * g + 2]), __builtin_bit_cast(u32x4, of[4 * g + 3])));
            return (bool)__any(__builtin_elementwise_max(__builtin_elementwise_max(v[0], v[1]), __builtin_elementwise_max(v[2], v[3])) == 0xFFFFFFFFu);
        };
        bool have1 = ((rdy >> 4) & 15) == 15;
#pragma unroll
        for (int g = 0; g < 2; ++g) {
#pragma unroll 1
            for (unsigned spins = 0; spins < SPIN_CAP && missing(g); ++spins) {
                rdy = lds_word(L + L_STORED) >= 8 ? 0xFFu : probe();
                asm volatile("" ::: "memory");
                if (g == 0 && !have1 && ((rdy >> 4) & 15) == 15) { loads(1); have1 = true; }
                if (((rdy >> (4 * g)) & 15) == 15) loads(g); else __builtin_amdgcn_s_sleep(RETRY_SLEEP);
            }
            if (g == 0) {
#pragma unroll
                for (int k = 0; k < 4; ++k) { acc[0] = MFMA(wv[k][0], of[k], acc[0]); acc[1] = MFMA(wv[k][1], of[k], acc[1]); }
                lds_wait(L + L_PAIR + (w ^ 1) * 4, 2);
            }
        }
        LAS char* PR = L + L_WOB + kq * 16384 + ot * 8192 + lane_ * 16;
#pragma unroll
        for (int nt = 0; nt < 2; ++nt)
#pragma unroll
            for (int k = 4; k < 8; ++k) acc[nt] = MFMA(wv[k][nt], of[k], acc[nt]);
        SBAR();
        STAMP(ws, 0, 7);
#pragma unroll
        for (int z = 0; z < 4; ++z) {
            const int b = 8 * (z & 1);
            const f32x4 u0 = {acc[z >> 1][b], acc[z >> 1][b + 1], acc[z >> 1][b + 2], acc[z >> 1][b + 3]}, u1 = {acc[z >> 1][b + 4], acc[z >> 1][b + 5], acc[z >> 1][b + 6], acc[z >> 1][b + 7]};
            if (z != kq) { *(LAS f32x4*)(PR + z * 2048) = u0; *(LAS f32x4*)(PR + z * 2048 + 1024) = u1; } else { keep0 = u0; keep1 = u1; }
        }
    }
    STAMP(ws, 0, 8);
    BAR();
    {
        f32x4 r0 = keep0, r1 = keep1;
#pragma unroll
        for (int z = 1; z < 4; ++z) {
            LAS const char* src = L + L_WOB + ((kq + z) & 3) * 16384 + ot * 8192 + kq * 2048 + lane_ * 16;
            r0 += *(LAS const f32x4*)src; r1 += *(LAS const f32x4*)(src + 1024);
        }
        unsigned a0 = pk2(r0[0], r0[1]), a1 = pk2(r0[2], r0[3]), b0 = pk2(r1[0], r1[1]), b1 = pk2(r1[2], r1[3]);
        auto s0_ = __builtin_amdgcn_permlane32_swap(a0, b0, false, false);
        auto s1_ = __builtin_amdgcn_permlane32_swap(a1, b1, false, false);
        *(u32x4*)(out + (size_t)(64 * otl + 32 * ot + r32_) * 512 + 64 * ocg + 16 * kq + 8 * h_) = (u32x4){s0_[0], s1_[0], s0_[1], s1_[1]};
    }
    if (blockIdx.x == 0 && w == 0 && lane_ == 0) set_flag(ws, WS_EP, tag);
    STAMP(ws, 0, 9);
    TS_DUMP(ws);
}

extern "C" void kernel_launch(void* const* d_in, const int* in_sizes, int n_in, void* d_out, int out_size, void* d_ws, size_t ws_size, hipStream_t stream) {
    fused_kernel<<<256, 768, 0, stream>>>((const uint16_t*)d_in[0], (const uint16_t*)d_in[1], (const uint16_t*)d_in[4], (const uint16_t*)d_in[5], (const uint16_t*)d_in[7],
                                          (const uint16_t*)d_in[2], (const uint16_t*)d_in[6], (uint16_t*)d_out, (char*)d_ws);
}
```

```cpp
#include <hip/hip_runtime.h>
#include <hip/hip_bf16.h>
#include <stdint.h>

typedef short bf16x8 __attribute__((ext_vector_type(8)));
typedef short s16x4 __attribute__((ext_vector_type(4)));
typedef float f32x16 __attribute__((ext_vector_type(16)));
typedef float f32x4 __attribute__((ext_vector_type(4)));
typedef float f32x2 __attribute__((ext_vector_type(2)));
typedef unsigned u32x4 __attribute__((ext_vector_type(4)));
typedef unsigned u32x2 __attribute__((ext_vector_type(2)));
typedef __bf16 bf16x2_t __attribute__((ext_vector_type(2)));
typedef unsigned long long u64;
#define LAS __attribute__((address_space(3)))
#define GAS __attribute__((address_space(1)))
#define DI __device__ __forceinline__
#define MFMA(a, b, c) __builtin_amdgcn_mfma_f32_32x32x16_bf16((a), (b), (c), 0, 0, 0)
typedef __bf16 bfx8 __attribute__((ext_vector_type(8)));
typedef __bf16 bfx16 __attribute__((ext_vector_type(16)));
#define PV(a, S, half, c) __builtin_amdgcn_smfmac_f32_32x32x32_bf16(__builtin_bit_cast(bfx8, a), \
    __builtin_bit_cast(bfx16, __builtin_shufflevector(S, S, 8 * half, 8 * half + 1, 8 * half + 2, 8 * half + 3, 8 * half + 4, 8 * half + 5, 8 * half + 6, 8 * half + 7)), (c), 0xDDDD, 0, 0)
#define SBAR() __builtin_amdgcn_sched_barrier(0)
#define RLX_AGENT __ATOMIC_RELAXED, __HIP_MEMORY_SCOPE_AGENT
#define EXP2(x) __builtin_amdgcn_exp2f(x)

constexpr float LOG2E = 1.4426950408889634f;
constexpr unsigned BLK = 16384;
constexpr unsigned WS_KV = 0;
constexpr unsigned WS_O = WS_KV + 256 * BLK;
constexpr unsigned WS_PK = WS_O + (4u << 20);
constexpr unsigned WS_KVF = WS_PK + 32 * 32 * 2176;
constexpr unsigned WS_F1 = WS_KVF + 32 * 2176;
constexpr unsigned WS_F2 = WS_F1 + 2048;
constexpr unsigned WS_F3 = WS_F2 + 8192;
constexpr unsigned WS_F5 = WS_F3 + 8192;
constexpr unsigned WS_EP = WS_F5 + 8192;
constexpr unsigned WS_END = WS_EP + 128;
constexpr int L_X = 0, L_RING = 65536;
constexpr int L_PHK = 65536, L_PHQ = 81920, L_KA = 0, L_VA = 8192, L_VB = 16384;
constexpr int L_WOA = 0, L_WOB = 65536;
constexpr int L_WPHI = L_RING + 3 * 16384;
constexpr int L_G = 131072, L_SYNC = L_G + 256, L_SEEN = L_SYNC, L_STORED = L_SYNC + 4, L_HCNT = L_SYNC + 8, L_OBS = L_SYNC + 12, L_PAIR = L_SYNC + 16, L_ONES = L_SYNC + 128, L_DUMMY = L_SYNC + 256, L_G2 = L_SYNC + 512;
constexpr int L_QA = L_G + 1024, L_EX = L_QA + 8192;
constexpr int LDS_BYTES = 163840;
constexpr int F1_STEP = 14, PRIO_SWITCH = 23, PK_DELAY = 24, WO_T0 = 4500, WO_DT = 1700, POLL_GAP = 2, POLL_FIRST = 6, RETRY_SLEEP = 2, PUB_DELAY = 4;
constexpr int CODE_LINES = 274;
constexpr unsigned SPIN_CAP = 1u << 20;

template <int N> struct IC { static constexpr int value = N; };
template <int I, int N, class F> DI void static_for(F&& f) { if constexpr (I < N) { f(IC<I>{}); static_for<I + 1, N>(f); } }
DI unsigned pk2(float a, float b) { f32x2 v = {a, b}; return __builtin_bit_cast(unsigned, __builtin_convertvector(v, bf16x2_t)); }
DI bf16x8 pack8(const f32x16& x, int s) {
    u32x4 p = {pk2(x[8 * s], x[8 * s + 1]), pk2(x[8 * s + 2], x[8 * s + 3]), pk2(x[8 * s + 4], x[8 * s + 5]), pk2(x[8 * s + 6], x[8 * s + 7])};
    return __builtin_bit_cast(bf16x8, p);
}
DI float fmx(float a, float b) { return __builtin_elementwise_maximum(a, b); }
DI float max32(float v) { auto r = __builtin_amdgcn_permlane32_swap(__float_as_uint(v), __float_as_uint(v), false, false); return fmx(__uint_as_float(r[0]), __uint_as_float(r[1])); }
DI float lmax16(const f32x16& s) {
    const float a = fmx(fmx(s[0], s[1]), s[2]), b = fmx(fmx(s[3], s[4]), s[5]), c = fmx(fmx(s[6], s[7]), s[8]), d = fmx(fmx(s[9], s[10]), s[11]), e = fmx(fmx(s[12], s[13]), s[14]);
    return fmx(fmx(fmx(a, b), c), fmx(fmx(d, e), s[15]));
}
DI float max16(const f32x16& s) { return max32(lmax16(s)); }
DI void glds2(unsigned v0, unsigned v1, const void* sbase, unsigned ldsdst) {
    unsigned keep;
    asm volatile("s_mov_b32 %0, m0\n\ts_mov_b32 m0, %4\n\ts_nop 0\n\tglobal_load_lds_dwordx4 %1, %3\n\tglobal_load_lds_dwordx4 %2, %3 offset:1024\n\ts_mov_b32 m0, %0"
                 : "=&s"(keep) : "v"(v0), "v"(v1), "s"(sbase), "s"(ldsdst) : "memory");
}
DI void glds4(const unsigned (&v)[4], const void* sbase, unsigned ldsdst) {
    unsigned keep;
    asm volatile("s_mov_b32 %0, m0\n\ts_mov_b32 m0, %6\n\ts_nop 0\n\tglobal_load_lds_dwordx4 %1, %5\n\tglobal_load_lds_dwordx4 %2, %5 offset:1024\n\t"
                 "global_load_lds_dwordx4 %3, %5 offset:2048\n\tglobal_load_lds_dwordx4 %4, %5 offset:3072\n\ts_mov_b32 m0, %0"
                 : "=&s"(keep) : "v"(v[0]), "v"(v[1]), "v"(v[2]), "v"(v[3]), "s"(sbase), "s"(ldsdst) : "memory");
}
DI void touch(unsigned voff, const void* sbase, unsigned ldsdst) {
    unsigned keep;
    asm volatile("s_mov_b32 %0, m0\n\ts_mov_b32 m0, %3\n\ts_nop 0\n\tglobal_load_lds_dword %1, %2 sc1\n\ts_mov_b32 m0, %0"
                 : "=&s"(keep) : "v"(voff), "s"(sbase), "s"(ldsdst) : "memory");
}
typedef short v4i16_t __attribute__((ext_vector_type(4)));
DI s16x4 trread(LAS const char* p) { return __builtin_bit_cast(s16x4, __builtin_amdgcn_ds_read_tr16_b64_v4i16((LAS v4i16_t*)p)); }
DI bf16x8 trfrag(LAS const char* p, int d2) { s16x4 lo = trread(p), hi = trread(p + d2); return __builtin_shufflevector(lo, hi, 0, 1, 2, 3, 4, 5, 6, 7); }
DI bf16x8 ldsfrag(LAS const char* p) { return *(LAS const bf16x8*)p; }
#define BAR() asm volatile("s_waitcnt lgkmcnt(0)\n\ts_barrier" ::: "memory")
#define VM_BAR(N) asm volatile("s_waitcnt vmcnt(" #N ") lgkmcnt(0)\n\ts_barrier" ::: "memory")
#define DRAIN() asm volatile("s_waitcnt vmcnt(0)" ::: "memory")
typedef __amdgpu_buffer_rsrc_t rsrc_t;
DI bf16x8 ld16(rsrc_t r, unsigned voff, unsigned soff) { return __builtin_bit_cast(bf16x8, __builtin_amdgcn_raw_buffer_load_b128(r, voff, soff, 16)); }
DI void st16(rsrc_t r, bf16x8 v, unsigned voff, unsigned soff) { __builtin_amdgcn_raw_buffer_store_b128(__builtin_bit_cast(u32x4, v), r, voff, soff, 16); }
DI unsigned lds_word(LAS char* p) { return __builtin_amdgcn_readfirstlane(*(LAS volatile unsigned*)p); }
DI void lds_wait(LAS char* p, unsigned want) {
#pragma unroll 1
    for (unsigned spins = 0; spins < (SPIN_CAP << 6) && lds_word(p) < want; ++spins) __builtin_amdgcn_s_sleep(1);
    asm volatile("" ::: "memory");
}
DI void lds_arrive(LAS char* p, int lane) {
    asm volatile("s_waitcnt lgkmcnt(0)" ::: "memory");
    if (lane == 0) __hip_atomic_fetch_add((LAS unsigned*)p, 1u, __ATOMIC_RELAXED, __HIP_MEMORY_SCOPE_WORKGROUP);
}
DI void set_flag(char* ws, unsigned off, u64 tag) { __hip_atomic_store((GAS u64*)(ws + off), tag, RLX_AGENT); }
template <int FIRST, int GAP> DI void wait_flags(char* ws, unsigned off, int n, u64 tag, int lane) {
    GAS u64* p = (GAS u64*)(ws + off) + (lane < n ? lane : 0);
    if (FIRST) __builtin_amdgcn_s_sleep(FIRST);
#pragma unroll 1
    for (unsigned spins = 0; spins < SPIN_CAP; ++spins) {
        const u64 v = __hip_atomic_load(p, RLX_AGENT);
        if (__all(v == tag)) break;
        __builtin_amdgcn_s_sleep(GAP);
    }
    __builtin_amdgcn_fence(__ATOMIC_ACQUIRE, "wavefront");
    asm volatile("" ::: "memory");
}

DI void softmax16(const f32x16& p, bf16x8 (&out)[2]) {
#define KEEP1(x) asm("" : "+v"(x))
    const float nm = -lmax16(p) * LOG2E;
    float e[16], t[8];
#pragma unroll
    for (int i = 0; i < 16; ++i) { e[i] = __builtin_fmaf(p[i], LOG2E, nm); KEEP1(e[i]); e[i] = EXP2(e[i]); }
#pragma unroll
    for (int i = 0; i < 8; ++i) { t[i] = e[2 * i] + e[2 * i + 1]; KEEP1(t[i]); }
#pragma unroll
    for (int i = 0; i < 4; ++i) { t[i] = t[2 * i] + t[2 * i + 1]; KEEP1(t[i]); }
    t[0] += t[1]; KEEP1(t[0]); t[2] += t[3]; KEEP1(t[2]);
    const float rs = __builtin_amdgcn_rcpf(t[0] + t[2]);
    u32x4 u[2];
#pragma unroll
    for (int i = 0; i < 16; ++i) { e[i] *= rs; KEEP1(e[i]); }
#pragma unroll
    for (int i = 0; i < 8; ++i) u[i >> 2][i & 3] = pk2(e[2 * i], e[2 * i + 1]);
    out[0] = __builtin_bit_cast(bf16x8, u[0]); out[1] = __builtin_bit_cast(bf16x8, u[1]);
}

__global__ __launch_bounds__(768, 3) void fused_kernel(const uint16_t* __restrict__ x, const uint16_t* __restrict__ WA, const uint16_t* __restrict__ WB,
                                                    const uint16_t* __restrict__ wphi, const uint16_t* __restrict__ gatew,
                                                    const uint16_t* __restrict__ woA, const uint16_t* __restrict__ woB,
                                                    uint16_t* __restrict__ out, char* ws) {
    __shared__ __attribute__((aligned(16))) char lds[LDS_BYTES];
    LAS char* L = (LAS char*)lds;
    u64 pc0; asm volatile("s_getpc_b64 %0" : "=s"(pc0));
    const unsigned lds0 = (unsigned)(uintptr_t)lds;
    const int tid = threadIdx.x, lane = tid & 63, w = __builtin_amdgcn_readfirstlane(tid >> 6);
    const int r32 = lane & 31, h = lane >> 5;
    const int xcd = blockIdx.x & 7, idx = blockIdx.x >> 3;
    const int qd = 2 * (xcd & 3) + (idx & 1), tl = 2 * (idx >> 1) + (xcd >> 2);
    const int s0 = ((tl >> 1) + 4 * (qd & 1)) & 7;
    const int otl = 4 * xcd + (idx >> 3), ocg = idx & 7;
    const rsrc_t rs = __builtin_amdgcn_make_buffer_rsrc(ws, 0, WS_END, 0x00020000);
    u64 tag;
#define EPOCH_BAR(N) do { asm volatile("s_load_dwordx2 %0, %1, 0x0\n\ts_waitcnt vmcnt(" #N ") lgkmcnt(0)\n\ts_barrier" : "=s"(tag) : "s"(ws + WS_EP) : "memory"); tag += 1; } while (0)
    const int q4 = (lane & 15) >> 2;
    const int trl = ((lane >> 4) & 1) * 32 + (lane & 3) * 8;
    asm volatile("" :: "s"(x), "s"(WA), "s"(WB), "s"(wphi), "s"(gatew), "s"(ws));
    if (__builtin_expect(w >= 8, 1)) {
        const int j = w - 8;
        const unsigned vsw = (unsigned)(((lane & 7) ^ (((lane >> 4) & 1) << 2)) << 4);
        unsigned vw[4];
#pragma unroll
        for (int u = 0; u < 4; ++u) vw[u] = (unsigned)((lane >> 3) * 3072) + vsw + u * (8 * 3072 - 1024);
        const unsigned vx0 = (unsigned)((lane >> 3) * 1024 + (((lane & 7) ^ (lane >> 4)) << 4)), vx1 = (vx0 ^ 64u) + 8192 - 1024;
        const char* xb = (const char*)x + (size_t)(64 * tl + 16 * j) * 1024;
        const size_t wo_ = (size_t)((j & 1) * 32) * 3072 + qd * 128;
        const char* wb[3] = {(const char*)WA + wo_ + 1024 + (j >> 1) * 1024, (const char*)WB + wo_ + (j >> 1) * 1024, (j >> 1) ? (const char*)WB + wo_ + 2048 : (const char*)WA + wo_};
        auto issue_x = [&](int i) { const int kc = (s0 + i) & 7; glds2(vx0, vx1, xb + kc * 128, lds0 + L_X + i * 8192 + j * 2048); };
        auto issue_w = [&](int i) { glds4(vw, wb[i >> 3] + ((s0 + i) & 7) * (64 * 3072), lds0 + L_RING + ((i + 4) % 6) * 16384 + j * 4096); };
        __builtin_amdgcn_s_setprio(3);
        issue_w(0); issue_x(0); issue_x(1); issue_x(2); issue_x(3);
        bf16x8 gw; unsigned warm;
        asm volatile("global_load_dwordx4 %0, %1, %2" : "=v"(gw) : "v"((lane & 15) * 64 + (lane >> 4) * 16), "s"(gatew) : "memory");
        glds2(lane * 16, lane * 16, wphi + qd * 1024, lds0 + L_WPHI);
        issue_w(1);
        int laneh = lane; asm volatile("" : "+v"(laneh));
        const int r32h = laneh & 31, hh = laneh >> 5, q4h = (laneh & 15) >> 2, trlh = ((laneh >> 4) & 1) * 32 + (laneh & 3) * 8;
        const unsigned vswh = (unsigned)(((laneh & 7) ^ (((laneh >> 4) & 1) << 2)) << 4);
        const int head = 4 * qd + j;
        const bool keep = (r32h >> 4) == (j & 1);
        constexpr unsigned OOB = 0x40000000u;
        unsigned a_ve[4], a_pf[2], a_kf[4], vo[4];
#pragma unroll
        for (int u = 0; u < 4; ++u) {
            a_ve[u] = keep ? L_VB + (j >> 1) * 4096 + u * 1024 + laneh * 16 : L_ONES;
            const unsigned n = u * 136 + ((r32h < 16 ? r32h : 16) * 2 + hh) * 4;
            a_kf[u] = (n >> 5) * 128 + (n & 31) * 2;
            vo[u] = (unsigned)((laneh >> 3) * 1024) + vswh + u * (8192 - 1024);
            asm volatile("" : "+v"(a_ve[u]), "+v"(a_kf[u]), "+v"(vo[u]));
        }
        a_pf[0] = L_PHK + j * 4096 + (4 * hh + q4h) * 64 + (trlh ^ ((2 * hh + (q4h >> 1)) * 8)); a_pf[1] = (a_pf[0] ^ 32) + 512;
        unsigned a_l16 = laneh * 16, a_q = L_PHQ + j * 4096 + laneh * 16, a_g2 = L_G2 + r32h * 4;
        unsigned v_pk = (keep || (r32h & 15) == 0) ? (unsigned)(((keep ? (r32h & 15) : 16) * 2 + hh) * 16) : OOB;
        unsigned v_one = laneh == 0 ? 0u : OOB, v_p32 = r32h * 8, v_p17 = (laneh < 17 ? laneh : 0) * 8, v_p8 = (laneh & 7) * 8;
        unsigned v_h1 = hh * 8704 + r32h * 4, v_h1s = (laneh < 32 && !(laneh & 1)) ? laneh * 2 : OOB, v_tch = ((laneh >> 3) & 1) * 32768 + (laneh & 7) * 128;
        bf16x8 none = {-1, -1, -1, -1, -1, -1, -1, -1};
        asm volatile("" : "+v"(a_pf[0]), "+v"(a_pf[1]), "+v"(a_l16), "+v"(a_q), "+v"(a_g2), "+v"(v_pk), "+v"(v_one), "+v"(v_p32), "+v"(v_p17), "+v"(v_p8));
        asm volatile("" : "+v"(v_h1), "+v"(v_h1s), "+v"(v_tch), "+v"(none));
        VM_BAR(13); issue_w(2); issue_w(3);
        VM_BAR(8); issue_x(4); issue_w(4);
        EPOCH_BAR(10); issue_x(5); issue_w(5);
        VM_BAR(12); issue_x(6); issue_w(6);
        VM_BAR(12); issue_x(7); issue_w(7);
        VM_BAR(12); issue_w(8); issue_w(9);
        asm volatile("global_load_dword %0, %1, %2" : "=v"(warm) : "v"(((tl >> 1) * 32 + (lane & 15)) * 3072), "s"(wb[2] - (j & 1) * 16 * 3072) : "memory");
        VM_BAR(15);
        VM_BAR(9); issue_w(10); issue_w(11);
        u32x2 tagv = {(unsigned)tag, (unsigned)(tag >> 32)};
        asm volatile("" : "+v"(tagv));
        f32x4 gl = {};
        const int grow = 16 * j + (lane & 15);
        int gxa = L_X + grow * 128 + (((lane >> 4) ^ ((grow >> 1) & 7)) << 4);
        u64 f1early = 0;
        float gt = 0.f;
        unsigned a_gt = lane < 16 ? L_G + grow * 4 : lane < 32 ? L_G2 + grow * 4 : L_ONES + (lane & 3) * 4;
        asm volatile("" : "+v"(a_gt));
#pragma unroll
        for (int i = 8; i < 24; i += 2) {
            if (i < 22) VM_BAR(8); else VM_BAR(0);
            if (i == 8) asm volatile("" : "+v"(gw), "+v"(warm));
            bf16x8 xg[4], ga[4];
            if (i < 16) {
#pragma unroll
                for (int u = 0; u < 4; ++u) {
                    xg[u] = ldsfrag(L + (gxa ^ ((u & 1) << 6)) + (i - 8 + (u >> 1)) * 8192);
                    ga[u] = (lane & 15) == 2 * ((s0 + i + (u >> 1)) & 7) + (u & 1) ? gw : bf16x8{};
                }
            }
            SBAR();
            if (i < 20) { issue_w(i + 4); issue_w(i + 5); }
            SBAR();
            if (i < 16) {
#pragma unroll
                for (int u = 0; u < 4; ++u) gl = __builtin_amdgcn_mfma_f32_16x16x32_bf16(ga[u], xg[u], gl, 0, 0, 0);
            }
            if (i == 22) {
                float z = (gl[0] + gl[1]) + (gl[2] + gl[3]);
                z += __shfl_xor(z, 16); z += __shfl_xor(z, 32);
                gt = __builtin_amdgcn_rcpf(1.f + EXP2(-z * LOG2E));
                gt = lane < 16 ? gt : lane < 32 ? 1.f - gt : __uint_as_float(0x3F803F80u);
                asm volatile("" : "+v"(gt));
            }
            if (i == 22) *(LAS float*)(L + a_gt) = gt;
            if (j == 0 && i == 20) {
                f1early = __hip_atomic_load((GAS u64*)(ws + WS_F1 + qd * 256) + (lane & 31), RLX_AGENT);
                const bool seen = __all(f1early == tag);
                if (lane < 36) *(LAS unsigned*)(L + L_SYNC + lane * 4) = lane >= 32 ? 0x3F803F80u : (lane == 0 && seen) ? 1u : 0u;
            }
            if (j == 0 && i == 22 && !__all(f1early == tag)) {
                wait_flags<0, 2>(ws, WS_F1 + qd * 256, 32, tag, lane);
                *(LAS volatile unsigned*)(L + L_SEEN) = 1;
            }
            SBAR();
        }
        auto publish_flag = [&](unsigned off) { DRAIN(); __builtin_amdgcn_raw_buffer_store_b64(tagv, rs, v_one, off, 16); };
        BAR();
        const u64 tp2 = __builtin_amdgcn_s_memtime();
        __builtin_amdgcn_s_sleep(6);
        bf16x8 phq[2][2]; float g2[2];
        {
            bf16x8 pf[4], ve[4];
#pragma unroll
            for (int u = 0; u < 4; ++u) {
                pf[u] = __builtin_shufflevector(trread(L + a_pf[0] + u * 1024), trread(L + a_pf[1] + u * 1024), 0, 1, 2, 3, 4, 5, 6, 7);
                ve[u] = ldsfrag(L + a_ve[u]);
                phq[u >> 1][u & 1] = ldsfrag(L + a_q + u * 1024);
            }
            g2[0] = *(LAS const float*)(L + a_g2); g2[1] = *(LAS const float*)(L + a_g2 + 128);
            lds_arrive(L + L_HCNT, lane);
            __builtin_amdgcn_s_sleep(PK_DELAY);
            f32x16 kv = MFMA(pf[0], ve[0], f32x16{});
#pragma unroll
            for (int u = 1; u < 4; ++u) kv = MFMA(pf[u], ve[u], kv);
#pragma unroll
            for (int g = 0; g < 4; ++g) st16(rs, __builtin_bit_cast(bf16x8, (f32x4){kv[4 * g], kv[4 * g + 1], kv[4 * g + 2], kv[4 * g + 3]}), v_pk, WS_PK + ((qd * 32 + tl) * 4 + j) * 2176 + g * 544);
            publish_flag(WS_F2 + (head * 32 + tl) * 8);
        }
        lds_wait(L + L_HCNT, 4);
        int wo_done = 0;
        auto wo_service = [&]() {
            if (wo_done < 2 && __builtin_amdgcn_s_memtime() - tp2 >= (u64)(WO_T0 + WO_DT * (j + 4 * wo_done))) {
                const char* src = (const char*)(wo_done ? woA : woB) + (size_t)(128 * j) * 1024 + ocg * 128;
#pragma unroll
                for (int i = 0; i < 4; ++i) glds4(vo, src + i * 32768, lds0 + (wo_done ? L_WOA : L_WOB) + j * 16384 + i * 4096);
                ++wo_done;
            }
        };
        auto wait_slow = [&](unsigned off, unsigned v, int first) {
#pragma unroll 1
            for (unsigned spins = 0; spins < SPIN_CAP; ++spins) {
#pragma unroll 1
                for (int z = 0; z < (spins ? POLL_GAP : first); ++z) { wo_service(); __builtin_amdgcn_s_sleep(6); }
                asm volatile("" ::: "memory");
                if (__all(__builtin_bit_cast(u64, __builtin_amdgcn_raw_buffer_load_b64(rs, v, off, 16)) == tag)) break;
            }
            __builtin_amdgcn_fence(__ATOMIC_ACQUIRE, "wavefront");
            asm volatile("" ::: "memory");
        };
        if (tl < 17) {
            wait_slow(WS_F2 + head * 256, v_p32, POLL_FIRST);
            float t[16];
#pragma unroll
            for (int i = 0; i < 16; ++i) t[i] = __uint_as_float(__builtin_amdgcn_raw_buffer_load_b32(rs, v_h1, WS_PK + (qd * 128 + j) * 2176 + tl * 128 + i * 17408, 16));
#pragma unroll
            for (int n = 8; n >= 1; n >>= 1)
#pragma unroll
                for (int i = 0; i < n; ++i) { t[i] += t[i + n]; KEEP1(t[i]); }
            auto r = __builtin_amdgcn_permlane32_swap(__float_as_uint(t[0]), __float_as_uint(t[0]), false, false);
            const float x0 = __uint_as_float(r[0]) + __uint_as_float(r[1]);
            const float x1 = __uint_as_float(__builtin_amdgcn_update_dpp(0, __float_as_uint(x0), 0xF5, 0xF, 0xF, true));
            __builtin_amdgcn_raw_buffer_store_b32(pk2(x0, x1), rs, v_h1s, WS_KVF + head * 2176 + tl * 128, 16);
            publish_flag(WS_F3 + (head * 32 + tl) * 8);
        }
        wait_slow(WS_F3 + head * 256, v_p17, tl < 17 ? POLL_FIRST : 2 * POLL_FIRST + 4);
        {
            u32x2 kq_[4];
#pragma unroll
            for (int g = 0; g < 4; ++g) kq_[g] = __builtin_amdgcn_raw_buffer_load_b64(rs, a_kf[g], WS_KVF + head * 2176, 16);
            const bf16x8 k0 = __builtin_bit_cast(bf16x8, __builtin_shufflevector(kq_[0], kq_[1], 0, 1, 2, 3)), k1 = __builtin_bit_cast(bf16x8, __builtin_shufflevector(kq_[2], kq_[3], 0, 1, 2, 3));
#pragma unroll
            for (int tt = 0; tt < 2; ++tt) {
                f32x16 ob = MFMA(k0, phq[tt][0], f32x16{});
                ob = MFMA(k1, phq[tt][1], ob);
                const float rd = g2[tt] * __builtin_amdgcn_rcpf(ob[8] + 1e-6f);
#pragma unroll
                for (int i = 0; i < 8; ++i) { float z = ob[i] * rd; KEEP1(z); ob[i] = z; }
                st16(rs, pack8(ob, 0), a_l16, WS_O + (2u << 20) + ((2 * tl + tt) * 32 + head) * 1024);
                st16(rs, none, a_l16, WS_O + ((2 * tl + tt) * 32 + head) * 1024);
            }
            publish_flag(WS_F5 + (tl * 32 + head) * 8);
        }
        wait_slow(WS_F5 + (otl * 32 + 8 * j) * 8, v_p8, POLL_FIRST);
        touch(v_tch, ws + WS_O + (2u << 20) + (2 * otl * 32 + 8 * j + ocg) * 1024, lds0 + L_DUMMY);
        while (wo_done < 2) { wo_service(); __builtin_amdgcn_s_sleep(6); }
        DRAIN();
        lds_arrive(L + L_OBS, lane);
        return;
    }

    if (w == 0) {
        const char* a = (const char*)(pc0 & ~127ull) + (idx * 9 + lane < CODE_LINES ? idx * 9 + lane : 0) * 128;
        if (lane == 9) {
            a = ws + (size_t)(idx < 21 ? idx : 0) * 524288;
            if (idx >= 22) a = idx < 25 ? (const char*)WB + (idx - 22) * 786368 : idx < 28 ? (const char*)out + (idx - 25) * 1048512 : idx < 30 ? (const char*)woA + (idx - 28) * 524224 : (const char*)woB + (idx - 30) * 524224;
        }
        unsigned keep;
        if (lane < 10) asm volatile("s_mov_b32 %0, m0\n\ts_mov_b32 m0, %2\n\ts_nop 0\n\tglobal_load_lds_dword %1, off sc1\n\ts_mov_b32 m0, %0" : "=&s"(keep) : "v"(a), "s"(lds0 + L_WPHI + 4096) : "memory");
    }
    const int p = w & 1;
    const int hq = w >> 1, kh = w & 1, head = 4 * qd + hq;
    const unsigned vK = w * 2048 + lane * 16;
    const unsigned vV = (unsigned)(w * 2048 + 1024 + h * 256 + (r32 & 15) * 16);
    const unsigned kvb = WS_KV + qd * 32 * BLK;
#define KVOFF(t) (kvb + (((t) + tl) & 31) * BLK)
    bf16x8 kr[4], v0r[4], v1r[4];
    const int kpos = ((lane & 0x33) | ((lane & 4) << 1) | ((lane & 8) >> 1)) * 16;
    bf16x8 kref;
    {
        const int xa0 = L_X + r32 * 128 + ((h ^ ((r32 >> 1) & 7)) << 4);
        const int wa0 = L_RING + ((w >> 1) & 1) * 8192 + (8 * h + q4) * 128 + ((p ^ (q4 >> 1)) * 64) + trl;
        bf16x8 xf[2][4][2], w0[2][4];
        f32x16 acc[2];
        int wsb[3], xsb[4];
#pragma unroll
        for (int i = 0; i < 3; ++i) { wsb[i] = wa0 + i * 32768; asm volatile("" : "+v"(wsb[i])); }
#pragma unroll
        for (int s = 0; s < 4; ++s) { xsb[s] = xa0 ^ (s << 5); asm volatile("" : "+v"(xsb[s])); }
        auto gemm = [&](int TEAM, int TYPE) __attribute__((always_inline)) {
            auto mine = [&](int i) __attribute__((always_inline)) { return i >= 0 && i < 24 && ((i >> 3) & 1) == TEAM; };
            auto rx = [&](int i, int s, int t) __attribute__((always_inline)) { if (mine(i)) xf[i & 1][s][t] = ldsfrag(L + xsb[s] + (i & 7) * 8192 + t * 4096); };
            auto rw = [&](int i, int s) __attribute__((always_inline)) { if (mine(i)) w0[i & 1][s] = trfrag(L + wsb[((i + 4) % 6) >> 1] + (i & 1) * 16384 + s * 2048, 512); };
            auto mm = [&](int i, int s, int t, bool now = false) __attribute__((always_inline)) {
                SBAR();
                if (mine(i) && (i != 7 || now)) {
                    const f32x16 c = ((i & 7) == 0 && s == 0) ? f32x16{} : acc[t];
                    acc[t] = (TEAM == 0 && TYPE) ? MFMA(xf[i & 1][s][t], w0[i & 1][s], c) : MFMA(w0[i & 1][s], xf[i & 1][s][t], c);
                }
                SBAR();
            };
            bf16x8 own[2][2], wf[2], ph[4][2];
            f32x16 f[4];
            static_for<0, 25>([&](auto ic) __attribute__((always_inline)) {
                constexpr int i = ic.value;
                if (i == F1_STEP) { VM_BAR(0); if (tid == 0) set_flag(ws, WS_F1 + (qd * 32 + tl) * 8, tag); }
                else if (i == 0) EPOCH_BAR(63);
                else if (i == 2) { __builtin_amdgcn_s_waitcnt(0x0F70); BAR(); }
                else if (i < 8 || (i < 24 && !(i & 1))) BAR();
                mm(i - 1, 0, 0); rw(i, 0); rx(i, 0, 0);
                mm(i - 1, 0, 1); rx(i, 0, 1); rw(i, 1);
                mm(i - 1, 1, 0); rx(i, 1, 0); rx(i, 1, 1);
                mm(i - 1, 1, 1); rw(i, 2); rx(i, 2, 0);
                mm(i - 1, 2, 0); rx(i, 2, 1); rw(i, 3);
                mm(i - 1, 2, 1); rx(i, 3, 0); rx(i, 3, 1);
                mm(i - 1, 3, 0);
                if (TEAM == 0 && (i == 18 || i == 19)) {
#pragma unroll
                    for (int a = 0; a < 2; ++a)
                        *(LAS bf16x8*)(L + (TYPE ? L_VA + ((p * 2 + (i & 1)) * 2 + a) * 1024 + lane * 16 : L_KA + ((2 * p + a) * 2 + (i & 1)) * 1024 + kpos)) = own[i & 1][a];
                }
                mm(i - 1, 3, 1);
                if (TEAM == 0 && i == 7) {
#pragma unroll
                    for (int z = 0; z < 8; ++z) mm(7, z >> 1, z & 1, true);
#pragma unroll
                    for (int t = 0; t < 2; ++t) {
                        if (TYPE == 0) {
#pragma unroll
                            for (int e = 0; e < 16; ++e) acc[t][e] *= 0.25f * LOG2E;
                        }
                        own[t][0] = pack8(acc[t], 0); own[t][1] = pack8(acc[t], 1);
                        asm volatile("" : "+v"(own[t][0]), "+v"(own[t][1]));
                    }
                }
                if (TEAM == 0 && i == 8) {
                    __builtin_amdgcn_s_sleep(PUB_DELAY);
#pragma unroll
                    for (int t = 0; t < 2; ++t) {
                        const unsigned so = kvb + tl * BLK + (4 * p + t) * 2048;
#pragma unroll
                        for (int a = 0; a < 2; ++a) {
                            if (TYPE == 0) st16(rs, own[t][a], kpos, so + a * 4096);
                            else st16(rs, own[t][a], (unsigned)((r32 >> 4) * 4096 + 1024 + h * 256 + (r32 & 15) * 16) + a * 512, so);
                        }
                    }
                }
                if (TEAM == 1) {
                    if (i == 1) {
                        const short sg = (lane & 4) ? (short)0x8000 : (short)0;
#pragma unroll
                        for (int a = 0; a < 2; ++a) {
                            wf[a] = trfrag(L + L_WPHI + (2 * p + a) * 512 + (4 * h + q4) * 32 + (((lane >> 4) & 1) * 2 + ((lane >> 1) & 1)) * 8, 256);
#pragma unroll
                            for (int e = 0; e < 8; ++e) wf[a][e] ^= sg;
                            asm volatile("" : "+v"(wf[a]));
                        }
                    }
                    if (i == 16) {
                        __builtin_amdgcn_s_setprio(1);
#pragma unroll
                        for (int m = 0; m < 4; ++m) f[m] = MFMA(wf[m >> 1], pack8(acc[m & 1], m >> 1), f32x16{});
                    }
                    if (i == 22) {
                        lds_wait(L + L_SEEN, 1);
                        touch(((tl + 1 + ((lane >> 4) & 1)) & 31) * BLK + (w - 4) * 2048 + (lane & 15) * 128, ws + kvb, lds0 + L_DUMMY);
                    }
                    if (i >= 16 && i < 24 && !(i & 1)) { softmax16(f[(i - 16) / 2], ph[(i - 16) / 2]); asm volatile("" : "+v"(ph[(i - 16) / 2][0]), "+v"(ph[(i - 16) / 2][1])); }
                }
                if (i == 24) {
                    kref = ldsfrag(L + L_KA + (hq * 2) * 1024 + lane * 16);
                    kr[0] = ldsfrag(L + L_KA + (hq * 2 + kh) * 1024 + lane * 16);
                    const int va = L_VA + ((hq >> 1) * 2 + kh) * 2048 + (h * 32 + (hq & 1) * 16 + r32) * 16;
                    v0r[0] = ldsfrag(L + (r32 < 16 ? va : L_ONES)); v1r[0] = ldsfrag(L + (r32 < 16 ? va + 1024 : L_ONES));
                }
                SBAR();
            });
            if (TEAM == 0) {
#pragma unroll
                for (int t = 0; t < 2; ++t)
#pragma unroll
                    for (int a = 0; a < 2; ++a)
                        *(LAS bf16x8*)(L + (TYPE ? L_VB + ((p * 2 + t) * 2 + a) * 1024 : L_QA + ((2 * p + a) * 2 + t) * 1024) + lane * 16) = pack8(acc[t], a);
            } else if (w < 6) {
#pragma unroll
                for (int m = 0; m < 8; ++m) *(LAS bf16x8*)(L + L_PHQ + (8 * p + m) * 1024 + lane * 16) = ph[m >> 1][m & 1];
            } else {
#pragma unroll
                for (int m = 0; m < 8; ++m) {
                    const u32x4 u = __builtin_bit_cast(u32x4, ph[m >> 1][m & 1]);
#pragma unroll
                    for (int g = 0; g < 2; ++g)
                        *(LAS u32x2*)(L + L_PHK + (4 * p + (m >> 1)) * 2048 + r32 * 64 + 8 * ((2 * (2 * (m & 1) + g) + h) ^ ((r32 >> 1) & 7))) = (u32x2){u[2 * g], u[2 * g + 1]};
                }
            }
        };
        if (w < 2) gemm(0, 0); else if (w < 4) gemm(0, 1); else gemm(1, 0);
    }
    const u32x4 rs4 = {(unsigned)(uintptr_t)ws, (unsigned)((uintptr_t)ws >> 32) & 0xFFFFu, WS_END, 0x00020000u};
#define LDK(R, t) asm volatile("buffer_load_dwordx4 %0, %1, %2, %3 offen sc1" : "=v"(R) : "v"(vK), "s"(rs4), "s"(KVOFF(t)))
#define LDV(R, t, S) asm volatile("s_mov_b64 exec, %4\n\tbuffer_load_dwordx4 %0, %1, %2, %3 offen offset:" #S " sc1\n\ts_mov_b64 exec, -1" : "+v"(R) : "v"(vV), "s"(rs4), "s"(KVOFF(t)), "s"(0x0000FFFF0000FFFFull))
#define LD2(a, b, t) asm volatile("buffer_load_dwordx4 %0, %6, %8, %9 offen sc1\n\tbuffer_load_dwordx4 %1, %6, %8, %10 offen sc1\n\ts_mov_b64 exec, %11\n\t" \
    "buffer_load_dwordx4 %2, %7, %8, %9 offen sc1\n\tbuffer_load_dwordx4 %3, %7, %8, %9 offen offset:512 sc1\n\t" \
    "buffer_load_dwordx4 %4, %7, %8, %10 offen sc1\n\tbuffer_load_dwordx4 %5, %7, %8, %10 offen offset:512 sc1\n\ts_mov_b64 exec, -1" \
    : "=&v"(kr[a]), "=&v"(kr[b]), "+v"(v0r[a]), "+v"(v1r[a]), "+v"(v0r[b]), "+v"(v1r[b]) : "v"(vK), "v"(vV), "s"(rs4), "s"(KVOFF(t)), "s"(KVOFF((t) + 1)), "s"(0x0000FFFF0000FFFFull))
#define WAITR2(N, R, Q) asm volatile("s_waitcnt vmcnt(" #N ")" : "+v"(R), "+v"(Q))
#define WAITR(N, R) asm volatile("s_waitcnt vmcnt(" #N ")" : "+v"(R))
#pragma unroll
    for (int t = 1; t < 4; ++t) { v0r[t] = ldsfrag(L + L_ONES); v1r[t] = ldsfrag(L + L_ONES); asm volatile("" ::: "memory"); }
    f32x16 oa = {}, ob = {};
    bf16x8 ofb[8];
    BAR();
    const bf16x8 qa = ldsfrag(L + L_QA + (hq * 2 + (kh ^ 1)) * 1024 + lane * 16), qb = ldsfrag(L + L_QA + (hq * 2 + kh) * 1024 + lane * 16);
    const float m0 = lmax16(MFMA(kref, qa, f32x16{})), m1 = lmax16(MFMA(kref, qb, f32x16{}));
    const int ot = w & 1, kq = w >> 1;
    {
        f32x16 negm;
        const float nm = -max32(fmx(m0, m1));
#pragma unroll
        for (int i = 0; i < 16; ++i) negm[i] = nm;
        f32x16 sa = MFMA(kr[0], qa, negm), sb = {};
        SBAR();
        LDK(kr[1], 1); LDV(v0r[1], 1, 0); LDV(v1r[1], 1, 512);
#define E(S, i) S[i] = EXP2(S[i])
#define PHASE(S, M1, M2, M3, LD1, LD2, LD3)                                                       \
    do {                                                                                          \
        E(S, 0); E(S, 1); SBAR();                                                                 \
        M1; LD1; SBAR();                                                                          \
        E(S, 2); E(S, 3); E(S, 4); E(S, 5); SBAR();                                               \
        M2; LD2; SBAR();                                                                          \
        E(S, 6); E(S, 7); E(S, 8); E(S, 9); SBAR();                                               \
        M3; LD3; SBAR();                                                                          \
        E(S, 10); E(S, 11); E(S, 12); E(S, 13); E(S, 14); E(S, 15); SBAR();                       \
    } while (0)
        const unsigned obo = WS_O + (2u << 20) + ((2 * otl + ot) * 32 + 8 * kq) * 1024 - w * 2048;
        auto handoff = [&]() {
            LAS char* EXW = L + L_EX + w * 2304 + lane * 16;
            *(LAS f32x4*)EXW = (f32x4){oa[0], oa[1], oa[2], oa[3]};
            *(LAS f32x4*)(EXW + 1024) = (f32x4){oa[4], oa[5], oa[6], oa[7]};
            *(LAS float*)(L + L_EX + w * 2304 + 2048 + lane * 4) = oa[8];
            asm volatile("" ::: "memory");
            *(LAS volatile unsigned*)(L + L_PAIR + w * 4) = 1;
        };
        const int psw = w < 4 ? PRIO_SWITCH : -1;
        if (w < 4) __builtin_amdgcn_s_setprio(2); else __builtin_amdgcn_s_setprio(1);
#define PSW(t, u) if (u == (PRIO_SWITCH & 3)) asm volatile("s_cmp_lg_u32 %0, %1\n\ts_cbranch_scc1 1f\n\ts_setprio 0\n1:" :: "s"(t), "s"(psw) : "scc")
#define PAIR(t, u)                                                                                                  \
        PSW(t, u);                                                                                                                                                      \
        PHASE(sa, ob = PV(v1r[(u + 3) & 3], sb, 1, ob), sb = MFMA(kr[u], qb, negm), WAITR(9, v0r[u]); oa = PV(v0r[u], sa, 0, oa), SBAR(); LD2(u ^ 2, u ^ 3, (t) + 2), (void)0, (void)0); \
        PHASE(sb, WAITR2(8, v1r[u], kr[u + 1]); oa = PV(v1r[u], sa, 1, oa), sa = MFMA(kr[u + 1], qa, negm), ob = PV(v0r[u], sb, 0, ob), (void)0, (void)0, (void)0);     \
        PSW((t) + 1, u + 1);                                                                                                                                            \
        PHASE(sa, ob = PV(v1r[u], sb, 1, ob), sb = MFMA(kr[u + 1], qb, negm), WAITR(7, v0r[u + 1]); oa = PV(v0r[u + 1], sa, 0, oa), (void)0, (void)0, (void)0);         \
        PHASE(sb, WAITR(6, v1r[u + 1]); oa = PV(v1r[u + 1], sa, 1, oa), WAITR(5, kr[u ^ 2]); sa = MFMA(kr[u ^ 2], qa, negm), ob = PV(v0r[u + 1], sb, 0, ob), (void)0, (void)0, (void)0)
#pragma unroll 1
        for (int t0 = 0; t0 < 28; t0 += 4) {
            PAIR(t0, 0);
            PAIR(t0 + 2, 2);
        }
        PAIR(28, 0);
#define OFB(k) ofb[k] = ld16(rs, vK, obo + (k) * 1024)
        asm volatile("s_waitcnt vmcnt(0)" : "+v"(v0r[2]), "+v"(v1r[2]), "+v"(kr[3]), "+v"(v0r[3]), "+v"(v1r[3]));
        lds_wait(L + L_OBS, 4);
        PHASE(sa, ob = PV(v1r[1], sb, 1, ob), sb = MFMA(kr[2], qb, negm), oa = PV(v0r[2], sa, 0, oa), OFB(0), OFB(1), OFB(2));
        PHASE(sb, oa = PV(v1r[2], sa, 1, oa), sa = MFMA(kr[3], qa, negm), ob = PV(v0r[2], sb, 0, ob), OFB(3), (void)0, (void)0);
        PHASE(sa, ob = PV(v1r[2], sb, 1, ob), sb = MFMA(kr[3], qb, negm), oa = PV(v0r[3], sa, 0, oa), OFB(4), OFB(5), OFB(6));
        PHASE(sb, oa = PV(v1r[3], sa, 1, oa), (void)0, ob = PV(v0r[3], sb, 0, ob), OFB(7), (void)0, handoff());
        ob = PV(v1r[3], sb, 1, ob);
#undef PHASE
#undef E
    }
    __builtin_amdgcn_s_setprio(0);
    int lane_ = __builtin_amdgcn_mbcnt_hi(~0u, __builtin_amdgcn_mbcnt_lo(~0u, 0u));
    asm volatile("" : "+v"(lane_));
    const int r32_ = lane_ & 31, h_ = lane_ >> 5, q4_ = (lane_ & 15) >> 2, trl_ = ((lane_ >> 4) & 1) * 32 + (lane_ & 3) * 8;
    const unsigned oao = WS_O + ((2 * otl + ot) * 32 + 8 * kq) * 1024, vp = lane_ < 8 ? (unsigned)(lane_ * 1024 + 1020) : 0x40000000u;
    auto probe = [&]() { return (unsigned)__ballot(__builtin_amdgcn_raw_buffer_load_b32(rs, vp, oao, 16) != 0xFFFFFFFFu); };
    const unsigned pr0 = __builtin_amdgcn_raw_buffer_load_b32(rs, vp, oao, 16);
    SBAR();
    const int wpo = kq * 16384 + (4 * h_ + q4_) * 128 + trl_;
    bf16x8 wv[8][2];
    auto wreads = [&](int base, int k0, int k1) {
#pragma unroll
        for (int k = k0; k < k1; ++k)
#pragma unroll
            for (int nt = 0; nt < 2; ++nt) wv[k][nt] = trfrag(L + base + wpo + k * 2048 + ((nt ^ (q4_ >> 1)) * 64), 1024);
    };
    const float gate = *(LAS const float*)(L + L_G + (kh * 32 + r32_) * 4);
    lds_wait(L + L_PAIR + (w ^ 1) * 4, 1);
    {
        LAS const char* EXR = L + L_EX + (w ^ 1) * 2304 + lane_ * 16;
        const f32x4 e0 = *(LAS const f32x4*)EXR, e1 = *(LAS const f32x4*)(EXR + 1024);
        const float e2 = *(LAS const float*)(L + L_EX + (w ^ 1) * 2304 + 2048 + lane_ * 4);
        SBAR();
        wreads(L_WOB, 0, 4);
        SBAR();
        const float l = ob[8] + e2;
#pragma unroll
        for (int i = 0; i < 4; ++i) { ob[i] += e0[i]; ob[4 + i] += e1[i]; }
        float rl = __builtin_amdgcn_rcpf(l), t = fmx(l, rl) * 1.0e19f;
#pragma unroll
        for (int i = 0; i < 8; i += 2) t = fmx(fmx(t, __builtin_fabsf(ob[i])), __builtin_fabsf(ob[i + 1]));
        if (__builtin_expect(__any(!(t < 1.0e38f)), 0)) {
            ob = f32x16{};
            float m = -INFINITY;
            for (int t2 = 0; t2 < 64; ++t2) {
                const unsigned so = kvb + (t2 >> 1) * BLK, fl = (t2 & 1) * 2048;
                const bf16x8 ones = {0x3F80, 0x3F80, 0x3F80, 0x3F80, 0x3F80, 0x3F80, 0x3F80, 0x3F80};
                const bf16x8 kf = ld16(rs, vK ^ fl, so), v0l = ld16(rs, vV ^ fl, so), v1l = ld16(rs, vV ^ fl, so + 512);
                const bf16x8 v0 = r32_ < 16 ? v0l : ones, v1 = r32_ < 16 ? v1l : ones;
                f32x16 s = MFMA(kf, qb, f32x16{});
                const float mn = fmx(max16(s), m), al = EXP2(m - mn);
                m = mn;
#pragma unroll
                for (int i = 0; i < 16; ++i) { ob[i] *= al; s[i] = EXP2(s[i] - mn); }
                ob = PV(v0, s, 0, ob);
                ob = PV(v1, s, 1, ob);
            }
            rl = __builtin_amdgcn_rcpf(ob[8]);
        }
        rl *= gate;
#pragma unroll
        for (int i = 0; i < 8; ++i) ob[i] *= rl;
        st16(rs, __builtin_bit_cast(bf16x8, __builtin_elementwise_min(__builtin_bit_cast(u32x4, pack8(ob, 0)), (u32x4){0xFFFFFFFEu, 0xFFFFFFFEu, 0xFFFFFFFEu, 0xFFFFFFFEu})),
             lane_ * 16, WS_O + ((2 * tl + kh) * 32 + head) * 1024);
        SBAR();
    }
    if (lane_ == 0) __hip_atomic_fetch_add((LAS unsigned*)(L + L_STORED), 1u, __ATOMIC_RELAXED, __HIP_MEMORY_SCOPE_WORKGROUP);
    f32x16 acc[2];
    f32x4 keep0 = {}, keep1 = {};
    {
        bf16x8 of[8];
        auto loads = [&](int g) {
#pragma unroll
            for (int k = 4 * g; k < 4 * g + 4; ++k) of[k] = ld16(rs, lane_ * 16 + (k & 3) * 1024, oao + (k & 4) * 1024);
        };
        const bf16x8 none = {-1, -1, -1, -1, -1, -1, -1, -1};
        unsigned rdy = (unsigned)__ballot(pr0 != 0xFFFFFFFFu);
#pragma unroll
        for (int g = 0; g < 2; ++g) { if (((rdy >> (4 * g)) & 15) == 15) loads(g); else of[4 * g] = none; }
        SBAR();
#pragma unroll
        for (int k = 0; k < 8; ++k) {
#pragma unroll
            for (int nt = 0; nt < 2; ++nt) acc[nt] = MFMA(wv[k][nt], ofb[k], k ? acc[nt] : f32x16{});
            SBAR();
            if (k < 4) wreads(L_WOB, k + 4, k + 5); else wreads(L_WOA, k - 4, k - 3);
            SBAR();
        }
        wreads(L_WOA, 4, 8);
        lds_arrive(L + L_PAIR + w * 4, lane_);
        SBAR();
        auto missing = [&](int g) {
            const u32x4 v = __builtin_elementwise_max(__builtin_elementwise_max(__builtin_bit_cast(u32x4, of[4 * g]), __builtin_bit_cast(u32x4, of[4 * g + 1])),
                                                      __builtin_elementwise_max(__builtin_bit_cast(u32x4, of[4 * g + 2]), __builtin_bit_cast(u32x4, of[4 * g + 3])));
            return (bool)__any(__builtin_elementwise_max(__builtin_elementwise_max(v[0], v[1]), __builtin_elementwise_max(v[2], v[3])) == 0xFFFFFFFFu);
        };
        bool have1 = ((rdy >> 4) & 15) == 15;
#pragma unroll
        for (int g = 0; g < 2; ++g) {
#pragma unroll 1
            for (unsigned spins = 0; spins < SPIN_CAP && missing(g); ++spins) {
                rdy = lds_word(L + L_STORED) >= 8 ? 0xFFu : probe();
                asm volatile("" ::: "memory");
                if (g == 0 && !have1 && ((rdy >> 4) & 15) == 15) { loads(1); have1 = true; }
                if (((rdy >> (4 * g)) & 15) == 15) loads(g); else __builtin_amdgcn_s_sleep(RETRY_SLEEP);
            }
            if (g == 0) {
#pragma unroll
                for (int k = 0; k < 4; ++k) { acc[0] = MFMA(wv[k][0], of[k], acc[0]); acc[1] = MFMA(wv[k][1], of[k], acc[1]); }
                lds_wait(L + L_PAIR + (w ^ 1) * 4, 2);
            }
        }
        LAS char* PR = L + L_WOB + kq * 16384 + ot * 8192 + lane_ * 16;
#pragma unroll
        for (int nt = 0; nt < 2; ++nt)
#pragma unroll
            for (int k = 4; k < 8; ++k) acc[nt] = MFMA(wv[k][nt], of[k], acc[nt]);
        SBAR();
#pragma unroll
        for (int z = 0; z < 4; ++z) {
            const int b = 8 * (z & 1);
            const f32x4 u0 = {acc[z >> 1][b], acc[z >> 1][b + 1], acc[z >> 1][b + 2], acc[z >> 1][b + 3]}, u1 = {acc[z >> 1][b + 4], acc[z >> 1][b + 5], acc[z >> 1][b + 6], acc[z >> 1][b + 7]};
            if (z != kq) { *(LAS f32x4*)(PR + z * 2048) = u0; *(LAS f32x4*)(PR + z * 2048 + 1024) = u1; } else { keep0 = u0; keep1 = u1; }
        }
    }
    BAR();
    {
        f32x4 r0 = keep0, r1 = keep1;
#pragma unroll
        for (int z = 1; z < 4; ++z) {
            LAS const char* src = L + L_WOB + ((kq + z) & 3) * 16384 + ot * 8192 + kq * 2048 + lane_ * 16;
            r0 += *(LAS const f32x4*)src; r1 += *(LAS const f32x4*)(src + 1024);
        }
        unsigned a0 = pk2(r0[0], r0[1]), a1 = pk2(r0[2], r0[3]), b0 = pk2(r1[0], r1[1]), b1 = pk2(r1[2], r1[3]);
        auto s0_ = __builtin_amdgcn_permlane32_swap(a0, b0, false, false);
        auto s1_ = __builtin_amdgcn_permlane32_swap(a1, b1, false, false);
        *(u32x4*)(out + (size_t)(64 * otl + 32 * ot + r32_) * 512 + 64 * ocg + 16 * kq + 8 * h_) = (u32x4){s0_[0], s1_[0], s0_[1], s1_[1]};
    }
    if (blockIdx.x == 0 && w == 0 && lane_ == 0) set_flag(ws, WS_EP, tag);
}

extern "C" void kernel_launch(void* const* d_in, const int* in_sizes, int n_in, void* d_out, int out_size, void* d_ws, size_t ws_size, hipStream_t stream) {
    fused_kernel<<<256, 768, 0, stream>>>((const uint16_t*)d_in[0], (const uint16_t*)d_in[1], (const uint16_t*)d_in[4], (const uint16_t*)d_in[5], (const uint16_t*)d_in[7],
                                          (const uint16_t*)d_in[2], (const uint16_t*)d_in[6], (uint16_t*)d_out, (char*)d_ws);
}
```

```cpp
#include <hip/hip_runtime.h>
#include <hip/hip_bf16.h>
#include <stdint.h>

typedef short bf16x8 __attribute__((ext_vector_type(8)));
typedef short s16x4 __attribute__((ext_vector_type(4)));
typedef float f32x16 __attribute__((ext_vector_type(16)));
typedef float f32x4 __attribute__((ext_vector_type(4)));
typedef float f32x2 __attribute__((ext_vector_type(2)));
typedef unsigned u32x4 __attribute__((ext_vector_type(4)));
typedef unsigned u32x2 __attribute__((ext_vector_type(2)));
typedef __bf16 bf16x2_t __attribute__((ext_vector_type(2)));
typedef unsigned long long u64;
#define LAS __attribute__((address_space(3)))
#define GAS __attribute__((address_space(1)))
#define DI __device__ __forceinline__
#define MFMA(a, b, c) __builtin_amdgcn_mfma_f32_32x32x16_bf16((a), (b), (c), 0, 0, 0)
typedef __bf16 bfx8 __attribute__((ext_vector_type(8)));
typedef __bf16 bfx16 __attribute__((ext_vector_type(16)));
#define PV(a, S, half, c) __builtin_amdgcn_smfmac_f32_32x32x32_bf16(__builtin_bit_cast(bfx8, a), \
    __builtin_bit_cast(bfx16, __builtin_shufflevector(S, S, 8 * half, 8 * half + 1, 8 * half + 2, 8 * half + 3, 8 * half + 4, 8 * half + 5, 8 * half + 6, 8 * half + 7)), (c), 0xDDDD, 0, 0)
#define SBAR() __builtin_amdgcn_sched_barrier(0)
#define RLX_AGENT __ATOMIC_RELAXED, __HIP_MEMORY_SCOPE_AGENT
#define EXP2(x) __builtin_amdgcn_exp2f(x)

constexpr float LOG2E = 1.4426950408889634f;
constexpr unsigned BLK = 16384;
constexpr unsigned WS_KV = 0;
constexpr unsigned WS_O = WS_KV + 256 * BLK;
constexpr unsigned WS_PK = WS_O + (4u << 20);
constexpr unsigned WS_KVF = WS_PK + 32 * 32 * 2176;
constexpr unsigned WS_F1 = WS_KVF + 32 * 2176;
constexpr unsigned WS_F2 = WS_F1 + 2048;
constexpr unsigned WS_F3 = WS_F2 + 8192;
constexpr unsigned WS_F5 = WS_F3 + 8192;
constexpr unsigned WS_EP = WS_F5 + 8192;
constexpr unsigned WS_END = WS_EP + 128;
constexpr int L_X = 0, L_RING = 65536;
constexpr int L_PHK = 65536, L_PHQ = 81920, L_KA = 0, L_VA = 8192, L_VB = 16384;
constexpr int L_WOA = 0, L_WOB = 65536;
constexpr int L_WPHI = L_RING + 3 * 16384;
constexpr int L_G = 131072, L_SYNC = L_G + 256, L_SEEN = L_SYNC, L_STORED = L_SYNC + 4, L_HCNT = L_SYNC + 8, L_OBS = L_SYNC + 12, L_PAIR = L_SYNC + 16, L_ZEROS = L_SYNC + 96, L_ONES = L_SYNC + 128, L_DUMMY = L_SYNC + 256, L_G2 = L_SYNC + 512;
constexpr int L_QA = L_G + 1024, L_EX = L_QA + 8192;
constexpr int LDS_BYTES = 163840;
constexpr int F1_STEP = 14, PRIO_SWITCH = 23, PK_DELAY = 24, WO_T0 = 6400, WO_DT = 270, POLL_GAP = 2, POLL_FIRST = 6, RETRY_SLEEP = 2, PUB_DELAY = 4;
constexpr int CODE_LINES = 262;
constexpr unsigned SPIN_CAP = 1u << 20;

template <int N> struct IC { static constexpr int value = N; };
template <int I, int N, class F> DI void static_for(F&& f) { if constexpr (I < N) { f(IC<I>{}); static_for<I + 1, N>(f); } }
DI unsigned pk2(float a, float b) { f32x2 v = {a, b}; return __builtin_bit_cast(unsigned, __builtin_convertvector(v, bf16x2_t)); }
DI bf16x8 pack8(const f32x16& x, int s) {
    u32x4 p = {pk2(x[8 * s], x[8 * s + 1]), pk2(x[8 * s + 2], x[8 * s + 3]), pk2(x[8 * s + 4], x[8 * s + 5]), pk2(x[8 * s + 6], x[8 * s + 7])};
    return __builtin_bit_cast(bf16x8, p);
}
DI float fmx(float a, float b) { return __builtin_elementwise_maximum(a, b); }
DI float max32(float v) { auto r = __builtin_amdgcn_permlane32_swap(__float_as_uint(v), __float_as_uint(v), false, false); return fmx(__uint_as_float(r[0]), __uint_as_float(r[1])); }
DI float lmax16(const f32x16& s) {
    const float a = fmx(fmx(s[0], s[1]), s[2]), b = fmx(fmx(s[3], s[4]), s[5]), c = fmx(fmx(s[6], s[7]), s[8]), d = fmx(fmx(s[9], s[10]), s[11]), e = fmx(fmx(s[12], s[13]), s[14]);
    return fmx(fmx(fmx(a, b), c), fmx(fmx(d, e), s[15]));
}
DI float max16(const f32x16& s) { return max32(lmax16(s)); }
DI void glds2(unsigned v0, unsigned v1, const void* sbase, unsigned ldsdst) {
    unsigned keep;
    asm volatile("s_mov_b32 %0, m0\n\ts_mov_b32 m0, %4\n\ts_nop 0\n\tglobal_load_lds_dwordx4 %1, %3\n\tglobal_load_lds_dwordx4 %2, %3 offset:1024\n\ts_mov_b32 m0, %0"
                 : "=&s"(keep) : "v"(v0), "v"(v1), "s"(sbase), "s"(ldsdst) : "memory");
}
DI void glds4(const unsigned (&v)[4], const void* sbase, unsigned ldsdst) {
    unsigned keep;
    asm volatile("s_mov_b32 %0, m0\n\ts_mov_b32 m0, %6\n\ts_nop 0\n\tglobal_load_lds_dwordx4 %1, %5\n\tglobal_load_lds_dwordx4 %2, %5 offset:1024\n\t"
                 "global_load_lds_dwordx4 %3, %5 offset:2048\n\tglobal_load_lds_dwordx4 %4, %5 offset:3072\n\ts_mov_b32 m0, %0"
                 : "=&s"(keep) : "v"(v[0]), "v"(v[1]), "v"(v[2]), "v"(v[3]), "s"(sbase), "s"(ldsdst) : "memory");
}
DI void touch(unsigned voff, const void* sbase, unsigned ldsdst) {
    unsigned keep;
    asm volatile("s_mov_b32 %0, m0\n\ts_mov_b32 m0, %3\n\ts_nop 0\n\tglobal_load_lds_dword %1, %2 sc1\n\ts_mov_b32 m0, %0"
                 : "=&s"(keep) : "v"(voff), "s"(sbase), "s"(ldsdst) : "memory");
}
typedef short v4i16_t __attribute__((ext_vector_type(4)));
DI s16x4 trread(LAS const char* p) { return __builtin_bit_cast(s16x4, __builtin_amdgcn_ds_read_tr16_b64_v4i16((LAS v4i16_t*)p)); }
DI bf16x8 trfrag(LAS const char* p, int d2) { s16x4 lo = trread(p), hi = trread(p + d2); return __builtin_shufflevector(lo, hi, 0, 1, 2, 3, 4, 5, 6, 7); }
DI bf16x8 ldsfrag(LAS const char* p) { return *(LAS const bf16x8*)p; }
#define BAR() asm volatile("s_waitcnt lgkmcnt(0)\n\ts_barrier" ::: "memory")
#define VM_BAR(N) asm volatile("s_waitcnt vmcnt(" #N ") lgkmcnt(0)\n\ts_barrier" ::: "memory")
#define DRAIN() asm volatile("s_waitcnt vmcnt(0)" ::: "memory")
typedef __amdgpu_buffer_rsrc_t rsrc_t;
DI bf16x8 ld16(rsrc_t r, unsigned voff, unsigned soff) { return __builtin_bit_cast(bf16x8, __builtin_amdgcn_raw_buffer_load_b128(r, voff, soff, 16)); }
DI void st16(rsrc_t r, bf16x8 v, unsigned voff, unsigned soff) { __builtin_amdgcn_raw_buffer_store_b128(__builtin_bit_cast(u32x4, v), r, voff, soff, 16); }
DI unsigned lds_word(LAS char* p) { return __builtin_amdgcn_readfirstlane(*(LAS volatile unsigned*)p); }
DI void lds_wait(LAS char* p, unsigned want) {
#pragma unroll 1
    for (unsigned spins = 0; spins < (SPIN_CAP << 6) && lds_word(p) < want; ++spins) __builtin_amdgcn_s_sleep(1);
    asm volatile("" ::: "memory");
}
DI void lds_arrive(LAS char* p, int lane) {
    asm volatile("s_waitcnt lgkmcnt(0)" ::: "memory");
    if (lane == 0) __hip_atomic_fetch_add((LAS unsigned*)p, 1u, __ATOMIC_RELAXED, __HIP_MEMORY_SCOPE_WORKGROUP);
}
DI void set_flag(char* ws, unsigned off, u64 tag) { __hip_atomic_store((GAS u64*)(ws + off), tag, RLX_AGENT); }
template <int FIRST, int GAP> DI void wait_flags(char* ws, unsigned off, int n, u64 tag, int lane) {
    GAS u64* p = (GAS u64*)(ws + off) + (lane < n ? lane : 0);
    if (FIRST) __builtin_amdgcn_s_sleep(FIRST);
#pragma unroll 1
    for (unsigned spins = 0; spins < SPIN_CAP; ++spins) {
        const u64 v = __hip_atomic_load(p, RLX_AGENT);
        if (__all(v == tag)) break;
        __builtin_amdgcn_s_sleep(GAP);
    }
    __builtin_amdgcn_fence(__ATOMIC_ACQUIRE, "wavefront");
    asm volatile("" ::: "memory");
}

DI void softmax16(const f32x16& p, bf16x8 (&out)[2]) {
#define KEEP1(x) asm("" : "+v"(x))
    const float nm = -lmax16(p) * LOG2E;
    float e[16], t[8];
#pragma unroll
    for (int i = 0; i < 16; ++i) { e[i] = __builtin_fmaf(p[i], LOG2E, nm); KEEP1(e[i]); e[i] = EXP2(e[i]); }
#pragma unroll
    for (int i = 0; i < 8; ++i) { t[i] = e[2 * i] + e[2 * i + 1]; KEEP1(t[i]); }
#pragma unroll
    for (int i = 0; i < 4; ++i) { t[i] = t[2 * i] + t[2 * i + 1]; KEEP1(t[i]); }
    t[0] += t[1]; KEEP1(t[0]); t[2] += t[3]; KEEP1(t[2]);
    const float rs = __builtin_amdgcn_rcpf(t[0] + t[2]);
    u32x4 u[2];
#pragma unroll
    for (int i = 0; i < 16; ++i) { e[i] *= rs; KEEP1(e[i]); }
#pragma unroll
    for (int i = 0; i < 8; ++i) u[i >> 2][i & 3] = pk2(e[2 * i], e[2 * i + 1]);
    out[0] = __builtin_bit_cast(bf16x8, u[0]); out[1] = __builtin_bit_cast(bf16x8, u[1]);
}

__global__ __launch_bounds__(768, 3) void fused_kernel(const uint16_t* __restrict__ x, const uint16_t* __restrict__ WA, const uint16_t* __restrict__ WB,
                                                    const uint16_t* __restrict__ wphi, const uint16_t* __restrict__ gatew,
                                                    const uint16_t* __restrict__ woA, const uint16_t* __restrict__ woB,
                                                    uint16_t* __restrict__ out, char* ws) {
    __shared__ __attribute__((aligned(16))) char lds[LDS_BYTES];
    LAS char* L = (LAS char*)lds;
    u64 pc0; asm volatile("s_getpc_b64 %0" : "=s"(pc0));
    const unsigned lds0 = (unsigned)(uintptr_t)lds;
    const int tid = threadIdx.x, lane = tid & 63, w = __builtin_amdgcn_readfirstlane(tid >> 6);
    const int r32 = lane & 31, h = lane >> 5;
    const int xcd = blockIdx.x & 7, idx = blockIdx.x >> 3;
    const int qd = 2 * (xcd & 3) + (idx & 1), tl = 2 * (idx >> 1) + (xcd >> 2);
    const int s0 = ((tl >> 1) + 4 * (qd & 1)) & 7;
    const int otl = 4 * xcd + (idx >> 3), ocg = idx & 7;
    const rsrc_t rs = __builtin_amdgcn_make_buffer_rsrc(ws, 0, WS_END, 0x00020000);
    u64 tag;
#define EPOCH_BAR(N) do { asm volatile("s_load_dwordx2 %0, %1, 0x0\n\ts_waitcnt vmcnt(" #N ") lgkmcnt(0)\n\ts_barrier" : "=s"(tag) : "s"(ws + WS_EP) : "memory"); tag += 1; } while (0)
    const int q4 = (lane & 15) >> 2;
    const int trl = ((lane >> 4) & 1) * 32 + (lane & 3) * 8;
    asm volatile("" :: "s"(x), "s"(WA), "s"(WB), "s"(wphi), "s"(gatew), "s"(ws));
    if (__builtin_expect(w >= 8, 1)) {
        const int j = w - 8;
        const unsigned vsw = (unsigned)(((lane & 7) ^ (((lane >> 4) & 1) << 2)) << 4);
        unsigned vw[4];
#pragma unroll
        for (int u = 0; u < 4; ++u) vw[u] = (unsigned)((lane >> 3) * 3072) + vsw + u * (8 * 3072 - 1024);
        const unsigned vx0 = (unsigned)((lane >> 3) * 1024 + (((lane & 7) ^ (lane >> 4)) << 4)), vx1 = (vx0 ^ 64u) + 8192 - 1024;
        const char* xb = (const char*)x + (size_t)(64 * tl + 16 * j) * 1024;
        const size_t wo_ = (size_t)((j & 1) * 32) * 3072 + qd * 128;
        const char* wb[3] = {(const char*)WA + wo_ + 1024 + (j >> 1) * 1024, (const char*)WB + wo_ + (j >> 1) * 1024, (j >> 1) ? (const char*)WB + wo_ + 2048 : (const char*)WA + wo_};
        auto issue_x = [&](int i) { const int kc = (s0 + i) & 7; glds2(vx0, vx1, xb + kc * 128, lds0 + L_X + i * 8192 + j * 2048); };
        auto issue_w = [&](int i) { glds4(vw, wb[i >> 3] + ((s0 + i) & 7) * (64 * 3072), lds0 + L_RING + ((i + 4) % 6) * 16384 + j * 4096); };
        __builtin_amdgcn_s_setprio(3);
        issue_w(0); issue_x(0); issue_x(1); issue_x(2); issue_x(3);
        bf16x8 gw; unsigned warm;
        asm volatile("global_load_dwordx4 %0, %1, %2" : "=v"(gw) : "v"((lane & 15) * 64 + (lane >> 4) * 16), "s"(gatew) : "memory");
        glds2(lane * 16, lane * 16, wphi + qd * 1024, lds0 + L_WPHI);
        issue_w(1);
        int laneh = lane; asm volatile("" : "+v"(laneh));
        const int r32h = laneh & 31, hh = laneh >> 5, q4h = (laneh & 15) >> 2, trlh = ((laneh >> 4) & 1) * 32 + (laneh & 3) * 8;
        const unsigned vswh = (unsigned)(((laneh & 7) ^ (((laneh >> 4) & 1) << 2)) << 4);
        const int head = 4 * qd + j;
        const bool keep = (r32h >> 4) == (j & 1);
        constexpr unsigned OOB = 0x40000000u;
        unsigned a_ve[4], a_pf[2], a_kf[4], vo[4];
#pragma unroll
        for (int u = 0; u < 4; ++u) {
            a_ve[u] = keep ? L_VB + (j >> 1) * 4096 + u * 1024 + laneh * 16 : L_ONES;
            const unsigned n = u * 136 + ((r32h < 16 ? r32h : 16) * 2 + hh) * 4;
            a_kf[u] = (n >> 5) * 128 + (n & 31) * 2;
            vo[u] = (unsigned)((laneh >> 3) * 1024) + vswh + u * (8192 - 1024);
            asm volatile("" : "+v"(a_ve[u]), "+v"(a_kf[u]), "+v"(vo[u]));
        }
        a_pf[0] = L_PHK + j * 4096 + (4 * hh + q4h) * 64 + (trlh ^ ((2 * hh + (q4h >> 1)) * 8)); a_pf[1] = (a_pf[0] ^ 32) + 512;
        unsigned a_l16 = laneh * 16, a_q = L_PHQ + j * 4096 + laneh * 16, a_g2 = L_G2 + r32h * 4;
        unsigned v_pk = (keep || (r32h & 15) == 0) ? (unsigned)(((keep ? (r32h & 15) : 16) * 2 + hh) * 16) : OOB;
        unsigned v_one = laneh == 0 ? 0u : OOB, v_p32 = r32h * 8, v_p17 = (laneh < 17 ? laneh : 0) * 8, v_p8 = (laneh & 7) * 8;
        unsigned v_h1 = hh * 8704 + r32h * 4, v_h1s = (laneh < 32 && !(laneh & 1)) ? laneh * 2 : OOB, v_tch = ((laneh >> 3) & 1) * 32768 + (laneh & 7) * 128;
        bf16x8 none = {-1, -1, -1, -1, -1, -1, -1, -1};
        asm volatile("" : "+v"(a_pf[0]), "+v"(a_pf[1]), "+v"(a_l16), "+v"(a_q), "+v"(a_g2), "+v"(v_pk), "+v"(v_one), "+v"(v_p32), "+v"(v_p17), "+v"(v_p8));
        asm volatile("" : "+v"(v_h1), "+v"(v_h1s), "+v"(v_tch), "+v"(none));
        VM_BAR(13); issue_w(2); issue_w(3);
        VM_BAR(8); issue_x(4); issue_w(4);
        EPOCH_BAR(10); issue_x(5); issue_w(5);
        VM_BAR(12); issue_x(6); issue_w(6);
        VM_BAR(12); issue_x(7); issue_w(7);
        VM_BAR(12); issue_w(8); issue_w(9);
        asm volatile("global_load_dword %0, %1, %2" : "=v"(warm) : "v"(((tl >> 1) * 32 + (lane & 15)) * 3072), "s"(wb[2] - (j & 1) * 16 * 3072) : "memory");
        VM_BAR(15);
        VM_BAR(9); issue_w(10); issue_w(11);
        u32x2 tagv = {(unsigned)tag, (unsigned)(tag >> 32)};
        asm volatile("" : "+v"(tagv));
        f32x4 gl = {};
        const int grow = 16 * j + (lane & 15);
        int gxa = L_X + grow * 128 + (((lane >> 4) ^ ((grow >> 1) & 7)) << 4);
        u64 f1early = 0;
        float gt = 0.f;
        unsigned a_gt = lane < 16 ? L_G + grow * 4 : lane < 32 ? L_G2 + grow * 4 : L_ONES + (lane & 3) * 4;
        asm volatile("" : "+v"(a_gt));
#pragma unroll
        for (int i = 8; i < 24; i += 2) {
            if (i < 22) VM_BAR(8); else VM_BAR(0);
            if (i == 8) asm volatile("" : "+v"(gw), "+v"(warm));
            bf16x8 xg[4], ga[4];
            if (i < 16) {
#pragma unroll
                for (int u = 0; u < 4; ++u) {
                    xg[u] = ldsfrag(L + (gxa ^ ((u & 1) << 6)) + (i - 8 + (u >> 1)) * 8192);
                    ga[u] = (lane & 15) == 2 * ((s0 + i + (u >> 1)) & 7) + (u & 1) ? gw : bf16x8{};
                }
            }
            SBAR();
            if (i < 20) { issue_w(i + 4); issue_w(i + 5); }
            SBAR();
            if (i < 16) {
#pragma unroll
                for (int u = 0; u < 4; ++u) gl = __builtin_amdgcn_mfma_f32_16x16x32_bf16(ga[u], xg[u], gl, 0, 0, 0);
            }
            if (i == 22) {
                float z = (gl[0] + gl[1]) + (gl[2] + gl[3]);
                z += __shfl_xor(z, 16); z += __shfl_xor(z, 32);
                gt = __builtin_amdgcn_rcpf(1.f + EXP2(-z * LOG2E));
                gt = lane < 16 ? gt : lane < 32 ? 1.f - gt : __uint_as_float(0x3F803F80u);
                asm volatile("" : "+v"(gt));
            }
            if (i == 22) *(LAS float*)(L + a_gt) = gt;
            if (j == 0 && i == 20) {
                f1early = __hip_atomic_load((GAS u64*)(ws + WS_F1 + qd * 256) + (lane & 31), RLX_AGENT);
                const bool seen = __all(f1early == tag);
                if (lane < 36) *(LAS unsigned*)(L + L_SYNC + lane * 4) = lane >= 32 ? 0x3F803F80u : (lane == 0 && seen) ? 1u : 0u;
            }
            if (j == 0 && i == 22 && !__all(f1early == tag)) {
                wait_flags<0, 2>(ws, WS_F1 + qd * 256, 32, tag, lane);
                *(LAS volatile unsigned*)(L + L_SEEN) = 1;
            }
            SBAR();
        }
        auto publish_flag = [&](unsigned off) { DRAIN(); __builtin_amdgcn_raw_buffer_store_b64(tagv, rs, v_one, off, 16); };
        BAR();
        const u64 tp2 = __builtin_amdgcn_s_memtime();
        __builtin_amdgcn_s_sleep(6);
        bf16x8 phq[2][2]; float g2[2];
        {
            bf16x8 pf[4], ve[4];
#pragma unroll
            for (int u = 0; u < 4; ++u) {
                pf[u] = __builtin_shufflevector(trread(L + a_pf[0] + u * 1024), trread(L + a_pf[1] + u * 1024), 0, 1, 2, 3, 4, 5, 6, 7);
                ve[u] = ldsfrag(L + a_ve[u]);
                phq[u >> 1][u & 1] = ldsfrag(L + a_q + u * 1024);
            }
            g2[0] = *(LAS const float*)(L + a_g2); g2[1] = *(LAS const float*)(L + a_g2 + 128);
            lds_arrive(L + L_HCNT, lane);
            __builtin_amdgcn_s_sleep(PK_DELAY);
            touch(v_p8 * 16, (const char*)(j & 2 ? woA : woB) + blockIdx.x * 2048 + (j & 1) * 1024, lds0 + L_DUMMY);
            f32x16 kv = MFMA(pf[0], ve[0], f32x16{});
#pragma unroll
            for (int u = 1; u < 4; ++u) kv = MFMA(pf[u], ve[u], kv);
#pragma unroll
            for (int g = 0; g < 4; ++g) st16(rs, __builtin_bit_cast(bf16x8, (f32x4){kv[4 * g], kv[4 * g + 1], kv[4 * g + 2], kv[4 * g + 3]}), v_pk, WS_PK + ((qd * 32 + tl) * 4 + j) * 2176 + g * 544);
            publish_flag(WS_F2 + (head * 32 + tl) * 8);
        }
        lds_wait(L + L_HCNT, 4);
        int wo_done = 0;
        auto wo_service = [&]() {
            if (wo_done < 8 && __builtin_amdgcn_s_memtime() - tp2 >= (u64)(WO_T0 + WO_DT * (j + 4 * wo_done))) {
                const int mtx = wo_done >> 2, pc = wo_done & 3;
                glds4(vo, (const char*)(mtx ? woA : woB) + (size_t)(128 * j) * 1024 + ocg * 128 + pc * 32768, lds0 + (mtx ? L_WOA : L_WOB) + j * 16384 + pc * 4096);
                ++wo_done;
            }
        };
        auto wait_slow = [&](unsigned off, unsigned v, int first) {
#pragma unroll 1
            for (unsigned spins = 0; spins < SPIN_CAP; ++spins) {
#pragma unroll 1
                for (int z = 0; z < (spins ? POLL_GAP : first); ++z) { wo_service(); __builtin_amdgcn_s_sleep(6); }
                asm volatile("" ::: "memory");
                if (__all(__builtin_bit_cast(u64, __builtin_amdgcn_raw_buffer_load_b64(rs, v, off, 16)) == tag)) break;
            }
            __builtin_amdgcn_fence(__ATOMIC_ACQUIRE, "wavefront");
            asm volatile("" ::: "memory");
        };
        if (tl < 17) {
            wait_slow(WS_F2 + head * 256, v_p32, POLL_FIRST);
            float t[16];
#pragma unroll
            for (int i = 0; i < 16; ++i) t[i] = __uint_as_float(__builtin_amdgcn_raw_buffer_load_b32(rs, v_h1, WS_PK + (qd * 128 + j) * 2176 + tl * 128 + i * 17408, 16));
#pragma unroll
            for (int n = 8; n >= 1; n >>= 1)
#pragma unroll
                for (int i = 0; i < n; ++i) { t[i] += t[i + n]; KEEP1(t[i]); }
            auto r = __builtin_amdgcn_permlane32_swap(__float_as_uint(t[0]), __float_as_uint(t[0]), false, false);
            const float x0 = __uint_as_float(r[0]) + __uint_as_float(r[1]);
            const float x1 = __uint_as_float(__builtin_amdgcn_update_dpp(0, __float_as_uint(x0), 0xF5, 0xF, 0xF, true));
            __builtin_amdgcn_raw_buffer_store_b32(pk2(x0, x1), rs, v_h1s, WS_KVF + head * 2176 + tl * 128, 16);
            publish_flag(WS_F3 + (head * 32 + tl) * 8);
        }
        wait_slow(WS_F3 + head * 256, v_p17, tl < 17 ? POLL_FIRST : 2 * POLL_FIRST + 4);
        {
            u32x2 kq_[4];
#pragma unroll
            for (int g = 0; g < 4; ++g) kq_[g] = __builtin_amdgcn_raw_buffer_load_b64(rs, a_kf[g], WS_KVF + head * 2176, 16);
            const bf16x8 k0 = __builtin_bit_cast(bf16x8, __builtin_shufflevector(kq_[0], kq_[1], 0, 1, 2, 3)), k1 = __builtin_bit_cast(bf16x8, __builtin_shufflevector(kq_[2], kq_[3], 0, 1, 2, 3));
#pragma unroll
            for (int tt = 0; tt < 2; ++tt) {
                f32x16 ob = MFMA(k0, phq[tt][0], f32x16{});
                ob = MFMA(k1, phq[tt][1], ob);
                const float rd = g2[tt] * __builtin_amdgcn_rcpf(ob[8] + 1e-6f);
#pragma unroll
                for (int i = 0; i < 8; ++i) { float z = ob[i] * rd; KEEP1(z); ob[i] = z; }
                st16(rs, pack8(ob, 0), a_l16, WS_O + (2u << 20) + ((2 * tl + tt) * 32 + head) * 1024);
                st16(rs, none, a_l16, WS_O + ((2 * tl + tt) * 32 + head) * 1024);
            }
            publish_flag(WS_F5 + (tl * 32 + head) * 8);
        }
        wait_slow(WS_F5 + (otl * 32 + 8 * j) * 8, v_p8, POLL_FIRST);
        touch(v_tch, ws + WS_O + (2u << 20) + (2 * otl * 32 + 8 * j + ocg) * 1024, lds0 + L_DUMMY);
        while (wo_done < 8) { wo_service(); __builtin_amdgcn_s_sleep(6); }
        DRAIN();
        lds_arrive(L + L_OBS, lane);
        return;
    }

    if (w == 0) {
        const char* a = (const char*)(pc0 & ~127ull) + (idx * 9 + lane < CODE_LINES ? idx * 9 + lane : 0) * 128;
        if (lane == 9) {
            a = ws + (size_t)(idx < 21 ? idx : 0) * 524288;
            if (idx >= 22) a = idx < 25 ? (const char*)WB + (idx - 22) * 786368 : idx < 28 ? (const char*)out + (idx - 25) * 1048512 : idx < 30 ? (const char*)woA + (idx - 28) * 524224 : (const char*)woB + (idx - 30) * 524224;
        }
        unsigned keep;
        if (lane < 10) asm volatile("s_mov_b32 %0, m0\n\ts_mov_b32 m0, %2\n\ts_nop 0\n\tglobal_load_lds_dword %1, off sc1\n\ts_mov_b32 m0, %0" : "=&s"(keep) : "v"(a), "s"(lds0 + L_WPHI + 4096) : "memory");
    }
    const int p = w & 1;
    const int hq = w >> 1, kh = w & 1, head = 4 * qd + hq;
    const unsigned vK = w * 2048 + lane * 16;
    const unsigned vV = (unsigned)(w * 2048 + 1024 + h * 256 + (r32 & 15) * 16);
    const unsigned kvb = WS_KV + qd * 32 * BLK;
#define KVOFF(t) (kvb + (((t) + tl) & 31) * BLK)
    bf16x8 kr[4], v0r[4], v1r[4];
    const int kpos = ((lane & 0x33) | ((lane & 4) << 1) | ((lane & 8) >> 1)) * 16;
    bf16x8 kref;
    const int cst = (r32 & 11) ? L_ZEROS : L_ONES;
    {
        const int xa0 = L_X + r32 * 128 + ((h ^ ((r32 >> 1) & 7)) << 4);
        const int wa0 = L_RING + ((w >> 1) & 1) * 8192 + (8 * h + q4) * 128 + ((p ^ (q4 >> 1)) * 64) + trl;
        bf16x8 xf[2][4][2], w0[2][4];
        f32x16 acc[2];
        int wsb[3], xsb[4];
#pragma unroll
        for (int i = 0; i < 3; ++i) { wsb[i] = wa0 + i * 32768; asm volatile("" : "+v"(wsb[i])); }
#pragma unroll
        for (int s = 0; s < 4; ++s) { xsb[s] = xa0 ^ (s << 5); asm volatile("" : "+v"(xsb[s])); }
        auto gemm = [&](int TEAM, int TYPE) __attribute__((always_inline)) {
            auto mine = [&](int i) __attribute__((always_inline)) { return i >= 0 && i < 24 && ((i >> 3) & 1) == TEAM; };
            auto rx = [&](int i, int s, int t) __attribute__((always_inline)) { if (mine(i)) xf[i & 1][s][t] = ldsfrag(L + xsb[s] + (i & 7) * 8192 + t * 4096); };
            auto rw = [&](int i, int s) __attribute__((always_inline)) { if (mine(i)) w0[i & 1][s] = trfrag(L + wsb[((i + 4) % 6) >> 1] + (i & 1) * 16384 + s * 2048, 512); };
            auto mm = [&](int i, int s, int t, bool now = false) __attribute__((always_inline)) {
                SBAR();
                if (mine(i) && (i != 7 || now)) {
                    const f32x16 c = ((i & 7) == 0 && s == 0) ? f32x16{} : acc[t];
                    acc[t] = (TEAM == 0 && TYPE) ? MFMA(xf[i & 1][s][t], w0[i & 1][s], c) : MFMA(w0[i & 1][s], xf[i & 1][s][t], c);
                }
                SBAR();
            };
            bf16x8 own[2][2], wf[2], ph[4][2];
            f32x16 f[4];
            static_for<0, 25>([&](auto ic) __attribute__((always_inline)) {
                constexpr int i = ic.value;
                if (i == F1_STEP) { VM_BAR(0); if (tid == 0) set_flag(ws, WS_F1 + (qd * 32 + tl) * 8, tag); }
                else if (i == 0) EPOCH_BAR(63);
                else if (i == 2) { __builtin_amdgcn_s_waitcnt(0x0F70); BAR(); }
                else if (i < 8 || (i < 24 && !(i & 1))) BAR();
                mm(i - 1, 0, 0); rw(i, 0); rx(i, 0, 0);
                mm(i - 1, 0, 1); rx(i, 0, 1); rw(i, 1);
                mm(i - 1, 1, 0); rx(i, 1, 0); rx(i, 1, 1);
                mm(i - 1, 1, 1); rw(i, 2); rx(i, 2, 0);
                mm(i - 1, 2, 0); rx(i, 2, 1); rw(i, 3);
                mm(i - 1, 2, 1); rx(i, 3, 0); rx(i, 3, 1);
                mm(i - 1, 3, 0);
                if (TEAM == 0 && (i == 18 || i == 19)) {
#pragma unroll
                    for (int a = 0; a < 2; ++a)
                        *(LAS bf16x8*)(L + (TYPE ? L_VA + ((p * 2 + (i & 1)) * 2 + a) * 1024 + lane * 16 : L_KA + ((2 * p + a) * 2 + (i & 1)) * 1024 + kpos)) = own[i & 1][a];
                }
                mm(i - 1, 3, 1);
                if (TEAM == 0 && i == 7) {
#pragma unroll
                    for (int z = 0; z < 8; ++z) mm(7, z >> 1, z & 1, true);
#pragma unroll
                    for (int t = 0; t < 2; ++t) {
                        if (TYPE == 0) {
#pragma unroll
                            for (int e = 0; e < 16; ++e) acc[t][e] *= 0.25f * LOG2E;
                        }
                        own[t][0] = pack8(acc[t], 0); own[t][1] = pack8(acc[t], 1);
                        asm volatile("" : "+v"(own[t][0]), "+v"(own[t][1]));
                    }
                }
                if (TEAM == 0 && i == 8) {
                    __builtin_amdgcn_s_sleep(PUB_DELAY);
#pragma unroll
                    for (int t = 0; t < 2; ++t) {
                        const unsigned so = kvb + tl * BLK + (4 * p + t) * 2048;
#pragma unroll
                        for (int a = 0; a < 2; ++a) {
                            if (TYPE == 0) st16(rs, own[t][a], kpos, so + a * 4096);
                            else st16(rs, own[t][a], (unsigned)((r32 >> 4) * 4096 + 1024 + h * 256 + (r32 & 15) * 16) + a * 512, so);
                        }
                    }
                }
                if (TEAM == 1) {
                    if (i == 1) {
                        const short sg = (lane & 4) ? (short)0x8000 : (short)0;
#pragma unroll
                        for (int a = 0; a < 2; ++a) {
                            wf[a] = trfrag(L + L_WPHI + (2 * p + a) * 512 + (4 * h + q4) * 32 + (((lane >> 4) & 1) * 2 + ((lane >> 1) & 1)) * 8, 256);
#pragma unroll
                            for (int e = 0; e < 8; ++e) wf[a][e] ^= sg;
                            asm volatile("" : "+v"(wf[a]));
                        }
                    }
                    if (i == 16) {
                        __builtin_amdgcn_s_setprio(1);
#pragma unroll
                        for (int m = 0; m < 4; ++m) f[m] = MFMA(wf[m >> 1], pack8(acc[m & 1], m >> 1), f32x16{});
                    }
                    if (i == 22) {
                        lds_wait(L + L_SEEN, 1);
                        touch(((tl + 1 + ((lane >> 4) & 1)) & 31) * BLK + (w - 4) * 2048 + (lane & 15) * 128, ws + kvb, lds0 + L_DUMMY);
                    }
                    if (i >= 16 && i < 24 && !(i & 1)) { softmax16(f[(i - 16) / 2], ph[(i - 16) / 2]); asm volatile("" : "+v"(ph[(i - 16) / 2][0]), "+v"(ph[(i - 16) / 2][1])); }
                }
                if (i == 24) {
                    kref = ldsfrag(L + L_KA + (hq * 2) * 1024 + lane * 16);
                    kr[0] = ldsfrag(L + L_KA + (hq * 2 + kh) * 1024 + lane * 16);
                    const int va = L_VA + ((hq >> 1) * 2 + kh) * 2048 + (h * 32 + (hq & 1) * 16 + r32) * 16;
                    v0r[0] = ldsfrag(L + (r32 < 16 ? va : cst)); v1r[0] = ldsfrag(L + (r32 < 16 ? va + 1024 : cst));
                }
                SBAR();
            });
            if (TEAM == 0) {
#pragma unroll
                for (int t = 0; t < 2; ++t)
#pragma unroll
                    for (int a = 0; a < 2; ++a)
                        *(LAS bf16x8*)(L + (TYPE ? L_VB + ((p * 2 + t) * 2 + a) * 1024 : L_QA + ((2 * p + a) * 2 + t) * 1024) + lane * 16) = pack8(acc[t], a);
            } else if (w < 6) {
#pragma unroll
                for (int m = 0; m < 8; ++m) *(LAS bf16x8*)(L + L_PHQ + (8 * p + m) * 1024 + lane * 16) = ph[m >> 1][m & 1];
            } else {
#pragma unroll
                for (int m = 0; m < 8; ++m) {
                    const u32x4 u = __builtin_bit_cast(u32x4, ph[m >> 1][m & 1]);
#pragma unroll
                    for (int g = 0; g < 2; ++g)
                        *(LAS u32x2*)(L + L_PHK + (4 * p + (m >> 1)) * 2048 + r32 * 64 + 8 * ((2 * (2 * (m & 1) + g) + h) ^ ((r32 >> 1) & 7))) = (u32x2){u[2 * g], u[2 * g + 1]};
                }
            }
        };
        if (w < 2) gemm(0, 0); else if (w < 4) gemm(0, 1); else gemm(1, 0);
    }
    const u32x4 rs4 = {(unsigned)(uintptr_t)ws, (unsigned)((uintptr_t)ws >> 32) & 0xFFFFu, WS_END, 0x00020000u};
#define LDK(R, t) asm volatile("buffer_load_dwordx4 %0, %1, %2, %3 offen sc1" : "=v"(R) : "v"(vK), "s"(rs4), "s"(KVOFF(t)))
#define LDV(R, t, S) asm volatile("s_mov_b64 exec, %4\n\tbuffer_load_dwordx4 %0, %1, %2, %3 offen offset:" #S " sc1\n\ts_mov_b64 exec, -1" : "+v"(R) : "v"(vV), "s"(rs4), "s"(KVOFF(t)), "s"(0x0000FFFF0000FFFFull))
#define LD2(a, b, t) asm volatile("buffer_load_dwordx4 %0, %6, %8, %9 offen sc1\n\tbuffer_load_dwordx4 %1, %6, %8, %10 offen sc1\n\ts_mov_b64 exec, %11\n\t" \
    "buffer_load_dwordx4 %2, %7, %8, %9 offen sc1\n\tbuffer_load_dwordx4 %3, %7, %8, %9 offen offset:512 sc1\n\t" \
    "buffer_load_dwordx4 %4, %7, %8, %10 offen sc1\n\tbuffer_load_dwordx4 %5, %7, %8, %10 offen offset:512 sc1\n\ts_mov_b64 exec, -1" \
    : "=&v"(kr[a]), "=&v"(kr[b]), "+v"(v0r[a]), "+v"(v1r[a]), "+v"(v0r[b]), "+v"(v1r[b]) : "v"(vK), "v"(vV), "s"(rs4), "s"(KVOFF(t)), "s"(KVOFF((t) + 1)), "s"(0x0000FFFF0000FFFFull))
#define WAITR2(N, R, Q) asm volatile("s_waitcnt vmcnt(" #N ")" : "+v"(R), "+v"(Q))
#define WAITR(N, R) asm volatile("s_waitcnt vmcnt(" #N ")" : "+v"(R))
#pragma unroll
    for (int t = 1; t < 4; ++t) { v0r[t] = ldsfrag(L + cst); v1r[t] = ldsfrag(L + cst); asm volatile("" ::: "memory"); }
    f32x16 oa = {}, ob = {};
    bf16x8 ofb[8];
    BAR();
    const bf16x8 qa = ldsfrag(L + L_QA + (hq * 2 + (kh ^ 1)) * 1024 + lane * 16), qb = ldsfrag(L + L_QA + (hq * 2 + kh) * 1024 + lane * 16);
    const float m0 = lmax16(MFMA(kref, qa, f32x16{})), m1 = lmax16(MFMA(kref, qb, f32x16{}));
    const int ot = w & 1, kq = w >> 1;
    {
        f32x16 negm;
        const float nm = -max32(fmx(m0, m1));
#pragma unroll
        for (int i = 0; i < 16; ++i) negm[i] = nm;
        f32x16 sa = MFMA(kr[0], qa, negm), sb = {};
        SBAR();
        LDK(kr[1], 1); LDV(v0r[1], 1, 0); LDV(v1r[1], 1, 512);
#define E(S, i) S[i] = EXP2(S[i])
#define PHASE(S, M1, M2, M3, X1, X2, X3)                                                          \
    do {                                                                                          \
        E(S, 0); E(S, 1); SBAR();                                                                 \
        M1; X1; SBAR();                                                                           \
        E(S, 2); E(S, 3); E(S, 4); E(S, 5); SBAR();                                               \
        M2; X2; SBAR();                                                                           \
        E(S, 6); E(S, 7); E(S, 8); E(S, 9); SBAR();                                               \
        M3; X3; SBAR();                                                                           \
        E(S, 10); E(S, 11); E(S, 12); E(S, 13); E(S, 14); E(S, 15); SBAR();                       \
    } while (0)
        const unsigned obo = WS_O + (2u << 20) + ((2 * otl + ot) * 32 + 8 * kq) * 1024 - w * 2048;
        auto handoff = [&]() {
            LAS char* EXW = L + L_EX + w * 2304 + lane * 16;
            *(LAS f32x4*)EXW = (f32x4){oa[0], oa[1], oa[2], oa[3]};
            *(LAS f32x4*)(EXW + 1024) = (f32x4){oa[4], oa[5], oa[6], oa[7]};
            *(LAS float*)(L + L_EX + w * 2304 + 2048 + lane * 4) = oa[8];
            asm volatile("" ::: "memory");
            *(LAS volatile unsigned*)(L + L_PAIR + w * 4) = 1;
        };
        const int psw = w < 4 ? PRIO_SWITCH : -1;
        if (w < 4) __builtin_amdgcn_s_setprio(2); else __builtin_amdgcn_s_setprio(1);
#define PSW(t, u) if (u == (PRIO_SWITCH & 3)) asm volatile("s_cmp_lg_u32 %0, %1\n\ts_cbranch_scc1 1f\n\ts_setprio 0\n1:" :: "s"(t), "s"(psw) : "scc")
#define PAIR(t, u)                                                                                                  \
        PSW(t, u);                                                                                                                                                      \
        PHASE(sa, ob = PV(v1r[(u + 3) & 3], sb, 1, ob), sb = MFMA(kr[u], qb, negm), WAITR(9, v0r[u]); oa = PV(v0r[u], sa, 0, oa), SBAR(); LD2(u ^ 2, u ^ 3, (t) + 2), (void)0, (void)0); \
        PHASE(sb, WAITR2(8, v1r[u], kr[u + 1]); oa = PV(v1r[u], sa, 1, oa), sa = MFMA(kr[u + 1], qa, negm), ob = PV(v0r[u], sb, 0, ob), (void)0, (void)0, (void)0);     \
        PSW((t) + 1, u + 1);                                                                                                                                            \
        PHASE(sa, ob = PV(v1r[u], sb, 1, ob), sb = MFMA(kr[u + 1], qb, negm), WAITR(7, v0r[u + 1]); oa = PV(v0r[u + 1], sa, 0, oa), (void)0, (void)0, (void)0);         \
        PHASE(sb, WAITR(6, v1r[u + 1]); oa = PV(v1r[u + 1], sa, 1, oa), WAITR(5, kr[u ^ 2]); sa = MFMA(kr[u ^ 2], qa, negm), ob = PV(v0r[u + 1], sb, 0, ob), (void)0, (void)0, (void)0)
#pragma unroll 1
        for (int t0 = 0; t0 < 28; t0 += 4) {
            PAIR(t0, 0);
            PAIR(t0 + 2, 2);
        }
        PAIR(28, 0);
#define OFB(k) ofb[k] = ld16(rs, vK, obo + (k) * 1024)
        asm volatile("s_waitcnt vmcnt(0)" : "+v"(v0r[2]), "+v"(v1r[2]), "+v"(kr[3]), "+v"(v0r[3]), "+v"(v1r[3]));
        lds_wait(L + L_OBS, 4);
        PHASE(sa, ob = PV(v1r[1], sb, 1, ob), sb = MFMA(kr[2], qb, negm), oa = PV(v0r[2], sa, 0, oa), OFB(0), OFB(1), OFB(2));
        PHASE(sb, oa = PV(v1r[2], sa, 1, oa), sa = MFMA(kr[3], qa, negm), ob = PV(v0r[2], sb, 0, ob), OFB(3), (void)0, (void)0);
        PHASE(sa, ob = PV(v1r[2], sb, 1, ob), sb = MFMA(kr[3], qb, negm), oa = PV(v0r[3], sa, 0, oa), OFB(4), OFB(5), OFB(6));
        PHASE(sb, oa = PV(v1r[3], sa, 1, oa), (void)0, ob = PV(v0r[3], sb, 0, ob), OFB(7), (void)0, handoff());
        ob = PV(v1r[3], sb, 1, ob);
#undef PHASE
#undef E
    }
    __builtin_amdgcn_s_setprio(0);
    int lane_ = __builtin_amdgcn_mbcnt_hi(~0u, __builtin_amdgcn_mbcnt_lo(~0u, 0u));
    asm volatile("" : "+v"(lane_));
    const int r32_ = lane_ & 31, h_ = lane_ >> 5, q4_ = (lane_ & 15) >> 2, trl_ = ((lane_ >> 4) & 1) * 32 + (lane_ & 3) * 8;
    const unsigned oao = WS_O + ((2 * otl + ot) * 32 + 8 * kq) * 1024, vp = lane_ < 8 ? (unsigned)(lane_ * 1024 + 1020) : 0x40000000u;
    auto probe = [&]() { return (unsigned)__ballot(__builtin_amdgcn_raw_buffer_load_b32(rs, vp, oao, 16) != 0xFFFFFFFFu); };
    const unsigned pr0 = __builtin_amdgcn_raw_buffer_load_b32(rs, vp, oao, 16);
    SBAR();
    const int wpo = kq * 16384 + (4 * h_ + q4_) * 128 + trl_;
    bf16x8 wv[8][2];
    auto wreads = [&](int base, int k0, int k1) {
#pragma unroll
        for (int k = k0; k < k1; ++k)
#pragma unroll
            for (int nt = 0; nt < 2; ++nt) wv[k][nt] = trfrag(L + base + wpo + k * 2048 + ((nt ^ (q4_ >> 1)) * 64), 1024);
    };
    const float gate = *(LAS const float*)(L + L_G + (kh * 32 + r32_) * 4);
    lds_wait(L + L_PAIR + (w ^ 1) * 4, 1);
    {
        LAS const char* EXR = L + L_EX + (w ^ 1) * 2304 + lane_ * 16;
        const f32x4 e0 = *(LAS const f32x4*)EXR, e1 = *(LAS const f32x4*)(EXR + 1024);
        const float e2 = *(LAS const float*)(L + L_EX + (w ^ 1) * 2304 + 2048 + lane_ * 4);
        SBAR();
        wreads(L_WOB, 0, 4);
        SBAR();
        const float l = ob[8] + e2;
#pragma unroll
        for (int i = 0; i < 4; ++i) { ob[i] += e0[i]; ob[4 + i] += e1[i]; }
        float rl = __builtin_amdgcn_rcpf(l), t = fmx(l, rl) * 1.0e19f;
#pragma unroll
        for (int i = 0; i < 8; i += 2) t = fmx(fmx(t, __builtin_fabsf(ob[i])), __builtin_fabsf(ob[i + 1]));
        if (__builtin_expect(__any(!(t < 1.0e38f)), 0)) {
            ob = f32x16{};
            float m = -INFINITY;
            for (int t2 = 0; t2 < 64; ++t2) {
                const unsigned so = kvb + (t2 >> 1) * BLK, fl = (t2 & 1) * 2048;
                const bf16x8 ones = {0x3F80, 0x3F80, 0x3F80, 0x3F80, 0x3F80, 0x3F80, 0x3F80, 0x3F80};
                const bf16x8 kf = ld16(rs, vK ^ fl, so), v0l = ld16(rs, vV ^ fl, so), v1l = ld16(rs, vV ^ fl, so + 512);
                const bf16x8 v0 = r32_ < 16 ? v0l : ones, v1 = r32_ < 16 ? v1l : ones;
                f32x16 s = MFMA(kf, qb, f32x16{});
                const float mn = fmx(max16(s), m), al = EXP2(m - mn);
                m = mn;
#pragma unroll
                for (int i = 0; i < 16; ++i) { ob[i] *= al; s[i] = EXP2(s[i] - mn); }
                ob = PV(v0, s, 0, ob);
                ob = PV(v1, s, 1, ob);
            }
            rl = __builtin_amdgcn_rcpf(ob[8]);
        }
        rl *= gate;
#pragma unroll
        for (int i = 0; i < 8; ++i) ob[i] *= rl;
        st16(rs, __builtin_bit_cast(bf16x8, __builtin_elementwise_min(__builtin_bit_cast(u32x4, pack8(ob, 0)), (u32x4){0xFFFFFFFEu, 0xFFFFFFFEu, 0xFFFFFFFEu, 0xFFFFFFFEu})),
             lane_ * 16, WS_O + ((2 * tl + kh) * 32 + head) * 1024);
        SBAR();
    }
    if (lane_ == 0) __hip_atomic_fetch_add((LAS unsigned*)(L + L_STORED), 1u, __ATOMIC_RELAXED, __HIP_MEMORY_SCOPE_WORKGROUP);
    f32x16 acc[2];
    f32x4 keep0 = {}, keep1 = {};
    {
        bf16x8 of[8];
        auto loads = [&](int g) {
#pragma unroll
            for (int k = 4 * g; k < 4 * g + 4; ++k) of[k] = ld16(rs, lane_ * 16 + (k & 3) * 1024, oao + (k & 4) * 1024);
        };
        const bf16x8 none = {-1, -1, -1, -1, -1, -1, -1, -1};
        unsigned rdy = (unsigned)__ballot(pr0 != 0xFFFFFFFFu);
#pragma unroll
        for (int g = 0; g < 2; ++g) { if (((rdy >> (4 * g)) & 15) == 15) loads(g); else of[4 * g] = none; }
        SBAR();
#pragma unroll
        for (int k = 0; k < 8; ++k) {
#pragma unroll
            for (int nt = 0; nt < 2; ++nt) acc[nt] = MFMA(wv[k][nt], ofb[k], k ? acc[nt] : f32x16{});
            SBAR();
            if (k < 4) wreads(L_WOB, k + 4, k + 5); else wreads(L_WOA, k - 4, k - 3);
            SBAR();
        }
        wreads(L_WOA, 4, 8);
        lds_arrive(L + L_PAIR + w * 4, lane_);
        SBAR();
        auto missing = [&](int g) {
            const u32x4 v = __builtin_elementwise_max(__builtin_elementwise_max(__builtin_bit_cast(u32x4, of[4 * g]), __builtin_bit_cast(u32x4, of[4 * g + 1])),
                                                      __builtin_elementwise_max(__builtin_bit_cast(u32x4, of[4 * g + 2]), __builtin_bit_cast(u32x4, of[4 * g + 3])));
            return (bool)__any(__builtin_elementwise_max(__builtin_elementwise_max(v[0], v[1]), __builtin_elementwise_max(v[2], v[3])) == 0xFFFFFFFFu);
        };
        bool have1 = ((rdy >> 4) & 15) == 15;
#pragma unroll
        for (int g = 0; g < 2; ++g) {
#pragma unroll 1
            for (unsigned spins = 0; spins < SPIN_CAP && missing(g); ++spins) {
                rdy = lds_word(L + L_STORED) >= 8 ? 0xFFu : probe();
                asm volatile("" ::: "memory");
                if (g == 0 && !have1 && ((rdy >> 4) & 15) == 15) { loads(1); have1 = true; }
                if (((rdy >> (4 * g)) & 15) == 15) loads(g); else __builtin_amdgcn_s_sleep(RETRY_SLEEP);
            }
            if (g == 0) {
#pragma unroll
                for (int k = 0; k < 4; ++k) { acc[0] = MFMA(wv[k][0], of[k], acc[0]); acc[1] = MFMA(wv[k][1], of[k], acc[1]); }
                lds_wait(L + L_PAIR + (w ^ 1) * 4, 2);
            }
        }
        LAS char* PR = L + L_WOB + kq * 16384 + ot * 8192 + lane_ * 16;
#pragma unroll
        for (int nt = 0; nt < 2; ++nt)
#pragma unroll
            for (int k = 4; k < 8; ++k) acc[nt] = MFMA(wv[k][nt], of[k], acc[nt]);
        SBAR();
#pragma unroll
        for (int z = 0; z < 4; ++z) {
            const int b = 8 * (z & 1);
            const f32x4 u0 = {acc[z >> 1][b], acc[z >> 1][b + 1], acc[z >> 1][b + 2], acc[z >> 1][b + 3]}, u1 = {acc[z >> 1][b + 4], acc[z >> 1][b + 5], acc[z >> 1][b + 6], acc[z >> 1][b + 7]};
            if (z != kq) { *(LAS f32x4*)(PR + z * 2048) = u0; *(LAS f32x4*)(PR + z * 2048 + 1024) = u1; } else { keep0 = u0; keep1 = u1; }
        }
    }
    BAR();
    {
        f32x4 r0 = keep0, r1 = keep1;
#pragma unroll
        for (int z = 1; z < 4; ++z) {
            LAS const char* src = L + L_WOB + ((kq + z) & 3) * 16384 + ot * 8192 + kq * 2048 + lane_ * 16;
            r0 += *(LAS const f32x4*)src; r1 += *(LAS const f32x4*)(src + 1024);
        }
        unsigned a0 = pk2(r0[0], r0[1]), a1 = pk2(r0[2], r0[3]), b0 = pk2(r1[0], r1[1]), b1 = pk2(r1[2], r1[3]);
        auto s0_ = __builtin_amdgcn_permlane32_swap(a0, b0, false, false);
        auto s1_ = __builtin_amdgcn_permlane32_swap(a1, b1, false, false);
        *(u32x4*)(out + (size_t)(64 * otl + 32 * ot + r32_) * 512 + 64 * ocg + 16 * kq + 8 * h_) = (u32x4){s0_[0], s1_[0], s0_[1], s1_[1]};
    }
    if (blockIdx.x == 0 && w == 0 && lane_ == 0) set_flag(ws, WS_EP, tag);
}

extern "C" void kernel_launch(void* const* d_in, const int* in_sizes, int n_in, void* d_out, int out_size, void* d_ws, size_t ws_size, hipStream_t stream) {
    fused_kernel<<<256, 768, 0, stream>>>((const uint16_t*)d_in[0], (const uint16_t*)d_in[1], (const uint16_t*)d_in[4], (const uint16_t*)d_in[5], (const uint16_t*)d_in[7],
                                          (const uint16_t*)d_in[2], (const uint16_t*)d_in[6], (uint16_t*)d_out, (char*)d_ws);
}
```

```cpp
#include <hip/hip_runtime.h>
#include <hip/hip_bf16.h>
#include <stdint.h>

typedef short bf16x8 __attribute__((ext_vector_type(8)));
typedef short s16x4 __attribute__((ext_vector_type(4)));
typedef float f32x16 __attribute__((ext_vector_type(16)));
typedef float f32x4 __attribute__((ext_vector_type(4)));
typedef float f32x2 __attribute__((ext_vector_type(2)));
typedef unsigned u32x4 __attribute__((ext_vector_type(4)));
typedef unsigned u32x2 __attribute__((ext_vector_type(2)));
typedef __bf16 bf16x2_t __attribute__((ext_vector_type(2)));
typedef unsigned long long u64;
#define LAS __attribute__((address_space(3)))
#define GAS __attribute__((address_space(1)))
#define DI __device__ __forceinline__
#define MFMA(a, b, c) __builtin_amdgcn_mfma_f32_32x32x16_bf16((a), (b), (c), 0, 0, 0)
typedef __bf16 bfx8 __attribute__((ext_vector_type(8)));
typedef __bf16 bfx16 __attribute__((ext_vector_type(16)));
#define PV(a, S, half, c) __builtin_amdgcn_smfmac_f32_32x32x32_bf16(__builtin_bit_cast(bfx8, a), \
    __builtin_bit_cast(bfx16, __builtin_shufflevector(S, S, 8 * half, 8 * half + 1, 8 * half + 2, 8 * half + 3, 8 * half + 4, 8 * half + 5, 8 * half + 6, 8 * half + 7)), (c), 0xDDDD, 0, 0)
#define SBAR() __builtin_amdgcn_sched_barrier(0)
#define RLX_AGENT __ATOMIC_RELAXED, __HIP_MEMORY_SCOPE_AGENT
#define EXP2(x) __builtin_amdgcn_exp2f(x)

constexpr float LOG2E = 1.4426950408889634f;
constexpr unsigned BLK = 16384;
constexpr unsigned WS_KV = 0;
constexpr unsigned WS_O = WS_KV + 256 * BLK;
constexpr unsigned WS_PK = WS_O + (4u << 20);
constexpr unsigned WS_KVF = WS_PK + 32 * 32 * 2176;
constexpr unsigned WS_F1 = WS_KVF + 32 * 2176;
constexpr unsigned WS_F2 = WS_F1 + 2048;
constexpr unsigned WS_F3 = WS_F2 + 8192;
constexpr unsigned WS_F5 = WS_F3 + 8192;
constexpr unsigned WS_EP = WS_F5 + 8192;
constexpr unsigned WS_END = WS_EP + 128;
constexpr int L_X = 0, L_RING = 65536;
constexpr int L_PHK = 65536, L_PHQ = 81920, L_KA = 0, L_VA = 8192, L_VB = 16384;
constexpr int L_WOA = 0, L_WOB = 65536;
constexpr int L_WPHI = L_RING + 3 * 16384;
constexpr int L_G = 131072, L_SYNC = L_G + 256, L_SEEN = L_SYNC, L_STORED = L_SYNC + 4, L_HCNT = L_SYNC + 8, L_OBS = L_SYNC + 12, L_PAIR = L_SYNC + 16, L_ZEROS = L_SYNC + 96, L_ONES = L_SYNC + 128, L_DUMMY = L_SYNC + 256, L_G2 = L_SYNC + 512;
constexpr int L_QA = L_G + 1024, L_EX = L_QA + 8192;
constexpr int LDS_BYTES = 163840;
constexpr int F1_STEP = 14, PRIO_SWITCH = 23, PK_DELAY = 24, WO_T0 = 6400, WO_DT = 270, POLL_GAP = 2, POLL_FIRST = 6, RETRY_SLEEP = 2, PUB_DELAY = 4;
constexpr int CODE_LINES = 261;
constexpr unsigned SPIN_CAP = 1u << 20;

template <int N> struct IC { static constexpr int value = N; };
template <int I, int N, class F> DI void static_for(F&& f) { if constexpr (I < N) { f(IC<I>{}); static_for<I + 1, N>(f); } }
DI unsigned pk2(float a, float b) { f32x2 v = {a, b}; return __builtin_bit_cast(unsigned, __builtin_convertvector(v, bf16x2_t)); }
DI bf16x8 pack8(const f32x16& x, int s) {
    u32x4 p = {pk2(x[8 * s], x[8 * s + 1]), pk2(x[8 * s + 2], x[8 * s + 3]), pk2(x[8 * s + 4], x[8 * s + 5]), pk2(x[8 * s + 6], x[8 * s + 7])};
    return __builtin_bit_cast(bf16x8, p);
}
DI float fmx(float a, float b) { return __builtin_elementwise_maximum(a, b); }
DI float max32(float v) { auto r = __builtin_amdgcn_permlane32_swap(__float_as_uint(v), __float_as_uint(v), false, false); return fmx(__uint_as_float(r[0]), __uint_as_float(r[1])); }
DI float lmax16(const f32x16& s) {
    const float a = fmx(fmx(s[0], s[1]), s[2]), b = fmx(fmx(s[3], s[4]), s[5]), c = fmx(fmx(s[6], s[7]), s[8]), d = fmx(fmx(s[9], s[10]), s[11]), e = fmx(fmx(s[12], s[13]), s[14]);
    return fmx(fmx(fmx(a, b), c), fmx(fmx(d, e), s[15]));
}
DI float max16(const f32x16& s) { return max32(lmax16(s)); }
DI void glds2(unsigned v0, unsigned v1, const void* sbase, unsigned ldsdst) {
    unsigned keep;
    asm volatile("s_mov_b32 %0, m0\n\ts_mov_b32 m0, %4\n\ts_nop 0\n\tglobal_load_lds_dwordx4 %1, %3\n\tglobal_load_lds_dwordx4 %2, %3 offset:1024\n\ts_mov_b32 m0, %0"
                 : "=&s"(keep) : "v"(v0), "v"(v1), "s"(sbase), "s"(ldsdst) : "memory");
}
DI void glds4(const unsigned (&v)[4], const void* sbase, unsigned ldsdst) {
    unsigned keep;
    asm volatile("s_mov_b32 %0, m0\n\ts_mov_b32 m0, %6\n\ts_nop 0\n\tglobal_load_lds_dwordx4 %1, %5\n\tglobal_load_lds_dwordx4 %2, %5 offset:1024\n\t"
                 "global_load_lds_dwordx4 %3, %5 offset:2048\n\tglobal_load_lds_dwordx4 %4, %5 offset:3072\n\ts_mov_b32 m0, %0"
                 : "=&s"(keep) : "v"(v[0]), "v"(v[1]), "v"(v[2]), "v"(v[3]), "s"(sbase), "s"(ldsdst) : "memory");
}
DI void touch(unsigned voff, const void* sbase, unsigned ldsdst) {
    unsigned keep;
    asm volatile("s_mov_b32 %0, m0\n\ts_mov_b32 m0, %3\n\ts_nop 0\n\tglobal_load_lds_dword %1, %2 sc1\n\ts_mov_b32 m0, %0"
                 : "=&s"(keep) : "v"(voff), "s"(sbase), "s"(ldsdst) : "memory");
}
typedef short v4i16_t __attribute__((ext_vector_type(4)));
DI s16x4 trread(LAS const char* p) { return __builtin_bit_cast(s16x4, __builtin_amdgcn_ds_read_tr16_b64_v4i16((LAS v4i16_t*)p)); }
DI bf16x8 trfrag(LAS const char* p, int d2) { s16x4 lo = trread(p), hi = trread(p + d2); return __builtin_shufflevector(lo, hi, 0, 1, 2, 3, 4, 5, 6, 7); }
DI bf16x8 ldsfrag(LAS const char* p) { return *(LAS const bf16x8*)p; }
#define BAR() asm volatile("s_waitcnt lgkmcnt(0)\n\ts_barrier" ::: "memory")
#define VM_BAR(N) asm volatile("s_waitcnt vmcnt(" #N ") lgkmcnt(0)\n\ts_barrier" ::: "memory")
#define DRAIN() asm volatile("s_waitcnt vmcnt(0)" ::: "memory")
typedef __amdgpu_buffer_rsrc_t rsrc_t;
DI bf16x8 ld16(rsrc_t r, unsigned voff, unsigned soff) { return __builtin_bit_cast(bf16x8, __builtin_amdgcn_raw_buffer_load_b128(r, voff, soff, 16)); }
DI void st16(rsrc_t r, bf16x8 v, unsigned voff, unsigned soff) { __builtin_amdgcn_raw_buffer_store_b128(__builtin_bit_cast(u32x4, v), r, voff, soff, 16); }
DI unsigned lds_word(LAS char* p) { return __builtin_amdgcn_readfirstlane(*(LAS volatile unsigned*)p); }
DI void lds_wait(LAS char* p, unsigned want) {
#pragma unroll 1
    for (unsigned spins = 0; spins < (SPIN_CAP << 6) && lds_word(p) < want; ++spins) __builtin_amdgcn_s_sleep(1);
    asm volatile("" ::: "memory");
}
DI void lds_arrive(LAS char* p, int lane) {
    asm volatile("s_waitcnt lgkmcnt(0)" ::: "memory");
    if (lane == 0) __hip_atomic_fetch_add((LAS unsigned*)p, 1u, __ATOMIC_RELAXED, __HIP_MEMORY_SCOPE_WORKGROUP);
}
DI void set_flag(char* ws, unsigned off, u64 tag) { __hip_atomic_store((GAS u64*)(ws + off), tag, RLX_AGENT); }
template <int FIRST, int GAP> DI void wait_flags(char* ws, unsigned off, int n, u64 tag, int lane) {
    GAS u64* p = (GAS u64*)(ws + off) + (lane < n ? lane : 0);
    if (FIRST) __builtin_amdgcn_s_sleep(FIRST);
#pragma unroll 1
    for (unsigned spins = 0; spins < SPIN_CAP; ++spins) {
        const u64 v = __hip_atomic_load(p, RLX_AGENT);
        if (__all(v == tag)) break;
        __builtin_amdgcn_s_sleep(GAP);
    }
    __builtin_amdgcn_fence(__ATOMIC_ACQUIRE, "wavefront");
    asm volatile("" ::: "memory");
}

DI void softmax16(const f32x16& p, bf16x8 (&out)[2]) {
#define KEEP1(x) asm("" : "+v"(x))
    const float nm = -lmax16(p) * LOG2E;
    float e[16], t[8];
#pragma unroll
    for (int i = 0; i < 16; ++i) { e[i] = __builtin_fmaf(p[i], LOG2E, nm); KEEP1(e[i]); e[i] = EXP2(e[i]); }
#pragma unroll
    for (int i = 0; i < 8; ++i) { t[i] = e[2 * i] + e[2 * i + 1]; KEEP1(t[i]); }
#pragma unroll
    for (int i = 0; i < 4; ++i) { t[i] = t[2 * i] + t[2 * i + 1]; KEEP1(t[i]); }
    t[0] += t[1]; KEEP1(t[0]); t[2] += t[3]; KEEP1(t[2]);
    const float rs = __builtin_amdgcn_rcpf(t[0] + t[2]);
    u32x4 u[2];
#pragma unroll
    for (int i = 0; i < 16; ++i) { e[i] *= rs; KEEP1(e[i]); }
#pragma unroll
    for (int i = 0; i < 8; ++i) u[i >> 2][i & 3] = pk2(e[2 * i], e[2 * i + 1]);
    out[0] = __builtin_bit_cast(bf16x8, u[0]); out[1] = __builtin_bit_cast(bf16x8, u[1]);
}

__global__ __launch_bounds__(768, 3) void fused_kernel(const uint16_t* __restrict__ x, const uint16_t* __restrict__ WA, const uint16_t* __restrict__ WB,
                                                    const uint16_t* __restrict__ wphi, const uint16_t* __restrict__ gatew,
                                                    const uint16_t* __restrict__ woA, const uint16_t* __restrict__ woB,
                                                    uint16_t* __restrict__ out, char* ws) {
    __shared__ __attribute__((aligned(16))) char lds[LDS_BYTES];
    LAS char* L = (LAS char*)lds;
    u64 pc0; asm volatile("s_getpc_b64 %0" : "=s"(pc0));
    const unsigned lds0 = (unsigned)(uintptr_t)lds;
    const int tid = threadIdx.x, lane = tid & 63, w = __builtin_amdgcn_readfirstlane(tid >> 6);
    const int r32 = lane & 31, h = lane >> 5;
    const int xcd = blockIdx.x & 7, idx = blockIdx.x >> 3;
    const int qd = 2 * (xcd & 3) + (idx & 1), tl = 2 * (idx >> 1) + (xcd >> 2);
    const int s0 = ((tl >> 1) + 4 * (qd & 1)) & 7;
    const int otl = 4 * xcd + (idx >> 3), ocg = idx & 7;
    const rsrc_t rs = __builtin_amdgcn_make_buffer_rsrc(ws, 0, WS_END, 0x00020000);
    u64 tag;
#define EPOCH_BAR(N) do { asm volatile("s_load_dwordx2 %0, %1, 0x0\n\ts_waitcnt vmcnt(" #N ") lgkmcnt(0)\n\ts_barrier" : "=s"(tag) : "s"(ws + WS_EP) : "memory"); tag += 1; } while (0)
    const int q4 = (lane & 15) >> 2;
    const int trl = ((lane >> 4) & 1) * 32 + (lane & 3) * 8;
    asm volatile("" :: "s"(x), "s"(WA), "s"(WB), "s"(wphi), "s"(gatew), "s"(ws));
    if (__builtin_expect(w >= 8, 1)) {
        const int j = w - 8;
        const unsigned vsw = (unsigned)(((lane & 7) ^ (((lane >> 4) & 1) << 2)) << 4);
        unsigned vw[4];
#pragma unroll
        for (int u = 0; u < 4; ++u) vw[u] = (unsigned)((lane >> 3) * 3072) + vsw + u * (8 * 3072 - 1024);
        const unsigned vx0 = (unsigned)((lane >> 3) * 1024 + (((lane & 7) ^ (lane >> 4)) << 4)), vx1 = (vx0 ^ 64u) + 8192 - 1024;
        const char* xb = (const char*)x + (size_t)(64 * tl + 16 * j) * 1024;
        const size_t wo_ = (size_t)((j & 1) * 32) * 3072 + qd * 128;
        const char* wb[3] = {(const char*)WA + wo_ + 1024 + (j >> 1) * 1024, (const char*)WB + wo_ + (j >> 1) * 1024, (j >> 1) ? (const char*)WB + wo_ + 2048 : (const char*)WA + wo_};
        auto issue_x = [&](int i) { const int kc = (s0 + i) & 7; glds2(vx0, vx1, xb + kc * 128, lds0 + L_X + i * 8192 + j * 2048); };
        auto issue_w = [&](int i) { glds4(vw, wb[i >> 3] + ((s0 + i) & 7) * (64 * 3072), lds0 + L_RING + ((i + 4) % 6) * 16384 + j * 4096); };
        __builtin_amdgcn_s_setprio(3);
        issue_w(0); issue_x(0); issue_x(1); issue_x(2); issue_x(3);
        bf16x8 gw; unsigned warm;
        asm volatile("global_load_dwordx4 %0, %1, %2" : "=v"(gw) : "v"((lane & 15) * 64 + (lane >> 4) * 16), "s"(gatew) : "memory");
        glds2(lane * 16, lane * 16, wphi + qd * 1024, lds0 + L_WPHI);
        issue_w(1);
        int laneh = lane; asm volatile("" : "+v"(laneh));
        const int r32h = laneh & 31, hh = laneh >> 5, q4h = (laneh & 15) >> 2, trlh = ((laneh >> 4) & 1) * 32 + (laneh & 3) * 8;
        const unsigned vswh = (unsigned)(((laneh & 7) ^ (((laneh >> 4) & 1) << 2)) << 4);
        const int head = 4 * qd + j;
        const bool keep = (r32h >> 4) == (j & 1);
        constexpr unsigned OOB = 0x40000000u;
        unsigned a_ve[4], a_pf[2], a_kf[4], vo[4];
#pragma unroll
        for (int u = 0; u < 4; ++u) {
            a_ve[u] = keep ? L_VB + (j >> 1) * 4096 + u * 1024 + laneh * 16 : L_ONES;
            const unsigned n = u * 136 + ((r32h < 16 ? r32h : 16) * 2 + hh) * 4;
            a_kf[u] = (n >> 5) * 128 + (n & 31) * 2;
            vo[u] = (unsigned)((laneh >> 3) * 1024) + vswh + u * (8192 - 1024);
            asm volatile("" : "+v"(a_ve[u]), "+v"(a_kf[u]), "+v"(vo[u]));
        }
        a_pf[0] = L_PHK + j * 4096 + (4 * hh + q4h) * 64 + (trlh ^ ((2 * hh + (q4h >> 1)) * 8)); a_pf[1] = (a_pf[0] ^ 32) + 512;
        unsigned a_l16 = laneh * 16, a_q = L_PHQ + j * 4096 + laneh * 16, a_g2 = L_G2 + r32h * 4;
        unsigned v_pk = (keep || (r32h & 15) == 0) ? (unsigned)(((keep ? (r32h & 15) : 16) * 2 + hh) * 16) : OOB;
        unsigned v_one = laneh == 0 ? 0u : OOB, v_p32 = r32h * 8, v_p17 = (laneh < 17 ? laneh : 0) * 8, v_p8 = (laneh & 7) * 8;
        unsigned v_h1 = hh * 8704 + r32h * 4, v_h1s = (laneh < 32 && !(laneh & 1)) ? laneh * 2 : OOB, v_tch = ((laneh >> 3) & 1) * 32768 + (laneh & 7) * 128;
        bf16x8 none = {-1, -1, -1, -1, -1, -1, -1, -1};
        asm volatile("" : "+v"(a_pf[0]), "+v"(a_pf[1]), "+v"(a_l16), "+v"(a_q), "+v"(a_g2), "+v"(v_pk), "+v"(v_one), "+v"(v_p32), "+v"(v_p17), "+v"(v_p8));
        asm volatile("" : "+v"(v_h1), "+v"(v_h1s), "+v"(v_tch), "+v"(none));
        VM_BAR(13); issue_w(2); issue_w(3);
        VM_BAR(8); issue_x(4); issue_w(4);
        EPOCH_BAR(10); issue_x(5); issue_w(5);
        VM_BAR(12); issue_x(6); issue_w(6);
        VM_BAR(12); issue_x(7); issue_w(7);
        VM_BAR(12); issue_w(8); issue_w(9);
        asm volatile("global_load_dword %0, %1, %2" : "=v"(warm) : "v"(((tl >> 1) * 32 + (lane & 15)) * 3072), "s"(wb[2] - (j & 1) * 16 * 3072) : "memory");
        VM_BAR(15);
        VM_BAR(9); issue_w(10); issue_w(11);
        u32x2 tagv = {(unsigned)tag, (unsigned)(tag >> 32)};
        asm volatile("" : "+v"(tagv));
        f32x4 gl = {};
        const int grow = 16 * j + (lane & 15);
        int gxa = L_X + grow * 128 + (((lane >> 4) ^ ((grow >> 1) & 7)) << 4);
        u64 f1early = 0;
        float gt = 0.f;
        unsigned a_gt = lane < 16 ? L_G + grow * 4 : lane < 32 ? L_G2 + grow * 4 : L_ONES + (lane & 3) * 4;
        asm volatile("" : "+v"(a_gt));
#pragma unroll
        for (int i = 8; i < 24; i += 2) {
            if (i < 22) VM_BAR(8); else VM_BAR(0);
            if (i == 8) asm volatile("" : "+v"(gw), "+v"(warm));
            bf16x8 xg[4], ga[4];
            if (i < 16) {
#pragma unroll
                for (int u = 0; u < 4; ++u) {
                    xg[u] = ldsfrag(L + (gxa ^ ((u & 1) << 6)) + (i - 8 + (u >> 1)) * 8192);
                    ga[u] = (lane & 15) == 2 * ((s0 + i + (u >> 1)) & 7) + (u & 1) ? gw : bf16x8{};
                }
            }
            SBAR();
            if (i < 20) { issue_w(i + 4); issue_w(i + 5); }
            SBAR();
            if (i < 16) {
#pragma unroll
                for (int u = 0; u < 4; ++u) gl = __builtin_amdgcn_mfma_f32_16x16x32_bf16(ga[u], xg[u], gl, 0, 0, 0);
            }
            if (i == 22) {
                float z = (gl[0] + gl[1]) + (gl[2] + gl[3]);
                z += __shfl_xor(z, 16); z += __shfl_xor(z, 32);
                gt = __builtin_amdgcn_rcpf(1.f + EXP2(-z * LOG2E));
                gt = lane < 16 ? gt : lane < 32 ? 1.f - gt : __uint_as_float(0x3F803F80u);
                asm volatile("" : "+v"(gt));
            }
            if (i == 22) *(LAS float*)(L + a_gt) = gt;
            if (j == 0 && i == 20) {
                f1early = __hip_atomic_load((GAS u64*)(ws + WS_F1 + qd * 256) + (lane & 31), RLX_AGENT);
                const bool seen = __all(f1early == tag);
                if (lane < 36) *(LAS unsigned*)(L + L_SYNC + lane * 4) = lane >= 32 ? 0x3F803F80u : (lane == 0 && seen) ? 1u : 0u;
            }
            if (j == 0 && i == 22 && !__all(f1early == tag)) {
                wait_flags<0, 2>(ws, WS_F1 + qd * 256, 32, tag, lane);
                *(LAS volatile unsigned*)(L + L_SEEN) = 1;
            }
            SBAR();
        }
        auto publish_flag = [&](unsigned off) { DRAIN(); __builtin_amdgcn_raw_buffer_store_b64(tagv, rs, v_one, off, 16); };
        BAR();
        const u64 tp2 = __builtin_amdgcn_s_memtime();
        __builtin_amdgcn_s_sleep(6);
        bf16x8 phq[2][2]; float g2[2];
        {
            bf16x8 pf[4], ve[4];
#pragma unroll
            for (int u = 0; u < 4; ++u) {
                pf[u] = __builtin_shufflevector(trread(L + a_pf[0] + u * 1024), trread(L + a_pf[1] + u * 1024), 0, 1, 2, 3, 4, 5, 6, 7);
                ve[u] = ldsfrag(L + a_ve[u]);
                phq[u >> 1][u & 1] = ldsfrag(L + a_q + u * 1024);
            }
            g2[0] = *(LAS const float*)(L + a_g2); g2[1] = *(LAS const float*)(L + a_g2 + 128);
            lds_arrive(L + L_HCNT, lane);
            __builtin_amdgcn_s_sleep(PK_DELAY);
            touch(v_p8 * 16, (const char*)(j & 2 ? woA : woB) + blockIdx.x * 2048 + (j & 1) * 1024, lds0 + L_DUMMY);
            f32x16 kv = MFMA(pf[0], ve[0], f32x16{});
#pragma unroll
            for (int u = 1; u < 4; ++u) kv = MFMA(pf[u], ve[u], kv);
#pragma unroll
            for (int g = 0; g < 4; ++g) st16(rs, __builtin_bit_cast(bf16x8, (f32x4){kv[4 * g], kv[4 * g + 1], kv[4 * g + 2], kv[4 * g + 3]}), v_pk, WS_PK + ((qd * 32 + tl) * 4 + j) * 2176 + g * 544);
            publish_flag(WS_F2 + (head * 32 + tl) * 8);
        }
        lds_wait(L + L_HCNT, 4);
        int wo_done = 0;
        auto wo_service = [&]() {
            if (wo_done < 8 && __builtin_amdgcn_s_memtime() - tp2 >= (u64)(WO_T0 + WO_DT * (j + 4 * wo_done))) {
                const int mtx = wo_done >> 2, pc = wo_done & 3;
                glds4(vo, (const char*)(mtx ? woA : woB) + (size_t)(128 * j) * 1024 + ocg * 128 + pc * 32768, lds0 + (mtx ? L_WOA : L_WOB) + j * 16384 + pc * 4096);
                ++wo_done;
            }
        };
        auto wait_slow = [&](unsigned off, unsigned v, int first) {
#pragma unroll 1
            for (unsigned spins = 0; spins < SPIN_CAP; ++spins) {
#pragma unroll 1
                for (int z = 0; z < (spins ? POLL_GAP : first); ++z) { wo_service(); __builtin_amdgcn_s_sleep(6); }
                asm volatile("" ::: "memory");
                if (__all(__builtin_bit_cast(u64, __builtin_amdgcn_raw_buffer_load_b64(rs, v, off, 16)) == tag)) break;
            }
            __builtin_amdgcn_fence(__ATOMIC_ACQUIRE, "wavefront");
            asm volatile("" ::: "memory");
        };
        if (tl < 17) {
            wait_slow(WS_F2 + head * 256, v_p32, POLL_FIRST);
            float t[16];
#pragma unroll
            for (int i = 0; i < 16; ++i) t[i] = __uint_as_float(__builtin_amdgcn_raw_buffer_load_b32(rs, v_h1, WS_PK + (qd * 128 + j) * 2176 + tl * 128 + i * 17408, 16));
#pragma unroll
            for (int n = 8; n >= 1; n >>= 1)
#pragma unroll
                for (int i = 0; i < n; ++i) { t[i] += t[i + n]; KEEP1(t[i]); }
            auto r = __builtin_amdgcn_permlane32_swap(__float_as_uint(t[0]), __float_as_uint(t[0]), false, false);
            const float x0 = __uint_as_float(r[0]) + __uint_as_float(r[1]);
            const float x1 = __uint_as_float(__builtin_amdgcn_update_dpp(0, __float_as_uint(x0), 0xF5, 0xF, 0xF, true));
            __builtin_amdgcn_raw_buffer_store_b32(pk2(x0, x1), rs, v_h1s, WS_KVF + head * 2176 + tl * 128, 16);
            publish_flag(WS_F3 + (head * 32 + tl) * 8);
        }
        wait_slow(WS_F3 + head * 256, v_p17, tl < 17 ? POLL_FIRST : 2 * POLL_FIRST + 4);
        {
            u32x2 kq_[4];
#pragma unroll
            for (int g = 0; g < 4; ++g) kq_[g] = __builtin_amdgcn_raw_buffer_load_b64(rs, a_kf[g], WS_KVF + head * 2176, 16);
            const bf16x8 k0 = __builtin_bit_cast(bf16x8, __builtin_shufflevector(kq_[0], kq_[1], 0, 1, 2, 3)), k1 = __builtin_bit_cast(bf16x8, __builtin_shufflevector(kq_[2], kq_[3], 0, 1, 2, 3));
#pragma unroll
            for (int tt = 0; tt < 2; ++tt) {
                f32x16 ob = MFMA(k0, phq[tt][0], f32x16{});
                ob = MFMA(k1, phq[tt][1], ob);
                const float rd = g2[tt] * __builtin_amdgcn_rcpf(ob[8] + 1e-6f);
#pragma unroll
                for (int i = 0; i < 8; ++i) { float z = ob[i] * rd; KEEP1(z); ob[i] = z; }
                st16(rs, pack8(ob, 0), a_l16, WS_O + (2u << 20) + ((2 * tl + tt) * 32 + head) * 1024);
                st16(rs, none, a_l16, WS_O + ((2 * tl + tt) * 32 + head) * 1024);
            }
            publish_flag(WS_F5 + (tl * 32 + head) * 8);
        }
        wait_slow(WS_F5 + (otl * 32 + 8 * j) * 8, v_p8, POLL_FIRST);
        touch(v_tch, ws + WS_O + (2u << 20) + (2 * otl * 32 + 8 * j + ocg) * 1024, lds0 + L_DUMMY);
        while (wo_done < 8) { wo_service(); __builtin_amdgcn_s_sleep(6); }
        DRAIN();
        lds_arrive(L + L_OBS, lane);
        return;
    }

    if (w == 0) {
        const char* a = (const char*)(pc0 & ~127ull) + (idx * 9 + lane < CODE_LINES ? idx * 9 + lane : 0) * 128;
        if (lane == 9) {
            a = ws + (size_t)(idx < 21 ? idx : 0) * 524288;
            if (idx >= 22) a = idx < 25 ? (const char*)WB + (idx - 22) * 786368 : idx < 28 ? (const char*)out + (idx - 25) * 1048512 : idx < 30 ? (const char*)woA + (idx - 28) * 524224 : (const char*)woB + (idx - 30) * 524224;
        }
        unsigned keep;
        if (lane < 10) asm volatile("s_mov_b32 %0, m0\n\ts_mov_b32 m0, %2\n\ts_nop 0\n\tglobal_load_lds_dword %1, off sc1\n\ts_mov_b32 m0, %0" : "=&s"(keep) : "v"(a), "s"(lds0 + L_WPHI + 4096) : "memory");
    }
    const int p = w & 1;
    const int hq = w >> 1, kh = w & 1, head = 4 * qd + hq;
    const unsigned vK = w * 2048 + lane * 16;
    const unsigned vV = (unsigned)(w * 2048 + 1024 + h * 256 + (r32 & 15) * 16);
    const unsigned kvb = WS_KV + qd * 32 * BLK;
#define KVOFF(t) (kvb + (((t) + tl) & 31) * BLK)
    bf16x8 kr[4], v0r[4], v1r[4];
    const int kpos = ((lane & 0x33) | ((lane & 4) << 1) | ((lane & 8) >> 1)) * 16;
    bf16x8 kref;
    const int cst = (r32 & 11) ? L_ZEROS : L_ONES;
    {
        const int xa0 = L_X + r32 * 128 + ((h ^ ((r32 >> 1) & 7)) << 4);
        const int wa0 = L_RING + ((w >> 1) & 1) * 8192 + (8 * h + q4) * 128 + ((p ^ (q4 >> 1)) * 64) + trl;
        bf16x8 xf[2][4][2], w0[2][4];
        f32x16 acc[2];
        int wsb[3], xsb[4];
#pragma unroll
        for (int i = 0; i < 3; ++i) { wsb[i] = wa0 + i * 32768; asm volatile("" : "+v"(wsb[i])); }
#pragma unroll
        for (int s = 0; s < 4; ++s) { xsb[s] = xa0 ^ (s << 5); asm volatile("" : "+v"(xsb[s])); }
        auto gemm = [&](int TEAM, int TYPE) __attribute__((always_inline)) {
            auto mine = [&](int i) __attribute__((always_inline)) { return i >= 0 && i < 24 && ((i >> 3) & 1) == TEAM; };
            auto rx = [&](int i, int s, int t) __attribute__((always_inline)) { if (mine(i)) xf[i & 1][s][t] = ldsfrag(L + xsb[s] + (i & 7) * 8192 + t * 4096); };
            auto rw = [&](int i, int s) __attribute__((always_inline)) { if (mine(i)) w0[i & 1][s] = trfrag(L + wsb[((i + 4) % 6) >> 1] + (i & 1) * 16384 + s * 2048, 512); };
            auto mm = [&](int i, int s, int t, bool now = false) __attribute__((always_inline)) {
                SBAR();
                if (mine(i) && (i != 7 || now)) {
                    const f32x16 c = ((i & 7) == 0 && s == 0) ? f32x16{} : acc[t];
                    acc[t] = (TEAM == 0 && TYPE) ? MFMA(xf[i & 1][s][t], w0[i & 1][s], c) : MFMA(w0[i & 1][s], xf[i & 1][s][t], c);
                }
                SBAR();
            };
            bf16x8 own[2][2], wf[2], ph[4][2];
            f32x16 f[4];
            static_for<0, 25>([&](auto ic) __attribute__((always_inline)) {
                constexpr int i = ic.value;
                if (i == F1_STEP) { VM_BAR(0); if (tid == 0) set_flag(ws, WS_F1 + (qd * 32 + tl) * 8, tag); }
                else if (i == 0) EPOCH_BAR(63);
                else if (i == 2) { __builtin_amdgcn_s_waitcnt(0x0F70); BAR(); }
                else if (i < 8 || (i < 24 && !(i & 1))) BAR();
                mm(i - 1, 0, 0); rw(i, 0); rx(i, 0, 0);
                mm(i - 1, 0, 1); rx(i, 0, 1); rw(i, 1);
                mm(i - 1, 1, 0); rx(i, 1, 0); rx(i, 1, 1);
                mm(i - 1, 1, 1); rw(i, 2); rx(i, 2, 0);
                mm(i - 1, 2, 0); rx(i, 2, 1); rw(i, 3);
                mm(i - 1, 2, 1); rx(i, 3, 0); rx(i, 3, 1);
                mm(i - 1, 3, 0);
                if (TEAM == 0 && (i == 18 || i == 19)) {
#pragma unroll
                    for (int a = 0; a < 2; ++a)
                        *(LAS bf16x8*)(L + (TYPE ? L_VA + ((p * 2 + (i & 1)) * 2 + a) * 1024 + lane * 16 : L_KA + ((2 * p + a) * 2 + (i & 1)) * 1024 + kpos)) = own[i & 1][a];
                }
                mm(i - 1, 3, 1);
                if (TEAM == 0 && i == 7) {
#pragma unroll
                    for (int z = 0; z < 8; ++z) mm(7, z >> 1, z & 1, true);
#pragma unroll
                    for (int t = 0; t < 2; ++t) {
                        if (TYPE == 0) {
#pragma unroll
                            for (int e = 0; e < 16; ++e) acc[t][e] *= 0.25f * LOG2E;
                        }
                        own[t][0] = pack8(acc[t], 0); own[t][1] = pack8(acc[t], 1);
                        asm volatile("" : "+v"(own[t][0]), "+v"(own[t][1]));
                    }
                }
                if (TEAM == 0 && i == 8) {
                    __builtin_amdgcn_s_sleep(PUB_DELAY);
#pragma unroll
                    for (int t = 0; t < 2; ++t) {
                        const unsigned so = kvb + tl * BLK + (4 * p + t) * 2048;
#pragma unroll
                        for (int a = 0; a < 2; ++a) {
                            if (TYPE == 0) st16(rs, own[t][a], kpos, so + a * 4096);
                            else st16(rs, own[t][a], (unsigned)((r32 >> 4) * 4096 + 1024 + h * 256 + (r32 & 15) * 16) + a * 512, so);
                        }
                    }
                }
                if (TEAM == 1) {
                    if (i == 1) {
                        const short sg = (lane & 4) ? (short)0x8000 : (short)0;
#pragma unroll
                        for (int a = 0; a < 2; ++a) {
                            wf[a] = trfrag(L + L_WPHI + (2 * p + a) * 512 + (4 * h + q4) * 32 + (((lane >> 4) & 1) * 2 + ((lane >> 1) & 1)) * 8, 256);
#pragma unroll
                            for (int e = 0; e < 8; ++e) wf[a][e] ^= sg;
                            asm volatile("" : "+v"(wf[a]));
                        }
                    }
                    if (i == 16) {
                        __builtin_amdgcn_s_setprio(1);
#pragma unroll
                        for (int m = 0; m < 4; ++m) f[m] = MFMA(wf[m >> 1], pack8(acc[m & 1], m >> 1), f32x16{});
                    }
                    if (i == 22) {
                        lds_wait(L + L_SEEN, 1);
                        touch(((tl + 1 + ((lane >> 4) & 1)) & 31) * BLK + (w - 4) * 2048 + (lane & 15) * 128, ws + kvb, lds0 + L_DUMMY);
                    }
                    if (i >= 16 && i < 24 && !(i & 1)) { softmax16(f[(i - 16) / 2], ph[(i - 16) / 2]); asm volatile("" : "+v"(ph[(i - 16) / 2][0]), "+v"(ph[(i - 16) / 2][1])); }
                }
                if (i == 24) {
                    kref = ldsfrag(L + L_KA + (hq * 2) * 1024 + lane * 16);
                    kr[0] = ldsfrag(L + L_KA + (hq * 2 + kh) * 1024 + lane * 16);
                    const int va = L_VA + ((hq >> 1) * 2 + kh) * 2048 + (h * 32 + (hq & 1) * 16 + r32) * 16;
                    v0r[0] = ldsfrag(L + (r32 < 16 ? va : cst)); v1r[0] = ldsfrag(L + (r32 < 16 ? va + 1024 : cst));
                }
                SBAR();
            });
            if (TEAM == 0) {
#pragma unroll
                for (int t = 0; t < 2; ++t)
#pragma unroll
                    for (int a = 0; a < 2; ++a)
                        *(LAS bf16x8*)(L + (TYPE ? L_VB + ((p * 2 + t) * 2 + a) * 1024 : L_QA + ((2 * p + a) * 2 + t) * 1024) + lane * 16) = pack8(acc[t], a);
            } else if (w < 6) {
#pragma unroll
                for (int m = 0; m < 8; ++m) *(LAS bf16x8*)(L + L_PHQ + (8 * p + m) * 1024 + lane * 16) = ph[m >> 1][m & 1];
            } else {
#pragma unroll
                for (int m = 0; m < 8; ++m) {
                    const u32x4 u = __builtin_bit_cast(u32x4, ph[m >> 1][m & 1]);
#pragma unroll
                    for (int g = 0; g < 2; ++g)
                        *(LAS u32x2*)(L + L_PHK + (4 * p + (m >> 1)) * 2048 + r32 * 64 + 8 * ((2 * (2 * (m & 1) + g) + h) ^ ((r32 >> 1) & 7))) = (u32x2){u[2 * g], u[2 * g + 1]};
                }
            }
        };
        if (w < 2) gemm(0, 0); else if (w < 4) gemm(0, 1); else gemm(1, 0);
    }
    const u32x4 rs4 = {(unsigned)(uintptr_t)ws, (unsigned)((uintptr_t)ws >> 32) & 0xFFFFu, WS_END, 0x00020000u};
#define LDK(R, t) asm volatile("buffer_load_dwordx4 %0, %1, %2, %3 offen sc1" : "=v"(R) : "v"(vK), "s"(rs4), "s"(KVOFF(t)))
#define LDV(R, t, S) asm volatile("s_mov_b64 exec, %4\n\tbuffer_load_dwordx4 %0, %1, %2, %3 offen offset:" #S " sc1\n\ts_mov_b64 exec, -1" : "+v"(R) : "v"(vV), "s"(rs4), "s"(KVOFF(t)), "s"(0x0000FFFF0000FFFFull))
#define LD2(a, b, t) asm volatile("buffer_load_dwordx4 %0, %6, %8, %9 offen sc1\n\tbuffer_load_dwordx4 %1, %6, %8, %10 offen sc1\n\ts_mov_b64 exec, %11\n\t" \
    "buffer_load_dwordx4 %2, %7, %8, %9 offen sc1\n\tbuffer_load_dwordx4 %3, %7, %8, %9 offen offset:512 sc1\n\t" \
    "buffer_load_dwordx4 %4, %7, %8, %10 offen sc1\n\tbuffer_load_dwordx4 %5, %7, %8, %10 offen offset:512 sc1\n\ts_mov_b64 exec, -1" \
    : "=&v"(kr[a]), "=&v"(kr[b]), "+v"(v0r[a]), "+v"(v1r[a]), "+v"(v0r[b]), "+v"(v1r[b]) : "v"(vK), "v"(vV), "s"(rs4), "s"(KVOFF(t)), "s"(KVOFF((t) + 1)), "s"(0x0000FFFF0000FFFFull))
#define WAITR2(N, R, Q) asm volatile("s_waitcnt vmcnt(" #N ")" : "+v"(R), "+v"(Q))
#define WAITR(N, R) asm volatile("s_waitcnt vmcnt(" #N ")" : "+v"(R))
#pragma unroll
    for (int t = 1; t < 4; ++t) { v0r[t] = ldsfrag(L + cst); v1r[t] = ldsfrag(L + cst); asm volatile("" ::: "memory"); }
    f32x16 oa = {}, ob = {};
    bf16x8 ofb[8];
    BAR();
    const bf16x8 qa = ldsfrag(L + L_QA + (hq * 2 + (kh ^ 1)) * 1024 + lane * 16), qb = ldsfrag(L + L_QA + (hq * 2 + kh) * 1024 + lane * 16);
    const float m0 = lmax16(MFMA(kref, qa, f32x16{})), m1 = lmax16(MFMA(kref, qb, f32x16{}));
    const int ot = w & 1, kq = w >> 1;
    {
        f32x16 negm;
        const float nm = -max32(fmx(m0, m1));
#pragma unroll
        for (int i = 0; i < 16; ++i) negm[i] = nm;
        f32x16 sa = MFMA(kr[0], qa, negm), sb = {};
        SBAR();
        LDK(kr[1], 1); LDV(v0r[1], 1, 0); LDV(v1r[1], 1, 512);
#define E(S, i) S[i] = EXP2(S[i])
#define PHASE(S, M1, M2, M3, X1, X2, X3)                                                          \
    do {                                                                                          \
        E(S, 0); E(S, 1); SBAR();                                                                 \
        M1; X1; SBAR();                                                                           \
        E(S, 2); E(S, 3); E(S, 4); E(S, 5); SBAR();                                               \
        M2; X2; SBAR();                                                                           \
        E(S, 6); E(S, 7); E(S, 8); E(S, 9); SBAR();                                               \
        M3; X3; SBAR();                                                                           \
        E(S, 10); E(S, 11); E(S, 12); E(S, 13); E(S, 14); E(S, 15); SBAR();                       \
    } while (0)
        const unsigned obo = WS_O + (2u << 20) + ((2 * otl + ot) * 32 + 8 * kq) * 1024 - w * 2048;
        auto handoff = [&]() {
            LAS char* EXW = L + L_EX + w * 2304 + lane * 16;
            *(LAS f32x4*)EXW = (f32x4){oa[0], oa[1], oa[2], oa[3]};
            *(LAS f32x4*)(EXW + 1024) = (f32x4){oa[4], oa[5], oa[6], oa[7]};
            *(LAS float*)(L + L_EX + w * 2304 + 2048 + lane * 4) = oa[8];
            asm volatile("" ::: "memory");
            *(LAS volatile unsigned*)(L + L_PAIR + w * 4) = 1;
        };
        const int psw = w < 4 ? PRIO_SWITCH : -1;
        if (w < 4) __builtin_amdgcn_s_setprio(2); else __builtin_amdgcn_s_setprio(1);
#define PSW(t, u) if (u == (PRIO_SWITCH & 3)) asm volatile("s_cmp_lg_u32 %0, %1\n\ts_cbranch_scc1 1f\n\ts_setprio 0\n1:" :: "s"(t), "s"(psw) : "scc")
#define PAIR(t, u)                                  \
        PSW(t, u);                                                                                                                                                      \
        PHASE(sa, ob = PV(v1r[(u + 3) & 3], sb, 1, ob), sb = MFMA(kr[u], qb, negm), oa = PV(v0r[u], sa, 0, oa), SBAR(); LD2(u ^ 2, u ^ 3, (t) + 2), SBAR(); WAITR(9, v0r[u]), (void)0); \
        WAITR2(8, v1r[u], kr[u + 1]); SBAR();                                                                                                                           \
        PHASE(sb, oa = PV(v1r[u], sa, 1, oa), sa = MFMA(kr[u + 1], qa, negm), ob = PV(v0r[u], sb, 0, ob), (void)0, (void)0, (void)0);                                   \
        PSW((t) + 1, u + 1);                                                                                                                                            \
        PHASE(sa, ob = PV(v1r[u], sb, 1, ob), sb = MFMA(kr[u + 1], qb, negm), oa = PV(v0r[u + 1], sa, 0, oa), (void)0, SBAR(); WAITR(7, v0r[u + 1]), (void)0);          \
        WAITR(6, v1r[u + 1]); SBAR();                                                                                                                                   \
        PHASE(sb, oa = PV(v1r[u + 1], sa, 1, oa), sa = MFMA(kr[u ^ 2], qa, negm), ob = PV(v0r[u + 1], sb, 0, ob), SBAR(); WAITR(5, kr[u ^ 2]), (void)0, (void)0)
#pragma unroll 1
        for (int t0 = 0; t0 < 28; t0 += 4) {
            PAIR(t0, 0);
            PAIR(t0 + 2, 2);
        }
        PAIR(28, 0);
#define OFB(k) ofb[k] = ld16(rs, vK, obo + (k) * 1024)
        asm volatile("s_waitcnt vmcnt(0)" : "+v"(v0r[2]), "+v"(v1r[2]), "+v"(kr[3]), "+v"(v0r[3]), "+v"(v1r[3]));
        lds_wait(L + L_OBS, 4);
        PHASE(sa, ob = PV(v1r[1], sb, 1, ob), sb = MFMA(kr[2], qb, negm), oa = PV(v0r[2], sa, 0, oa), OFB(0), OFB(1), OFB(2));
        PHASE(sb, oa = PV(v1r[2], sa, 1, oa), sa = MFMA(kr[3], qa, negm), ob = PV(v0r[2], sb, 0, ob), OFB(3), (void)0, (void)0);
        PHASE(sa, ob = PV(v1r[2], sb, 1, ob), sb = MFMA(kr[3], qb, negm), oa = PV(v0r[3], sa, 0, oa), OFB(4), OFB(5), OFB(6));
        PHASE(sb, oa = PV(v1r[3], sa, 1, oa), (void)0, ob = PV(v0r[3], sb, 0, ob), OFB(7), (void)0, handoff());
        ob = PV(v1r[3], sb, 1, ob);
#undef PHASE
#undef E
    }
    __builtin_amdgcn_s_setprio(0);
    int lane_ = __builtin_amdgcn_mbcnt_hi(~0u, __builtin_amdgcn_mbcnt_lo(~0u, 0u));
    asm volatile("" : "+v"(lane_));
    const int r32_ = lane_ & 31, h_ = lane_ >> 5, q4_ = (lane_ & 15) >> 2, trl_ = ((lane_ >> 4) & 1) * 32 + (lane_ & 3) * 8;
    const unsigned oao = WS_O + ((2 * otl + ot) * 32 + 8 * kq) * 1024, vp = lane_ < 8 ? (unsigned)(lane_ * 1024 + 1020) : 0x40000000u;
    auto probe = [&]() { return (unsigned)__ballot(__builtin_amdgcn_raw_buffer_load_b32(rs, vp, oao, 16) != 0xFFFFFFFFu); };
    const unsigned pr0 = __builtin_amdgcn_raw_buffer_load_b32(rs, vp, oao, 16);
    SBAR();
    const int wpo = kq * 16384 + (4 * h_ + q4_) * 128 + trl_;
    bf16x8 wv[8][2];
    auto wreads = [&](int base, int k0, int k1) {
#pragma unroll
        for (int k = k0; k < k1; ++k)
#pragma unroll
            for (int nt = 0; nt < 2; ++nt) wv[k][nt] = trfrag(L + base + wpo + k * 2048 + ((nt ^ (q4_ >> 1)) * 64), 1024);
    };
    const float gate = *(LAS const float*)(L + L_G + (kh * 32 + r32_) * 4);
    lds_wait(L + L_PAIR + (w ^ 1) * 4, 1);
    {
        LAS const char* EXR = L + L_EX + (w ^ 1) * 2304 + lane_ * 16;
        const f32x4 e0 = *(LAS const f32x4*)EXR, e1 = *(LAS const f32x4*)(EXR + 1024);
        const float e2 = *(LAS const float*)(L + L_EX + (w ^ 1) * 2304 + 2048 + lane_ * 4);
        SBAR();
        wreads(L_WOB, 0, 4);
        SBAR();
        const float l = ob[8] + e2;
#pragma unroll
        for (int i = 0; i < 4; ++i) { ob[i] += e0[i]; ob[4 + i] += e1[i]; }
        float rl = __builtin_amdgcn_rcpf(l), t = fmx(l, rl) * 1.0e19f;
#pragma unroll
        for (int i = 0; i < 8; i += 2) t = fmx(fmx(t, __builtin_fabsf(ob[i])), __builtin_fabsf(ob[i + 1]));
        if (__builtin_expect(__any(!(t < 1.0e38f)), 0)) {
            ob = f32x16{};
            float m = -INFINITY;
            for (int t2 = 0; t2 < 64; ++t2) {
                const unsigned so = kvb + (t2 >> 1) * BLK, fl = (t2 & 1) * 2048;
                const bf16x8 ones = {0x3F80, 0x3F80, 0x3F80, 0x3F80, 0x3F80, 0x3F80, 0x3F80, 0x3F80};
                const bf16x8 kf = ld16(rs, vK ^ fl, so), v0l = ld16(rs, vV ^ fl, so), v1l = ld16(rs, vV ^ fl, so + 512);
                const bf16x8 v0 = r32_ < 16 ? v0l : ones, v1 = r32_ < 16 ? v1l : ones;
                f32x16 s = MFMA(kf, qb, f32x16{});
                const float mn = fmx(max16(s), m), al = EXP2(m - mn);
                m = mn;
#pragma unroll
                for (int i = 0; i < 16; ++i) { ob[i] *= al; s[i] = EXP2(s[i] - mn); }
                ob = PV(v0, s, 0, ob);
                ob = PV(v1, s, 1, ob);
            }
            rl = __builtin_amdgcn_rcpf(ob[8]);
        }
        rl *= gate;
#pragma unroll
        for (int i = 0; i < 8; ++i) ob[i] *= rl;
        st16(rs, __builtin_bit_cast(bf16x8, __builtin_elementwise_min(__builtin_bit_cast(u32x4, pack8(ob, 0)), (u32x4){0xFFFFFFFEu, 0xFFFFFFFEu, 0xFFFFFFFEu, 0xFFFFFFFEu})),
             lane_ * 16, WS_O + ((2 * tl + kh) * 32 + head) * 1024);
        SBAR();
    }
    if (lane_ == 0) __hip_atomic_fetch_add((LAS unsigned*)(L + L_STORED), 1u, __ATOMIC_RELAXED, __HIP_MEMORY_SCOPE_WORKGROUP);
    f32x16 acc[2];
    f32x4 keep0 = {}, keep1 = {};
    unsigned dst = (64 * otl + 32 * ot + r32_) * 1024 + 128 * ocg + 32 * kq + 16 * h_;
    int src[3];
#pragma unroll
    for (int z = 0; z < 3; ++z) src[z] = L_WOB + ((kq + 1 + z) & 3) * 16384 + ot * 8192 + kq * 2048 + lane_ * 16;
    {
        bf16x8 of[8];
        auto loads = [&](int g) {
#pragma unroll
            for (int k = 4 * g; k < 4 * g + 4; ++k) of[k] = ld16(rs, lane_ * 16 + (k & 3) * 1024, oao + (k & 4) * 1024);
        };
        const bf16x8 none = {-1, -1, -1, -1, -1, -1, -1, -1};
        unsigned rdy = (unsigned)__ballot(pr0 != 0xFFFFFFFFu);
#pragma unroll
        for (int g = 0; g < 2; ++g) { if (((rdy >> (4 * g)) & 15) == 15) loads(g); else of[4 * g] = none; }
        SBAR();
#pragma unroll
        for (int k = 0; k < 8; ++k) {
#pragma unroll
            for (int nt = 0; nt < 2; ++nt) acc[nt] = MFMA(wv[k][nt], ofb[k], k ? acc[nt] : f32x16{});
            SBAR();
            if (k < 4) wreads(L_WOB, k + 4, k + 5); else wreads(L_WOA, k - 4, k - 3);
            SBAR();
        }
        wreads(L_WOA, 4, 8);
        lds_arrive(L + L_PAIR + w * 4, lane_);
        SBAR();
        auto missing = [&](int g) {
            const u32x4 v = __builtin_elementwise_max(__builtin_elementwise_max(__builtin_bit_cast(u32x4, of[4 * g]), __builtin_bit_cast(u32x4, of[4 * g + 1])),
                                                      __builtin_elementwise_max(__builtin_bit_cast(u32x4, of[4 * g + 2]), __builtin_bit_cast(u32x4, of[4 * g + 3])));
            return (bool)__any(__builtin_elementwise_max(__builtin_elementwise_max(v[0], v[1]), __builtin_elementwise_max(v[2], v[3])) == 0xFFFFFFFFu);
        };
        asm volatile("" : "+v"(dst), "+v"(src[0]), "+v"(src[1]), "+v"(src[2]));
        bool have1 = ((rdy >> 4) & 15) == 15;
#pragma unroll
        for (int g = 0; g < 2; ++g) {
#pragma unroll 1
            for (unsigned spins = 0; spins < SPIN_CAP && missing(g); ++spins) {
                rdy = lds_word(L + L_STORED) >= 8 ? 0xFFu : probe();
                asm volatile("" ::: "memory");
                if (g == 0 && !have1 && ((rdy >> 4) & 15) == 15) { loads(1); have1 = true; }
                if (((rdy >> (4 * g)) & 15) == 15) loads(g); else __builtin_amdgcn_s_sleep(RETRY_SLEEP);
            }
            if (g == 0) {
#pragma unroll
                for (int k = 0; k < 4; ++k) { acc[0] = MFMA(wv[k][0], of[k], acc[0]); acc[1] = MFMA(wv[k][1], of[k], acc[1]); }
                lds_wait(L + L_PAIR + (w ^ 1) * 4, 2);
            }
        }
        LAS char* PR = L + L_WOB + kq * 16384 + ot * 8192 + lane_ * 16;
#pragma unroll
        for (int nt = 0; nt < 2; ++nt)
#pragma unroll
            for (int k = 4; k < 8; ++k) acc[nt] = MFMA(wv[k][nt], of[k], acc[nt]);
        SBAR();
#pragma unroll
        for (int z = 0; z < 4; ++z) {
            const int b = 8 * (z & 1);
            const f32x4 u0 = {acc[z >> 1][b], acc[z >> 1][b + 1], acc[z >> 1][b + 2], acc[z >> 1][b + 3]}, u1 = {acc[z >> 1][b + 4], acc[z >> 1][b + 5], acc[z >> 1][b + 6], acc[z >> 1][b + 7]};
            if (z != kq) { *(LAS f32x4*)(PR + z * 2048) = u0; *(LAS f32x4*)(PR + z * 2048 + 1024) = u1; } else { keep0 = u0; keep1 = u1; }
        }
    }
    BAR();
    {
        f32x4 u[3][2];
#pragma unroll
        for (int z = 0; z < 3; ++z) { u[z][0] = *(LAS const f32x4*)(L + src[z]); u[z][1] = *(LAS const f32x4*)(L + src[z] + 1024); }
        SBAR();
        const f32x4 r0 = ((keep0 + u[0][0]) + u[1][0]) + u[2][0], r1 = ((keep1 + u[0][1]) + u[1][1]) + u[2][1];
        unsigned a0 = pk2(r0[0], r0[1]), a1 = pk2(r0[2], r0[3]), b0 = pk2(r1[0], r1[1]), b1 = pk2(r1[2], r1[3]);
        auto s0_ = __builtin_amdgcn_permlane32_swap(a0, b0, false, false);
        auto s1_ = __builtin_amdgcn_permlane32_swap(a1, b1, false, false);
        *(u32x4*)((char*)out + dst) = (u32x4){s0_[0], s1_[0], s0_[1], s1_[1]};
    }
    if (blockIdx.x == 0 && w == 0 && lane_ == 0) set_flag(ws, WS_EP, tag);
}

extern "C" void kernel_launch(void* const* d_in, const int* in_sizes, int n_in, void* d_out, int out_size, void* d_ws, size_t ws_size, hipStream_t stream) {
    fused_kernel<<<256, 768, 0, stream>>>((const uint16_t*)d_in[0], (const uint16_t*)d_in[1], (const uint16_t*)d_in[4], (const uint16_t*)d_in[5], (const uint16_t*)d_in[7],
                                          (const uint16_t*)d_in[2], (const uint16_t*)d_in[6], (uint16_t*)d_out, (char*)d_ws);
}
```

```cpp
#include <hip/hip_runtime.h>
#include <hip/hip_bf16.h>
#include <stdint.h>

typedef short bf16x8 __attribute__((ext_vector_type(8)));
typedef short s16x4 __attribute__((ext_vector_type(4)));
typedef float f32x16 __attribute__((ext_vector_type(16)));
typedef float f32x4 __attribute__((ext_vector_type(4)));
typedef float f32x2 __attribute__((ext_vector_type(2)));
typedef unsigned u32x4 __attribute__((ext_vector_type(4)));
typedef unsigned u32x2 __attribute__((ext_vector_type(2)));
typedef __bf16 bf16x2_t __attribute__((ext_vector_type(2)));
typedef unsigned long long u64;
#define LAS __attribute__((address_space(3)))
#define GAS __attribute__((address_space(1)))
#define DI __device__ __forceinline__
#define MFMA(a, b, c) __builtin_amdgcn_mfma_f32_32x32x16_bf16((a), (b), (c), 0, 0, 0)
typedef __bf16 bfx8 __attribute__((ext_vector_type(8)));
typedef __bf16 bfx16 __attribute__((ext_vector_type(16)));
#define PV(a, S, half, c) __builtin_amdgcn_smfmac_f32_32x32x32_bf16(__builtin_bit_cast(bfx8, a), \
    __builtin_bit_cast(bfx16, __builtin_shufflevector(S, S, 8 * half, 8 * half + 1, 8 * half + 2, 8 * half + 3, 8 * half + 4, 8 * half + 5, 8 * half + 6, 8 * half + 7)), (c), 0xDDDD, 0, 0)
#define SBAR() __builtin_amdgcn_sched_barrier(0)
#define RLX_AGENT __ATOMIC_RELAXED, __HIP_MEMORY_SCOPE_AGENT
#define EXP2(x) __builtin_amdgcn_exp2f(x)

constexpr float LOG2E = 1.4426950408889634f;
constexpr unsigned BLK = 16384;
constexpr unsigned WS_KV = 0;
constexpr unsigned WS_O = WS_KV + 256 * BLK;
constexpr unsigned WS_PK = WS_O + (4u << 20);
constexpr unsigned WS_KVF = WS_PK + 32 * 32 * 2176;
constexpr unsigned WS_F1 = WS_KVF + 32 * 2176;
constexpr unsigned WS_F2 = WS_F1 + 2048;
constexpr unsigned WS_F3 = WS_F2 + 8192;
constexpr unsigned WS_F5 = WS_F3 + 8192;
constexpr unsigned WS_EP = WS_F5 + 8192;
constexpr unsigned WS_END = WS_EP + 128;
constexpr int L_X = 0, L_RING = 65536;
constexpr int L_PHK = 65536, L_PHQ = 81920, L_VB = 16384;
constexpr int L_WOA = 0, L_WOB = 65536;
constexpr int L_WPHI = L_RING + 3 * 16384;
constexpr int L_G = 131072, L_SYNC = L_G + 256, L_STORED = L_SYNC + 4, L_HCNT = L_SYNC + 8, L_OBS = L_SYNC + 12, L_PAIR = L_SYNC + 16, L_ZEROS = L_SYNC + 96, L_ONES = L_SYNC + 128, L_DUMMY = L_SYNC + 256, L_G2 = L_SYNC + 512;
constexpr int L_QA = L_G + 1024, L_EX = L_QA + 8192;
constexpr int LDS_BYTES = 163840;
constexpr int F1_STEP = 14, PRIO_SWITCH = 23, PK_DELAY = 24, WO_T0 = 6400, WO_DT = 270, POLL_GAP = 2, POLL_FIRST = 6, RETRY_SLEEP = 2, PUB_DELAY = 4;
constexpr int CODE_LINES = 257;
constexpr unsigned SPIN_CAP = 1u << 20;

template <int N> struct IC { static constexpr int value = N; };
template <int I, int N, class F> DI void static_for(F&& f) { if constexpr (I < N) { f(IC<I>{}); static_for<I + 1, N>(f); } }
DI unsigned pk2(float a, float b) { f32x2 v = {a, b}; return __builtin_bit_cast(unsigned, __builtin_convertvector(v, bf16x2_t)); }
DI bf16x8 pack8(const f32x16& x, int s) {
    u32x4 p = {pk2(x[8 * s], x[8 * s + 1]), pk2(x[8 * s + 2], x[8 * s + 3]), pk2(x[8 * s + 4], x[8 * s + 5]), pk2(x[8 * s + 6], x[8 * s + 7])};
    return __builtin_bit_cast(bf16x8, p);
}
DI float fmx(float a, float b) { return __builtin_elementwise_maximum(a, b); }
DI float max32(float v) { auto r = __builtin_amdgcn_permlane32_swap(__float_as_uint(v), __float_as_uint(v), false, false); return fmx(__uint_as_float(r[0]), __uint_as_float(r[1])); }
DI float lmax16(const f32x16& s) {
    const float a = fmx(fmx(s[0], s[1]), s[2]), b = fmx(fmx(s[3], s[4]), s[5]), c = fmx(fmx(s[6], s[7]), s[8]), d = fmx(fmx(s[9], s[10]), s[11]), e = fmx(fmx(s[12], s[13]), s[14]);
    return fmx(fmx(fmx(a, b), c), fmx(fmx(d, e), s[15]));
}
DI float max16(const f32x16& s) { return max32(lmax16(s)); }
DI void glds2(unsigned v0, unsigned v1, const void* sbase, unsigned ldsdst) {
    unsigned keep;
    asm volatile("s_mov_b32 %0, m0\n\ts_mov_b32 m0, %4\n\ts_nop 0\n\tglobal_load_lds_dwordx4 %1, %3\n\tglobal_load_lds_dwordx4 %2, %3 offset:1024\n\ts_mov_b32 m0, %0"
                 : "=&s"(keep) : "v"(v0), "v"(v1), "s"(sbase), "s"(ldsdst) : "memory");
}
DI void glds4(const unsigned (&v)[4], const void* sbase, unsigned ldsdst) {
    unsigned keep;
    asm volatile("s_mov_b32 %0, m0\n\ts_mov_b32 m0, %6\n\ts_nop 0\n\tglobal_load_lds_dwordx4 %1, %5\n\tglobal_load_lds_dwordx4 %2, %5 offset:1024\n\t"
                 "global_load_lds_dwordx4 %3, %5 offset:2048\n\tglobal_load_lds_dwordx4 %4, %5 offset:3072\n\ts_mov_b32 m0, %0"
                 : "=&s"(keep) : "v"(v[0]), "v"(v[1]), "v"(v[2]), "v"(v[3]), "s"(sbase), "s"(ldsdst) : "memory");
}
DI void touch(unsigned voff, const void* sbase, unsigned ldsdst) {
    unsigned keep;
    asm volatile("s_mov_b32 %0, m0\n\ts_mov_b32 m0, %3\n\ts_nop 0\n\tglobal_load_lds_dword %1, %2 sc1\n\ts_mov_b32 m0, %0"
                 : "=&s"(keep) : "v"(voff), "s"(sbase), "s"(ldsdst) : "memory");
}
typedef short v4i16_t __attribute__((ext_vector_type(4)));
DI s16x4 trread(LAS const char* p) { return __builtin_bit_cast(s16x4, __builtin_amdgcn_ds_read_tr16_b64_v4i16((LAS v4i16_t*)p)); }
DI bf16x8 trfrag(LAS const char* p, int d2) { s16x4 lo = trread(p), hi = trread(p + d2); return __builtin_shufflevector(lo, hi, 0, 1, 2, 3, 4, 5, 6, 7); }
DI bf16x8 ldsfrag(LAS const char* p) { return *(LAS const bf16x8*)p; }
#define BAR() asm volatile("s_waitcnt lgkmcnt(0)\n\ts_barrier" ::: "memory")
#define VM_BAR(N) asm volatile("s_waitcnt vmcnt(" #N ") lgkmcnt(0)\n\ts_barrier" ::: "memory")
#define DRAIN() asm volatile("s_waitcnt vmcnt(0)" ::: "memory")
typedef __amdgpu_buffer_rsrc_t rsrc_t;
DI bf16x8 ld16(rsrc_t r, unsigned voff, unsigned soff) { return __builtin_bit_cast(bf16x8, __builtin_amdgcn_raw_buffer_load_b128(r, voff, soff, 16)); }
DI void st16(rsrc_t r, bf16x8 v, unsigned voff, unsigned soff) { __builtin_amdgcn_raw_buffer_store_b128(__builtin_bit_cast(u32x4, v), r, voff, soff, 16); }
DI unsigned lds_word(LAS char* p) { return __builtin_amdgcn_readfirstlane(*(LAS volatile unsigned*)p); }
DI void lds_wait(LAS char* p, unsigned want) {
#pragma unroll 1
    for (unsigned spins = 0; spins < (SPIN_CAP << 6) && lds_word(p) < want; ++spins) __builtin_amdgcn_s_sleep(1);
    asm volatile("" ::: "memory");
}
DI void lds_arrive(LAS char* p, int lane) {
    asm volatile("s_waitcnt lgkmcnt(0)" ::: "memory");
    if (lane == 0) __hip_atomic_fetch_add((LAS unsigned*)p, 1u, __ATOMIC_RELAXED, __HIP_MEMORY_SCOPE_WORKGROUP);
}
DI void set_flag(char* ws, unsigned off, u64 tag) { __hip_atomic_store((GAS u64*)(ws + off), tag, RLX_AGENT); }
template <int FIRST, int GAP> DI void wait_flags(char* ws, unsigned off, int n, u64 tag, int lane) {
    GAS u64* p = (GAS u64*)(ws + off) + (lane < n ? lane : 0);
    if (FIRST) __builtin_amdgcn_s_sleep(FIRST);
#pragma unroll 1
    for (unsigned spins = 0; spins < SPIN_CAP; ++spins) {
        const u64 v = __hip_atomic_load(p, RLX_AGENT);
        if (__all(v == tag)) break;
        __builtin_amdgcn_s_sleep(GAP);
    }
    __builtin_amdgcn_fence(__ATOMIC_ACQUIRE, "wavefront");
    asm volatile("" ::: "memory");
}

DI void softmax16(const f32x16& p, bf16x8 (&out)[2]) {
#define KEEP1(x) asm("" : "+v"(x))
    const float nm = -lmax16(p) * LOG2E;
    float e[16], t[8];
#pragma unroll
    for (int i = 0; i < 16; ++i) { e[i] = __builtin_fmaf(p[i], LOG2E, nm); KEEP1(e[i]); e[i] = EXP2(e[i]); }
#pragma unroll
    for (int i = 0; i < 8; ++i) { t[i] = e[2 * i] + e[2 * i + 1]; KEEP1(t[i]); }
#pragma unroll
    for (int i = 0; i < 4; ++i) { t[i] = t[2 * i] + t[2 * i + 1]; KEEP1(t[i]); }
    t[0] += t[1]; KEEP1(t[0]); t[2] += t[3]; KEEP1(t[2]);
    const float rs = __builtin_amdgcn_rcpf(t[0] + t[2]);
    u32x4 u[2];
#pragma unroll
    for (int i = 0; i < 16; ++i) { e[i] *= rs; KEEP1(e[i]); }
#pragma unroll
    for (int i = 0; i < 8; ++i) u[i >> 2][i & 3] = pk2(e[2 * i], e[2 * i + 1]);
    out[0] = __builtin_bit_cast(bf16x8, u[0]); out[1] = __builtin_bit_cast(bf16x8, u[1]);
}

__global__ __launch_bounds__(768, 3) void fused_kernel(const uint16_t* __restrict__ x, const uint16_t* __restrict__ WA, const uint16_t* __restrict__ WB,
                                                    const uint16_t* __restrict__ wphi, const uint16_t* __restrict__ gatew,
                                                    const uint16_t* __restrict__ woA, const uint16_t* __restrict__ woB,
                                                    uint16_t* __restrict__ out, char* ws) {
    __shared__ __attribute__((aligned(16))) char lds[LDS_BYTES];
    LAS char* L = (LAS char*)lds;
    u64 pc0; asm volatile("s_getpc_b64 %0" : "=s"(pc0));
    const unsigned lds0 = (unsigned)(uintptr_t)lds;
    const int tid = threadIdx.x, lane = tid & 63, w = __builtin_amdgcn_readfirstlane(tid >> 6);
    const int r32 = lane & 31, h = lane >> 5;
    const int xcd = blockIdx.x & 7, idx = blockIdx.x >> 3;
    const int qd = 2 * (xcd & 3) + (idx & 1), tl = 2 * (idx >> 1) + (xcd >> 2);
    const int s0 = ((tl >> 1) + 4 * (qd & 1)) & 7;
    const int otl = 4 * xcd + (idx >> 3), ocg = idx & 7;
    const rsrc_t rs = __builtin_amdgcn_make_buffer_rsrc(ws, 0, WS_END, 0x00020000);
    u64 tag = 0; u32x2 ep;
#define LOAD_EPOCH() asm volatile("global_load_dwordx2 %0, %1, %2 sc1" : "=v"(ep) : "v"(0), "s"(ws + WS_EP) : "memory")
#define MAKE_TAG() do { asm volatile("" : "+v"(ep)); tag = ((u64)__builtin_amdgcn_readfirstlane(ep[1]) << 32 | (unsigned)__builtin_amdgcn_readfirstlane(ep[0])) + 1; } while (0)
    const int q4 = (lane & 15) >> 2;
    const int trl = ((lane >> 4) & 1) * 32 + (lane & 3) * 8;
    asm volatile("" :: "s"(x), "s"(WA), "s"(WB), "s"(wphi), "s"(gatew), "s"(ws));
    if (__builtin_expect(w >= 8, 1)) {
        const int j = w - 8;
        const unsigned vsw = (unsigned)(((lane & 7) ^ (((lane >> 4) & 1) << 2)) << 4);
        unsigned vw[4];
#pragma unroll
        for (int u = 0; u < 4; ++u) vw[u] = (unsigned)((lane >> 3) * 3072) + vsw + u * (8 * 3072 - 1024);
        const unsigned vx0 = (unsigned)((lane >> 3) * 1024 + (((lane & 7) ^ (lane >> 4)) << 4)), vx1 = (vx0 ^ 64u) + 8192 - 1024;
        const char* xb = (const char*)x + (size_t)(64 * tl + 16 * j) * 1024;
        const size_t wo_ = (size_t)((j & 1) * 32) * 3072 + qd * 128;
        const char* wb[3] = {(const char*)WA + wo_ + 1024 + (j >> 1) * 1024, (const char*)WB + wo_ + (j >> 1) * 1024, (j >> 1) ? (const char*)WB + wo_ + 2048 : (const char*)WA + wo_};
        auto issue_x = [&](int i) { const int kc = (s0 + i) & 7; glds2(vx0, vx1, xb + kc * 128, lds0 + L_X + i * 8192 + j * 2048); };
        auto issue_w = [&](int i) { glds4(vw, wb[i >> 3] + ((s0 + i) & 7) * (64 * 3072), lds0 + L_RING + ((i + 4) % 6) * 16384 + j * 4096); };
        __builtin_amdgcn_s_setprio(3);
        issue_w(0); issue_x(0); issue_x(1); issue_x(2); issue_x(3);
        bf16x8 gw; unsigned warm;
        asm volatile("global_load_dwordx4 %0, %1, %2" : "=v"(gw) : "v"((lane & 15) * 64 + (lane >> 4) * 16), "s"(gatew) : "memory");
        glds2(lane * 16, lane * 16, wphi + qd * 1024, lds0 + L_WPHI);
        issue_w(1);
        int laneh = lane; asm volatile("" : "+v"(laneh));
        const int r32h = laneh & 31, hh = laneh >> 5, q4h = (laneh & 15) >> 2, trlh = ((laneh >> 4) & 1) * 32 + (laneh & 3) * 8;
        const unsigned vswh = (unsigned)(((laneh & 7) ^ (((laneh >> 4) & 1) << 2)) << 4);
        const int head = 4 * qd + j;
        const bool keep = (r32h >> 4) == (j & 1);
        constexpr unsigned OOB = 0x40000000u;
        unsigned a_ve[4], a_pf[2], a_kf[4], vo[4];
#pragma unroll
        for (int u = 0; u < 4; ++u) {
            a_ve[u] = keep ? L_VB + (j >> 1) * 4096 + u * 1024 + laneh * 16 : L_ONES;
            const unsigned n = u * 136 + ((r32h < 16 ? r32h : 16) * 2 + hh) * 4;
            a_kf[u] = (n >> 5) * 128 + (n & 31) * 2;
            vo[u] = (unsigned)((laneh >> 3) * 1024) + vswh + u * (8192 - 1024);
            asm volatile("" : "+v"(a_ve[u]), "+v"(a_kf[u]), "+v"(vo[u]));
        }
        a_pf[0] = L_PHK + j * 4096 + (4 * hh + q4h) * 64 + (trlh ^ ((2 * hh + (q4h >> 1)) * 8)); a_pf[1] = (a_pf[0] ^ 32) + 512;
        unsigned a_l16 = laneh * 16, a_q = L_PHQ + j * 4096 + laneh * 16, a_g2 = L_G2 + r32h * 4;
        unsigned v_pk = (keep || (r32h & 15) == 0) ? (unsigned)(((keep ? (r32h & 15) : 16) * 2 + hh) * 16) : OOB;
        unsigned v_one = laneh == 0 ? 0u : OOB, v_p32 = r32h * 8, v_p17 = (laneh < 17 ? laneh : 0) * 8, v_p8 = (laneh & 7) * 8;
        unsigned v_h1 = hh * 8704 + r32h * 4, v_h1s = (laneh < 32 && !(laneh & 1)) ? laneh * 2 : OOB, v_tch = ((laneh >> 3) & 1) * 32768 + (laneh & 7) * 128;
        bf16x8 none = {-1, -1, -1, -1, -1, -1, -1, -1};
        asm volatile("" : "+v"(a_pf[0]), "+v"(a_pf[1]), "+v"(a_l16), "+v"(a_q), "+v"(a_g2), "+v"(v_pk), "+v"(v_one), "+v"(v_p32), "+v"(v_p17), "+v"(v_p8));
        asm volatile("" : "+v"(v_h1), "+v"(v_h1s), "+v"(v_tch), "+v"(none));
        VM_BAR(13); issue_w(2); issue_w(3);
        VM_BAR(8); issue_x(4); issue_w(4);
        VM_BAR(10); issue_x(5); issue_w(5);
        VM_BAR(12); issue_x(6); issue_w(6);
        VM_BAR(12); issue_x(7); issue_w(7);
        VM_BAR(12);
        asm volatile("global_load_dword %0, %1, %2" : "=v"(warm) : "v"(((tl >> 1) * 32 + (lane & 15)) * 3072), "s"(wb[2] - (j & 1) * 16 * 3072) : "memory");
        LOAD_EPOCH();
        issue_w(8);
        VM_BAR(12);
        VM_BAR(6); issue_w(9); issue_w(10); issue_w(11);
        u32x2 tagv;
        f32x4 gl = {};
        const int grow = 16 * j + (lane & 15);
        int gxa = L_X + grow * 128 + (((lane >> 4) ^ ((grow >> 1) & 7)) << 4);
        u64 f1early = 0;
        unsigned a_gt = ((lane & 16) ? L_G2 : L_G) + grow * 4;
        asm volatile("" : "+v"(a_gt));
#pragma unroll
        for (int i = 8; i < 24; i += 2) {
            if (i < 22) VM_BAR(8); else VM_BAR(0);
            if (i == 8) asm volatile("" : "+v"(gw), "+v"(warm));
            bf16x8 xg[4];
            if (i < 16) {
#pragma unroll
                for (int u = 0; u < 4; ++u) xg[u] = ldsfrag(L + (gxa ^ ((u & 1) << 6)) + (i - 8 + (u >> 1)) * 8192);
            }
            SBAR();
            if (i < 20) { issue_w(i + 4); issue_w(i + 5); }
            SBAR();
            if (i < 16) {
                asm volatile("" : "+v"(gw));
#pragma unroll
                for (int u = 0; u < 4; ++u)
                    gl = __builtin_amdgcn_mfma_f32_16x16x32_bf16((lane & 15) == 2 * ((s0 + i + (u >> 1)) & 7) + (u & 1) ? gw : bf16x8{}, xg[u], gl, 0, 0, 0);
                asm volatile("" : "+v"(gl));
            }
            if (j == 0 && i == 22) {
                if (!__all(f1early == tag)) wait_flags<0, 2>(ws, WS_F1 + qd * 256, 32, tag, lane);
                touch(a_l16 * 8, ws + WS_KV + (qd * 32 + ((tl + 1) & 31)) * BLK, lds0 + L_DUMMY);
                touch(a_l16 * 8, ws + WS_KV + (qd * 32 + ((tl + 2) & 31)) * BLK, lds0 + L_DUMMY);
            }
            if (i == 22) {
                float z = (gl[0] + gl[1]) + (gl[2] + gl[3]);
                z += __shfl_xor(z, 16); z += __shfl_xor(z, 32);
                const float g = __builtin_amdgcn_rcpf(1.f + EXP2(-z * LOG2E));
                *(LAS float*)(L + a_gt) = (lane & 16) ? 1.f - g : g;
            }
            if (i == 16) { MAKE_TAG(); tagv = (u32x2){(unsigned)tag, (unsigned)(tag >> 32)}; asm volatile("" : "+v"(tagv)); }
            if (j == 0 && i == 20) {
                f1early = __hip_atomic_load((GAS u64*)(ws + WS_F1 + qd * 256) + (lane & 31), RLX_AGENT);
                if (lane < 36) *(LAS unsigned*)(L + L_SYNC + lane * 4) = lane >= 32 ? 0x3F803F80u : 0u;
            }
            SBAR();
        }
        auto publish_flag = [&](unsigned off) { DRAIN(); __builtin_amdgcn_raw_buffer_store_b64(tagv, rs, v_one, off, 16); };
        BAR();
        const u64 tp2 = __builtin_amdgcn_s_memtime();
        __builtin_amdgcn_s_sleep(6);
        bf16x8 phq[2][2]; float g2[2];
        {
            bf16x8 pf[4], ve[4];
#pragma unroll
            for (int u = 0; u < 4; ++u) {
                pf[u] = __builtin_shufflevector(trread(L + a_pf[0] + u * 1024), trread(L + a_pf[1] + u * 1024), 0, 1, 2, 3, 4, 5, 6, 7);
                ve[u] = ldsfrag(L + a_ve[u]);
                phq[u >> 1][u & 1] = ldsfrag(L + a_q + u * 1024);
            }
            g2[0] = *(LAS const float*)(L + a_g2); g2[1] = *(LAS const float*)(L + a_g2 + 128);
            lds_arrive(L + L_HCNT, lane);
            __builtin_amdgcn_s_sleep(PK_DELAY);
            touch(v_p8 * 16, (const char*)(j & 2 ? woA : woB) + blockIdx.x * 2048 + (j & 1) * 1024, lds0 + L_DUMMY);
            f32x16 kv = MFMA(pf[0], ve[0], f32x16{});
#pragma unroll
            for (int u = 1; u < 4; ++u) kv = MFMA(pf[u], ve[u], kv);
#pragma unroll
            for (int g = 0; g < 4; ++g) st16(rs, __builtin_bit_cast(bf16x8, (f32x4){kv[4 * g], kv[4 * g + 1], kv[4 * g + 2], kv[4 * g + 3]}), v_pk, WS_PK + ((qd * 32 + tl) * 4 + j) * 2176 + g * 544);
            publish_flag(WS_F2 + (head * 32 + tl) * 8);
        }
        lds_wait(L + L_HCNT, 4);
        int wo_done = 0;
        auto wo_service = [&]() {
            if (wo_done < 8 && __builtin_amdgcn_s_memtime() - tp2 >= (u64)(WO_T0 + WO_DT * (j + 4 * wo_done))) {
                const int mtx = wo_done >> 2, pc = wo_done & 3;
                glds4(vo, (const char*)(mtx ? woA : woB) + (size_t)(128 * j) * 1024 + ocg * 128 + pc * 32768, lds0 + (mtx ? L_WOA : L_WOB) + j * 16384 + pc * 4096);
                ++wo_done;
            }
        };
        auto wait_slow = [&](unsigned off, unsigned v, int first) {
#pragma unroll 1
            for (unsigned spins = 0; spins < SPIN_CAP; ++spins) {
#pragma unroll 1
                for (int z = 0; z < (spins ? POLL_GAP : first); ++z) { wo_service(); __builtin_amdgcn_s_sleep(6); }
                asm volatile("" ::: "memory");
                if (__all(__builtin_bit_cast(u64, __builtin_amdgcn_raw_buffer_load_b64(rs, v, off, 16)) == tag)) break;
            }
            __builtin_amdgcn_fence(__ATOMIC_ACQUIRE, "wavefront");
            asm volatile("" ::: "memory");
        };
        if (tl < 17) {
            wait_slow(WS_F2 + head * 256, v_p32, POLL_FIRST);
            float t[16];
#pragma unroll
            for (int i = 0; i < 16; ++i) t[i] = __uint_as_float(__builtin_amdgcn_raw_buffer_load_b32(rs, v_h1, WS_PK + (qd * 128 + j) * 2176 + tl * 128 + i * 17408, 16));
#pragma unroll
            for (int n = 8; n >= 1; n >>= 1)
#pragma unroll
                for (int i = 0; i < n; ++i) { t[i] += t[i + n]; KEEP1(t[i]); }
            auto r = __builtin_amdgcn_permlane32_swap(__float_as_uint(t[0]), __float_as_uint(t[0]), false, false);
            const float x0 = __uint_as_float(r[0]) + __uint_as_float(r[1]);
            const float x1 = __uint_as_float(__builtin_amdgcn_update_dpp(0, __float_as_uint(x0), 0xF5, 0xF, 0xF, true));
            __builtin_amdgcn_raw_buffer_store_b32(pk2(x0, x1), rs, v_h1s, WS_KVF + head * 2176 + tl * 128, 16);
            publish_flag(WS_F3 + (head * 32 + tl) * 8);
        }
        wait_slow(WS_F3 + head * 256, v_p17, tl < 17 ? POLL_FIRST : 2 * POLL_FIRST + 4);
        {
            u32x2 kq_[4];
#pragma unroll
            for (int g = 0; g < 4; ++g) kq_[g] = __builtin_amdgcn_raw_buffer_load_b64(rs, a_kf[g], WS_KVF + head * 2176, 16);
            const bf16x8 k0 = __builtin_bit_cast(bf16x8, __builtin_shufflevector(kq_[0], kq_[1], 0, 1, 2, 3)), k1 = __builtin_bit_cast(bf16x8, __builtin_shufflevector(kq_[2], kq_[3], 0, 1, 2, 3));
#pragma unroll
            for (int tt = 0; tt < 2; ++tt) {
                f32x16 ob = MFMA(k0, phq[tt][0], f32x16{});
                ob = MFMA(k1, phq[tt][1], ob);
                const float rd = g2[tt] * __builtin_amdgcn_rcpf(ob[8] + 1e-6f);
#pragma unroll
                for (int i = 0; i < 8; ++i) { float z = ob[i] * rd; KEEP1(z); ob[i] = z; }
                st16(rs, pack8(ob, 0), a_l16, WS_O + (2u << 20) + ((2 * tl + tt) * 32 + head) * 1024);
                st16(rs, none, a_l16, WS_O + ((2 * tl + tt) * 32 + head) * 1024);
            }
            publish_flag(WS_F5 + (tl * 32 + head) * 8);
        }
        wait_slow(WS_F5 + (otl * 32 + 8 * j) * 8, v_p8, POLL_FIRST);
        touch(v_tch, ws + WS_O + (2u << 20) + (2 * otl * 32 + 8 * j + ocg) * 1024, lds0 + L_DUMMY);
        while (wo_done < 8) { wo_service(); __builtin_amdgcn_s_sleep(6); }
        DRAIN();
        lds_arrive(L + L_OBS, lane);
        return;
    }

    if (w == 0) {
        const char* a = (const char*)(pc0 & ~127ull) + (idx * 9 + lane < CODE_LINES ? idx * 9 + lane : 0) * 128;
        if (lane == 9) {
            a = ws + (size_t)(idx < 21 ? idx : 0) * 524288;
            if (idx >= 22) a = idx < 25 ? (const char*)WB + (idx - 22) * 786368 : idx < 28 ? (const char*)out + (idx - 25) * 1048512 : idx < 30 ? (const char*)woA + (idx - 28) * 524224 : (const char*)woB + (idx - 30) * 524224;
        }
        unsigned keep;
        if (lane < 10) asm volatile("s_mov_b32 %0, m0\n\ts_mov_b32 m0, %2\n\ts_nop 0\n\tglobal_load_lds_dword %1, off sc1\n\ts_mov_b32 m0, %0" : "=&s"(keep) : "v"(a), "s"(lds0 + L_WPHI + 4096) : "memory");
    }
    const int p = w & 1;
    const int hq = w >> 1, kh = w & 1, head = 4 * qd + hq;
    const unsigned vK = w * 2048 + lane * 16;
    const unsigned vV = (unsigned)(w * 2048 + 1024 + h * 256 + (r32 & 15) * 16);
    const unsigned kvb = WS_KV + qd * 32 * BLK;
#define KVOFF(t) (kvb + (((t) + tl) & 31) * BLK)
    bf16x8 kr[4], v0r[4], v1r[4];
    const int kpos = ((lane & 0x33) | ((lane & 4) << 1) | ((lane & 8) >> 1)) * 16;
    bf16x8 kref; unsigned ownw = 0;
    const int cst = (r32 & 11) ? L_ZEROS : L_ONES;
    {
        const int xa0 = L_X + r32 * 128 + ((h ^ ((r32 >> 1) & 7)) << 4);
        const int wa0 = L_RING + ((w >> 1) & 1) * 8192 + (8 * h + q4) * 128 + ((p ^ (q4 >> 1)) * 64) + trl;
        bf16x8 xf[2][4][2], w0[2][4];
        f32x16 acc[2];
        int wsb[3], xsb[4];
#pragma unroll
        for (int i = 0; i < 3; ++i) { wsb[i] = wa0 + i * 32768; asm volatile("" : "+v"(wsb[i])); }
#pragma unroll
        for (int s = 0; s < 4; ++s) { xsb[s] = xa0 ^ (s << 5); asm volatile("" : "+v"(xsb[s])); }
        auto gemm = [&](int TEAM, int TYPE) __attribute__((always_inline)) {
            auto mine = [&](int i) __attribute__((always_inline)) { return i >= 0 && i < 24 && ((i >> 3) & 1) == TEAM; };
            auto rx = [&](int i, int s, int t) __attribute__((always_inline)) { if (mine(i)) xf[i & 1][s][t] = ldsfrag(L + xsb[s] + (i & 7) * 8192 + t * 4096); };
            auto rw = [&](int i, int s) __attribute__((always_inline)) { if (mine(i)) w0[i & 1][s] = trfrag(L + wsb[((i + 4) % 6) >> 1] + (i & 1) * 16384 + s * 2048, 512); };
            auto mm = [&](int i, int s, int t, bool now = false) __attribute__((always_inline)) {
                SBAR();
                if (mine(i) && (i != 7 || now)) {
                    const f32x16 c = ((i & 7) == 0 && s == 0) ? f32x16{} : acc[t];
                    acc[t] = (TEAM == 0 && TYPE) ? MFMA(xf[i & 1][s][t], w0[i & 1][s], c) : MFMA(w0[i & 1][s], xf[i & 1][s][t], c);
                }
                SBAR();
            };
            bf16x8 own[2][2], wf[2], ph[4][2];
            f32x16 f[4];
            static_for<0, 25>([&](auto ic) __attribute__((always_inline)) {
                constexpr int i = ic.value;
                if (i == F1_STEP) { VM_BAR(0); if (TEAM == 0 && TYPE == 0) { MAKE_TAG(); if (tid == 0) set_flag(ws, WS_F1 + (qd * 32 + tl) * 8, tag); } }
                else if (i == 2) { __builtin_amdgcn_s_waitcnt(0x0F70); BAR(); }
                else if (i < 8 || (i < 24 && !(i & 1))) BAR();
                mm(i - 1, 0, 0); rw(i, 0); rx(i, 0, 0);
                mm(i - 1, 0, 1); rx(i, 0, 1); rw(i, 1);
                mm(i - 1, 1, 0); rx(i, 1, 0); rx(i, 1, 1);
                mm(i - 1, 1, 1); rw(i, 2); rx(i, 2, 0);
                mm(i - 1, 2, 0); rx(i, 2, 1); rw(i, 3);
                mm(i - 1, 2, 1); rx(i, 3, 0); rx(i, 3, 1);
                mm(i - 1, 3, 0);
                mm(i - 1, 3, 1);
                if (TEAM == 0 && TYPE == 0 && i == 7) LOAD_EPOCH();
                if (TEAM == 0 && i == 7) {
#pragma unroll
                    for (int z = 0; z < 8; ++z) mm(7, z >> 1, z & 1, true);
#pragma unroll
                    for (int t = 0; t < 2; ++t) {
                        if (TYPE == 0) {
#pragma unroll
                            for (int e = 0; e < 16; ++e) acc[t][e] *= 0.25f * LOG2E;
                        }
                        own[t][0] = pack8(acc[t], 0); own[t][1] = pack8(acc[t], 1);
                        asm volatile("" : "+v"(own[t][0]), "+v"(own[t][1]));
                    }
                }
                if (TEAM == 0 && i == 8) {
                    __builtin_amdgcn_s_sleep(PUB_DELAY);
#pragma unroll
                    for (int t = 0; t < 2; ++t) {
                        const unsigned so = kvb + tl * BLK + (4 * p + t) * 2048;
#pragma unroll
                        for (int a = 0; a < 2; ++a) {
                            if (TYPE == 0) st16(rs, own[t][a], kpos, so + a * 4096);
                            else st16(rs, own[t][a], (unsigned)((r32 >> 4) * 4096 + 1024 + h * 256 + (r32 & 15) * 16) + a * 512, so);
                        }
                    }
                }
                if (TEAM == 1) {
                    if (i == 1) {
                        const short sg = (lane & 4) ? (short)0x8000 : (short)0;
#pragma unroll
                        for (int a = 0; a < 2; ++a) {
                            wf[a] = trfrag(L + L_WPHI + (2 * p + a) * 512 + (4 * h + q4) * 32 + (((lane >> 4) & 1) * 2 + ((lane >> 1) & 1)) * 8, 256);
#pragma unroll
                            for (int e = 0; e < 8; ++e) wf[a][e] ^= sg;
                            asm volatile("" : "+v"(wf[a]));
                        }
                    }
                    if (i == 16) {
                        __builtin_amdgcn_s_setprio(1);
#pragma unroll
                        for (int m = 0; m < 4; ++m) f[m] = MFMA(wf[m >> 1], pack8(acc[m & 1], m >> 1), f32x16{});
                    }
                    if (i == 19) asm volatile("global_load_dword %0, %1, %2 sc1" : "=v"(ownw) : "v"(r32 * 128), "s"(ws + kvb + tl * BLK + (w - 4) * 4096) : "memory");
                    if (i >= 16 && i < 24 && !(i & 1)) { softmax16(f[(i - 16) / 2], ph[(i - 16) / 2]); asm volatile("" : "+v"(ph[(i - 16) / 2][0]), "+v"(ph[(i - 16) / 2][1])); }
                }
                SBAR();
            });
            if (TEAM == 0) {
#pragma unroll
                for (int t = 0; t < 2; ++t)
#pragma unroll
                    for (int a = 0; a < 2; ++a)
                        *(LAS bf16x8*)(L + (TYPE ? L_VB + ((p * 2 + t) * 2 + a) * 1024 : L_QA + ((2 * p + a) * 2 + t) * 1024) + lane * 16) = pack8(acc[t], a);
            } else if (w < 6) {
#pragma unroll
                for (int m = 0; m < 8; ++m) *(LAS bf16x8*)(L + L_PHQ + (8 * p + m) * 1024 + lane * 16) = ph[m >> 1][m & 1];
            } else {
#pragma unroll
                for (int m = 0; m < 8; ++m) {
                    const u32x4 u = __builtin_bit_cast(u32x4, ph[m >> 1][m & 1]);
#pragma unroll
                    for (int g = 0; g < 2; ++g)
                        *(LAS u32x2*)(L + L_PHK + (4 * p + (m >> 1)) * 2048 + r32 * 64 + 8 * ((2 * (2 * (m & 1) + g) + h) ^ ((r32 >> 1) & 7))) = (u32x2){u[2 * g], u[2 * g + 1]};
                }
            }
        };
        if (w < 2) gemm(0, 0); else if (w < 4) gemm(0, 1); else gemm(1, 0);
    }
    const u32x4 rs4 = {(unsigned)(uintptr_t)ws, (unsigned)((uintptr_t)ws >> 32) & 0xFFFFu, WS_END, 0x00020000u};
#define LDK(R, t) asm volatile("buffer_load_dwordx4 %0, %1, %2, %3 offen sc1" : "=v"(R) : "v"(vK), "s"(rs4), "s"(KVOFF(t)))
#define LDV(R, t, S) asm volatile("s_mov_b64 exec, %4\n\tbuffer_load_dwordx4 %0, %1, %2, %3 offen offset:" #S " sc1\n\ts_mov_b64 exec, -1" : "+v"(R) : "v"(vV), "s"(rs4), "s"(KVOFF(t)), "s"(0x0000FFFF0000FFFFull))
#define LD2(a, b, t) asm volatile("buffer_load_dwordx4 %0, %6, %8, %9 offen sc1\n\tbuffer_load_dwordx4 %1, %6, %8, %10 offen sc1\n\ts_mov_b64 exec, %11\n\t" \
    "buffer_load_dwordx4 %2, %7, %8, %9 offen sc1\n\tbuffer_load_dwordx4 %3, %7, %8, %9 offen offset:512 sc1\n\t" \
    "buffer_load_dwordx4 %4, %7, %8, %10 offen sc1\n\tbuffer_load_dwordx4 %5, %7, %8, %10 offen offset:512 sc1\n\ts_mov_b64 exec, -1" \
    : "=&v"(kr[a]), "=&v"(kr[b]), "+v"(v0r[a]), "+v"(v1r[a]), "+v"(v0r[b]), "+v"(v1r[b]) : "v"(vK), "v"(vV), "s"(rs4), "s"(KVOFF(t)), "s"(KVOFF((t) + 1)), "s"(0x0000FFFF0000FFFFull))
#define WAITR2(N, R, Q) asm volatile("s_waitcnt vmcnt(" #N ")" : "+v"(R), "+v"(Q))
#define WAITR(N, R) asm volatile("s_waitcnt vmcnt(" #N ")" : "+v"(R))
#pragma unroll
    for (int t = 0; t < 4; ++t) { v0r[t] = ldsfrag(L + cst); v1r[t] = ldsfrag(L + cst); asm volatile("" ::: "memory"); }
    asm volatile("buffer_load_dwordx4 %0, %1, %2, %3 offen sc1" : "=v"(kref) : "v"(vK & ~2048u), "s"(rs4), "s"(KVOFF(0)));
    LDK(kr[0], 0); LDV(v0r[0], 0, 0); LDV(v1r[0], 0, 512);
    f32x16 oa = {}, ob = {};
    bf16x8 ofb[8];
    BAR();
    const bf16x8 qa = ldsfrag(L + L_QA + (hq * 2 + (kh ^ 1)) * 1024 + lane * 16), qb = ldsfrag(L + L_QA + (hq * 2 + kh) * 1024 + lane * 16);
    asm volatile("s_waitcnt vmcnt(2)" : "+v"(kref), "+v"(kr[0]), "+v"(ownw));
    const float m0 = lmax16(MFMA(kref, qa, f32x16{})), m1 = lmax16(MFMA(kref, qb, f32x16{}));
    const int ot = w & 1, kq = w >> 1;
    {
        f32x16 negm;
        const float nm = -max32(fmx(m0, m1));
#pragma unroll
        for (int i = 0; i < 16; ++i) negm[i] = nm;
        f32x16 sa = MFMA(kr[0], qa, negm), sb = {};
        SBAR();
        LDK(kr[1], 1); LDV(v0r[1], 1, 0); LDV(v1r[1], 1, 512);
#define E(S, i) S[i] = EXP2(S[i])
#define PHASE(S, M1, M2, M3, X1, X2, X3)                                                          \
    do {                                                                                          \
        E(S, 0); E(S, 1); SBAR();                                                                 \
        M1; X1; SBAR();                                                                           \
        E(S, 2); E(S, 3); E(S, 4); E(S, 5); SBAR();                                               \
        M2; X2; SBAR();                                                                           \
        E(S, 6); E(S, 7); E(S, 8); E(S, 9); SBAR();                                               \
        M3; X3; SBAR();                                                                           \
        E(S, 10); E(S, 11); E(S, 12); E(S, 13); E(S, 14); E(S, 15); SBAR();                       \
    } while (0)
        const unsigned obo = WS_O + (2u << 20) + ((2 * otl + ot) * 32 + 8 * kq) * 1024 - w * 2048;
        auto handoff = [&]() {
            LAS char* EXW = L + L_EX + w * 2304 + lane * 16;
            *(LAS f32x4*)EXW = (f32x4){oa[0], oa[1], oa[2], oa[3]};
            *(LAS f32x4*)(EXW + 1024) = (f32x4){oa[4], oa[5], oa[6], oa[7]};
            *(LAS float*)(L + L_EX + w * 2304 + 2048 + lane * 4) = oa[8];
            asm volatile("" ::: "memory");
            *(LAS volatile unsigned*)(L + L_PAIR + w * 4) = 1;
        };
        const int psw = w < 4 ? PRIO_SWITCH : -1;
        if (w < 4) __builtin_amdgcn_s_setprio(2); else __builtin_amdgcn_s_setprio(1);
#define PSW(t, u) if (u == (PRIO_SWITCH & 3)) asm volatile("s_cmp_lg_u32 %0, %1\n\ts_cbranch_scc1 1f\n\ts_setprio 0\n1:" :: "s"(t), "s"(psw) : "scc")
#define PAIR(t, u)                                  \
        PSW(t, u);                                                                                                                                                      \
        PHASE(sa, ob = PV(v1r[(u + 3) & 3], sb, 1, ob), sb = MFMA(kr[u], qb, negm), oa = PV(v0r[u], sa, 0, oa), SBAR(); LD2(u ^ 2, u ^ 3, (t) + 2), SBAR(); WAITR(9, v0r[u]), (void)0); \
        WAITR2(8, v1r[u], kr[u + 1]); SBAR();                                                                                                                           \
        PHASE(sb, oa = PV(v1r[u], sa, 1, oa), sa = MFMA(kr[u + 1], qa, negm), ob = PV(v0r[u], sb, 0, ob), (void)0, (void)0, (void)0);                                   \
        PSW((t) + 1, u + 1);                                                                                                                                            \
        PHASE(sa, ob = PV(v1r[u], sb, 1, ob), sb = MFMA(kr[u + 1], qb, negm), oa = PV(v0r[u + 1], sa, 0, oa), (void)0, SBAR(); WAITR(7, v0r[u + 1]), (void)0);          \
        WAITR(6, v1r[u + 1]); SBAR();                                                                                                                                   \
        PHASE(sb, oa = PV(v1r[u + 1], sa, 1, oa), sa = MFMA(kr[u ^ 2], qa, negm), ob = PV(v0r[u + 1], sb, 0, ob), SBAR(); WAITR(5, kr[u ^ 2]), (void)0, (void)0)
#pragma unroll 1
        for (int t0 = 0; t0 < 28; t0 += 4) {
            PAIR(t0, 0);
            PAIR(t0 + 2, 2);
        }
        PAIR(28, 0);
#define OFB(k) ofb[k] = ld16(rs, vK, obo + (k) * 1024)
        asm volatile("s_waitcnt vmcnt(0)" : "+v"(v0r[2]), "+v"(v1r[2]), "+v"(kr[3]), "+v"(v0r[3]), "+v"(v1r[3]));
        lds_wait(L + L_OBS, 4);
        PHASE(sa, ob = PV(v1r[1], sb, 1, ob), sb = MFMA(kr[2], qb, negm), oa = PV(v0r[2], sa, 0, oa), OFB(0), OFB(1), OFB(2));
        PHASE(sb, oa = PV(v1r[2], sa, 1, oa), sa = MFMA(kr[3], qa, negm), ob = PV(v0r[2], sb, 0, ob), OFB(3), (void)0, (void)0);
        PHASE(sa, ob = PV(v1r[2], sb, 1, ob), sb = MFMA(kr[3], qb, negm), oa = PV(v0r[3], sa, 0, oa), OFB(4), OFB(5), OFB(6));
        PHASE(sb, oa = PV(v1r[3], sa, 1, oa), (void)0, ob = PV(v0r[3], sb, 0, ob), OFB(7), (void)0, handoff());
        ob = PV(v1r[3], sb, 1, ob);
#undef PHASE
#undef E
    }
    __builtin_amdgcn_s_setprio(0);
    int lane_ = __builtin_amdgcn_mbcnt_hi(~0u, __builtin_amdgcn_mbcnt_lo(~0u, 0u));
    asm volatile("" : "+v"(lane_));
    const int r32_ = lane_ & 31, h_ = lane_ >> 5, q4_ = (lane_ & 15) >> 2, trl_ = ((lane_ >> 4) & 1) * 32 + (lane_ & 3) * 8;
    const unsigned oao = WS_O + ((2 * otl + ot) * 32 + 8 * kq) * 1024, vp = lane_ < 8 ? (unsigned)(lane_ * 1024 + 1020) : 0x40000000u;
    auto probe = [&]() { return (unsigned)__ballot(__builtin_amdgcn_raw_buffer_load_b32(rs, vp, oao, 16) != 0xFFFFFFFFu); };
    const unsigned pr0 = __builtin_amdgcn_raw_buffer_load_b32(rs, vp, oao, 16);
    SBAR();
    const int wpo = kq * 16384 + (4 * h_ + q4_) * 128 + trl_;
    bf16x8 wv[8][2];
    auto wreads = [&](int base, int k0, int k1) {
#pragma unroll
        for (int k = k0; k < k1; ++k)
#pragma unroll
            for (int nt = 0; nt < 2; ++nt) wv[k][nt] = trfrag(L + base + wpo + k * 2048 + ((nt ^ (q4_ >> 1)) * 64), 1024);
    };
    const float gate = *(LAS const float*)(L + L_G + (kh * 32 + r32_) * 4);
    lds_wait(L + L_PAIR + (w ^ 1) * 4, 1);
    {
        LAS const char* EXR = L + L_EX + (w ^ 1) * 2304 + lane_ * 16;
        const f32x4 e0 = *(LAS const f32x4*)EXR, e1 = *(LAS const f32x4*)(EXR + 1024);
        const float e2 = *(LAS const float*)(L + L_EX + (w ^ 1) * 2304 + 2048 + lane_ * 4);
        SBAR();
        wreads(L_WOB, 0, 4);
        SBAR();
        const float l = ob[8] + e2;
#pragma unroll
        for (int i = 0; i < 4; ++i) { ob[i] += e0[i]; ob[4 + i] += e1[i]; }
        float rl = __builtin_amdgcn_rcpf(l), t = fmx(l, rl) * 1.0e19f;
#pragma unroll
        for (int i = 0; i < 8; i += 2) t = fmx(fmx(t, __builtin_fabsf(ob[i])), __builtin_fabsf(ob[i + 1]));
        if (__builtin_expect(__any(!(t < 1.0e38f)), 0)) {
            ob = f32x16{};
            float m = -INFINITY;
            for (int t2 = 0; t2 < 64; ++t2) {
                const unsigned so = kvb + (t2 >> 1) * BLK, fl = (t2 & 1) * 2048;
                const bf16x8 ones = {0x3F80, 0x3F80, 0x3F80, 0x3F80, 0x3F80, 0x3F80, 0x3F80, 0x3F80};
                const bf16x8 kf = ld16(rs, vK ^ fl, so), v0l = ld16(rs, vV ^ fl, so), v1l = ld16(rs, vV ^ fl, so + 512);
                const bf16x8 v0 = r32_ < 16 ? v0l : ones, v1 = r32_ < 16 ? v1l : ones;
                f32x16 s = MFMA(kf, qb, f32x16{});
                const float mn = fmx(max16(s), m), al = EXP2(m - mn);
                m = mn;
#pragma unroll
                for (int i = 0; i < 16; ++i) { ob[i] *= al; s[i] = EXP2(s[i] - mn); }
                ob = PV(v0, s, 0, ob);
                ob = PV(v1, s, 1, ob);
            }
            rl = __builtin_amdgcn_rcpf(ob[8]);
        }
        rl *= gate;
#pragma unroll
        for (int i = 0; i < 8; ++i) ob[i] *= rl;
        st16(rs, __builtin_bit_cast(bf16x8, __builtin_elementwise_min(__builtin_bit_cast(u32x4, pack8(ob, 0)), (u32x4){0xFFFFFFFEu, 0xFFFFFFFEu, 0xFFFFFFFEu, 0xFFFFFFFEu})),
             lane_ * 16, WS_O + ((2 * tl + kh) * 32 + head) * 1024);
        SBAR();
    }
    if (lane_ == 0) __hip_atomic_fetch_add((LAS unsigned*)(L + L_STORED), 1u, __ATOMIC_RELAXED, __HIP_MEMORY_SCOPE_WORKGROUP);
    f32x16 acc[2];
    f32x4 keep0 = {}, keep1 = {};
    unsigned dst = (64 * otl + 32 * ot + r32_) * 1024 + 128 * ocg + 32 * kq + 16 * h_;
    int src[3];
#pragma unroll
    for (int z = 0; z < 3; ++z) src[z] = L_WOB + ((kq + 1 + z) & 3) * 16384 + ot * 8192 + kq * 2048 + lane_ * 16;
    {
        bf16x8 of[8];
        auto loads = [&](int g) {
#pragma unroll
            for (int k = 4 * g; k < 4 * g + 4; ++k) of[k] = ld16(rs, lane_ * 16 + (k & 3) * 1024, oao + (k & 4) * 1024);
        };
        const bf16x8 none = {-1, -1, -1, -1, -1, -1, -1, -1};
        unsigned rdy = (unsigned)__ballot(pr0 != 0xFFFFFFFFu);
#pragma unroll
        for (int g = 0; g < 2; ++g) { if (((rdy >> (4 * g)) & 15) == 15) loads(g); else of[4 * g] = none; }
        SBAR();
#pragma unroll
        for (int k = 0; k < 8; ++k) {
#pragma unroll
            for (int nt = 0; nt < 2; ++nt) acc[nt] = MFMA(wv[k][nt], ofb[k], k ? acc[nt] : f32x16{});
            SBAR();
            if (k < 4) wreads(L_WOB, k + 4, k + 5); else wreads(L_WOA, k - 4, k - 3);
            SBAR();
        }
        wreads(L_WOA, 4, 8);
        lds_arrive(L + L_PAIR + w * 4, lane_);
        SBAR();
        auto missing = [&](int g) {
            const u32x4 v = __builtin_elementwise_max(__builtin_elementwise_max(__builtin_bit_cast(u32x4, of[4 * g]), __builtin_bit_cast(u32x4, of[4 * g + 1])),
                                                      __builtin_elementwise_max(__builtin_bit_cast(u32x4, of[4 * g + 2]), __builtin_bit_cast(u32x4, of[4 * g + 3])));
            return (bool)__any(__builtin_elementwise_max(__builtin_elementwise_max(v[0], v[1]), __builtin_elementwise_max(v[2], v[3])) == 0xFFFFFFFFu);
        };
        asm volatile("" : "+v"(dst), "+v"(src[0]), "+v"(src[1]), "+v"(src[2]));
        bool have1 = ((rdy >> 4) & 15) == 15;
#pragma unroll
        for (int g = 0; g < 2; ++g) {
#pragma unroll 1
            for (unsigned spins = 0; spins < SPIN_CAP && missing(g); ++spins) {
                rdy = lds_word(L + L_STORED) >= 8 ? 0xFFu : probe();
                asm volatile("" ::: "memory");
                if (g == 0 && !have1 && ((rdy >> 4) & 15) == 15) { loads(1); have1 = true; }
                if (((rdy >> (4 * g)) & 15) == 15) loads(g); else __builtin_amdgcn_s_sleep(RETRY_SLEEP);
            }
            if (g == 0) {
#pragma unroll
                for (int k = 0; k < 4; ++k) { acc[0] = MFMA(wv[k][0], of[k], acc[0]); acc[1] = MFMA(wv[k][1], of[k], acc[1]); }
                lds_wait(L + L_PAIR + (w ^ 1) * 4, 2);
            }
        }
        LAS char* PR = L + L_WOB + kq * 16384 + ot * 8192 + lane_ * 16;
#pragma unroll
        for (int nt = 0; nt < 2; ++nt)
#pragma unroll
            for (int k = 4; k < 8; ++k) acc[nt] = MFMA(wv[k][nt], of[k], acc[nt]);
        SBAR();
#pragma unroll
        for (int z = 0; z < 4; ++z) {
            const int b = 8 * (z & 1);
            const f32x4 u0 = {acc[z >> 1][b], acc[z >> 1][b + 1], acc[z >> 1][b + 2], acc[z >> 1][b + 3]}, u1 = {acc[z >> 1][b + 4], acc[z >> 1][b + 5], acc[z >> 1][b + 6], acc[z >> 1][b + 7]};
            if (z != kq) { *(LAS f32x4*)(PR + z * 2048) = u0; *(LAS f32x4*)(PR + z * 2048 + 1024) = u1; } else { keep0 = u0; keep1 = u1; }
        }
    }
    BAR();
    {
        f32x4 u[3][2];
#pragma unroll
        for (int z = 0; z < 3; ++z) { u[z][0] = *(LAS const f32x4*)(L + src[z]); u[z][1] = *(LAS const f32x4*)(L + src[z] + 1024); }
        SBAR();
        const f32x4 r0 = ((keep0 + u[0][0]) + u[1][0]) + u[2][0], r1 = ((keep1 + u[0][1]) + u[1][1]) + u[2][1];
        unsigned a0 = pk2(r0[0], r0[1]), a1 = pk2(r0[2], r0[3]), b0 = pk2(r1[0], r1[1]), b1 = pk2(r1[2], r1[3]);
        auto s0_ = __builtin_amdgcn_permlane32_swap(a0, b0, false, false);
        auto s1_ = __builtin_amdgcn_permlane32_swap(a1, b1, false, false);
        *(u32x4*)((char*)out + dst) = (u32x4){s0_[0], s1_[0], s0_[1], s1_[1]};
    }
    if (blockIdx.x == 0 && w == 0 && lane_ == 0) set_flag(ws, WS_EP, tag);
}

extern "C" void kernel_launch(void* const* d_in, const int* in_sizes, int n_in, void* d_out, int out_size, void* d_ws, size_t ws_size, hipStream_t stream) {
    fused_kernel<<<256, 768, 0, stream>>>((const uint16_t*)d_in[0], (const uint16_t*)d_in[1], (const uint16_t*)d_in[4], (const uint16_t*)d_in[5], (const uint16_t*)d_in[7],
                                          (const uint16_t*)d_in[2], (const uint16_t*)d_in[6], (uint16_t*)d_out, (char*)d_ws);
}
```

```cpp
#include <hip/hip_runtime.h>
#include <hip/hip_bf16.h>
#include <stdint.h>

typedef short bf16x8 __attribute__((ext_vector_type(8)));
typedef short s16x4 __attribute__((ext_vector_type(4)));
typedef float f32x16 __attribute__((ext_vector_type(16)));
typedef float f32x4 __attribute__((ext_vector_type(4)));
typedef float f32x2 __attribute__((ext_vector_type(2)));
typedef unsigned u32x4 __attribute__((ext_vector_type(4)));
typedef unsigned u32x2 __attribute__((ext_vector_type(2)));
typedef __bf16 bf16x2_t __attribute__((ext_vector_type(2)));
typedef unsigned long long u64;
#define LAS __attribute__((address_space(3)))
#define GAS __attribute__((address_space(1)))
#define DI __device__ __forceinline__
#define MFMA(a, b, c) __builtin_amdgcn_mfma_f32_32x32x16_bf16((a), (b), (c), 0, 0, 0)
typedef __bf16 bfx8 __attribute__((ext_vector_type(8)));
typedef __bf16 bfx16 __attribute__((ext_vector_type(16)));
#define PV(a, S, half, c) __builtin_amdgcn_smfmac_f32_32x32x32_bf16(__builtin_bit_cast(bfx8, a), \
    __builtin_bit_cast(bfx16, __builtin_shufflevector(S, S, 8 * half, 8 * half + 1, 8 * half + 2, 8 * half + 3, 8 * half + 4, 8 * half + 5, 8 * half + 6, 8 * half + 7)), (c), 0xDDDD, 0, 0)
#define SBAR() __builtin_amdgcn_sched_barrier(0)
#define RLX_AGENT __ATOMIC_RELAXED, __HIP_MEMORY_SCOPE_AGENT
#define EXP2(x) __builtin_amdgcn_exp2f(x)

constexpr float LOG2E = 1.4426950408889634f;
constexpr unsigned BLK = 16384;
constexpr unsigned WS_KV = 0;
constexpr unsigned WS_O = WS_KV + 256 * BLK;
constexpr unsigned WS_PK = WS_O + (4u << 20);
constexpr unsigned WS_KVF = WS_PK + 32 * 32 * 2176;
constexpr unsigned WS_F1 = WS_KVF + 32 * 2176;
constexpr unsigned WS_F2 = WS_F1 + 2048;
constexpr unsigned WS_F3 = WS_F2 + 8192;
constexpr unsigned WS_F5 = WS_F3 + 8192;
constexpr unsigned WS_EP = WS_F5 + 8192;
constexpr unsigned WS_END = WS_EP + 128;
constexpr int L_X = 0, L_RING = 65536;
constexpr int L_PHK = 65536, L_PHQ = 81920, L_VB = 16384;
constexpr int L_WOA = 0, L_WOB = 65536;
constexpr int L_WPHI = L_RING + 3 * 16384;
constexpr int L_G = 131072, L_SYNC = L_G + 256, L_STORED = L_SYNC + 4, L_HCNT = L_SYNC + 8, L_OBS = L_SYNC + 12, L_PAIR = L_SYNC + 16, L_ZEROS = L_SYNC + 96, L_ONES = L_SYNC + 128, L_DUMMY = L_SYNC + 256, L_G2 = L_SYNC + 512;
constexpr int L_QA = L_G + 1024, L_EX = L_QA + 8192;
constexpr int LDS_BYTES = 163840;
constexpr int F1_STEP = 14, PRIO_SWITCH = 23, PK_DELAY = 24, WO_T0 = 6400, WO_DT = 270, POLL_GAP = 2, POLL_FIRST = 6, RETRY_SLEEP = 2, PUB_DELAY = 4;
constexpr int CODE_LINES = 257;
constexpr unsigned SPIN_CAP = 1u << 20;

template <int N> struct IC { static constexpr int value = N; };
template <int I, int N, class F> DI void static_for(F&& f) { if constexpr (I < N) { f(IC<I>{}); static_for<I + 1, N>(f); } }
DI unsigned pk2(float a, float b) { f32x2 v = {a, b}; return __builtin_bit_cast(unsigned, __builtin_convertvector(v, bf16x2_t)); }
DI bf16x8 pack8(const f32x16& x, int s) {
    u32x4 p = {pk2(x[8 * s], x[8 * s + 1]), pk2(x[8 * s + 2], x[8 * s + 3]), pk2(x[8 * s + 4], x[8 * s + 5]), pk2(x[8 * s + 6], x[8 * s + 7])};
    return __builtin_bit_cast(bf16x8, p);
}
DI float fmx(float a, float b) { return __builtin_elementwise_maximum(a, b); }
DI float max32(float v) { auto r = __builtin_amdgcn_permlane32_swap(__float_as_uint(v), __float_as_uint(v), false, false); return fmx(__uint_as_float(r[0]), __uint_as_float(r[1])); }
DI float lmax16(const f32x16& s) {
    const float a = fmx(fmx(s[0], s[1]), s[2]), b = fmx(fmx(s[3], s[4]), s[5]), c = fmx(fmx(s[6], s[7]), s[8]), d = fmx(fmx(s[9], s[10]), s[11]), e = fmx(fmx(s[12], s[13]), s[14]);
    return fmx(fmx(fmx(a, b), c), fmx(fmx(d, e), s[15]));
}
DI float max16(const f32x16& s) { return max32(lmax16(s)); }
DI void glds2(unsigned v0, unsigned v1, const void* sbase, unsigned ldsdst) {
    unsigned keep;
    asm volatile("s_mov_b32 %0, m0\n\ts_mov_b32 m0, %4\n\ts_nop 0\n\tglobal_load_lds_dwordx4 %1, %3\n\tglobal_load_lds_dwordx4 %2, %3 offset:1024\n\ts_mov_b32 m0, %0"
                 : "=&s"(keep) : "v"(v0), "v"(v1), "s"(sbase), "s"(ldsdst) : "memory");
}
DI void glds4(const unsigned (&v)[4], const void* sbase, unsigned ldsdst) {
    unsigned keep;
    asm volatile("s_mov_b32 %0, m0\n\ts_mov_b32 m0, %6\n\ts_nop 0\n\tglobal_load_lds_dwordx4 %1, %5\n\tglobal_load_lds_dwordx4 %2, %5 offset:1024\n\t"
                 "global_load_lds_dwordx4 %3, %5 offset:2048\n\tglobal_load_lds_dwordx4 %4, %5 offset:3072\n\ts_mov_b32 m0, %0"
                 : "=&s"(keep) : "v"(v[0]), "v"(v[1]), "v"(v[2]), "v"(v[3]), "s"(sbase), "s"(ldsdst) : "memory");
}
DI void touch(unsigned voff, const void* sbase, unsigned ldsdst) {
    unsigned keep;
    asm volatile("s_mov_b32 %0, m0\n\ts_mov_b32 m0, %3\n\ts_nop 0\n\tglobal_load_lds_dword %1, %2 sc1\n\ts_mov_b32 m0, %0"
                 : "=&s"(keep) : "v"(voff), "s"(sbase), "s"(ldsdst) : "memory");
}
typedef short v4i16_t __attribute__((ext_vector_type(4)));
DI s16x4 trread(LAS const char* p) { return __builtin_bit_cast(s16x4, __builtin_amdgcn_ds_read_tr16_b64_v4i16((LAS v4i16_t*)p)); }
DI bf16x8 trfrag(LAS const char* p, int d2) { s16x4 lo = trread(p), hi = trread(p + d2); return __builtin_shufflevector(lo, hi, 0, 1, 2, 3, 4, 5, 6, 7); }
DI bf16x8 ldsfrag(LAS const char* p) { return *(LAS const bf16x8*)p; }
#define BAR() asm volatile("s_waitcnt lgkmcnt(0)\n\ts_barrier" ::: "memory")
#define VM_BAR(N) asm volatile("s_waitcnt vmcnt(" #N ") lgkmcnt(0)\n\ts_barrier" ::: "memory")
#define DRAIN() asm volatile("s_waitcnt vmcnt(0)" ::: "memory")
typedef __amdgpu_buffer_rsrc_t rsrc_t;
DI bf16x8 ld16(rsrc_t r, unsigned voff, unsigned soff) { return __builtin_bit_cast(bf16x8, __builtin_amdgcn_raw_buffer_load_b128(r, voff, soff, 16)); }
DI void st16(rsrc_t r, bf16x8 v, unsigned voff, unsigned soff) { __builtin_amdgcn_raw_buffer_store_b128(__builtin_bit_cast(u32x4, v), r, voff, soff, 16); }
DI unsigned lds_word(LAS char* p) { return __builtin_amdgcn_readfirstlane(*(LAS volatile unsigned*)p); }
DI void lds_wait(LAS char* p, unsigned want) {
#pragma unroll 1
    for (unsigned spins = 0; spins < (SPIN_CAP << 6) && lds_word(p) < want; ++spins) __builtin_amdgcn_s_sleep(1);
    asm volatile("" ::: "memory");
}
DI void lds_arrive(LAS char* p, int lane) {
    asm volatile("s_waitcnt lgkmcnt(0)" ::: "memory");
    if (lane == 0) __hip_atomic_fetch_add((LAS unsigned*)p, 1u, __ATOMIC_RELAXED, __HIP_MEMORY_SCOPE_WORKGROUP);
}
DI void set_flag(char* ws, unsigned off, u64 tag) { __hip_atomic_store((GAS u64*)(ws + off), tag, RLX_AGENT); }
template <int FIRST, int GAP> DI void wait_flags(char* ws, unsigned off, int n, u64 tag, int lane) {
    GAS u64* p = (GAS u64*)(ws + off) + (lane < n ? lane : 0);
    if (FIRST) __builtin_amdgcn_s_sleep(FIRST);
#pragma unroll 1
    for (unsigned spins = 0; spins < SPIN_CAP; ++spins) {
        const u64 v = __hip_atomic_load(p, RLX_AGENT);
        if (__all(v == tag)) break;
        __builtin_amdgcn_s_sleep(GAP);
    }
    __builtin_amdgcn_fence(__ATOMIC_ACQUIRE, "wavefront");
    asm volatile("" ::: "memory");
}

DI void softmax16(const f32x16& p, bf16x8 (&out)[2]) {
#define KEEP1(x) asm("" : "+v"(x))
    const float nm = -lmax16(p) * LOG2E;
    float e[16], t[8];
#pragma unroll
    for (int i = 0; i < 16; ++i) { e[i] = __builtin_fmaf(p[i], LOG2E, nm); KEEP1(e[i]); e[i] = EXP2(e[i]); }
#pragma unroll
    for (int i = 0; i < 8; ++i) { t[i] = e[2 * i] + e[2 * i + 1]; KEEP1(t[i]); }
#pragma unroll
    for (int i = 0; i < 4; ++i) { t[i] = t[2 * i] + t[2 * i + 1]; KEEP1(t[i]); }
    t[0] += t[1]; KEEP1(t[0]); t[2] += t[3]; KEEP1(t[2]);
    const float rs = __builtin_amdgcn_rcpf(t[0] + t[2]);
    u32x4 u[2];
#pragma unroll
    for (int i = 0; i < 16; ++i) { e[i] *= rs; KEEP1(e[i]); }
#pragma unroll
    for (int i = 0; i < 8; ++i) u[i >> 2][i & 3] = pk2(e[2 * i], e[2 * i + 1]);
    out[0] = __builtin_bit_cast(bf16x8, u[0]); out[1] = __builtin_bit_cast(bf16x8, u[1]);
}

__global__ __launch_bounds__(768, 3) void fused_kernel(const uint16_t* __restrict__ x, const uint16_t* __restrict__ WA, const uint16_t* __restrict__ WB,
                                                    const uint16_t* __restrict__ wphi, const uint16_t* __restrict__ gatew,
                                                    const uint16_t* __restrict__ woA, const uint16_t* __restrict__ woB,
                                                    uint16_t* __restrict__ out, char* ws) {
    __shared__ __attribute__((aligned(16))) char lds[LDS_BYTES];
    LAS char* L = (LAS char*)lds;
    u64 pc0; asm volatile("s_getpc_b64 %0" : "=s"(pc0));
    const unsigned lds0 = (unsigned)(uintptr_t)lds;
    const int tid = threadIdx.x, lane = tid & 63, w = __builtin_amdgcn_readfirstlane(tid >> 6);
    const int r32 = lane & 31, h = lane >> 5;
    const int xcd = blockIdx.x & 7, idx = blockIdx.x >> 3;
    const int qd = 2 * (xcd & 3) + (idx & 1), tl = 2 * (idx >> 1) + (xcd >> 2);
    const int s0 = ((tl >> 1) + 4 * (qd & 1)) & 7;
    const int otl = 4 * xcd + (idx >> 3), ocg = idx & 7;
    const rsrc_t rs = __builtin_amdgcn_make_buffer_rsrc(ws, 0, WS_END, 0x00020000);
    u64 tag = 0; u32x2 ep;
#define LOAD_EPOCH() asm volatile("global_load_dwordx2 %0, %1, %2 sc1" : "=v"(ep) : "v"(0), "s"(ws + WS_EP) : "memory")
#define MAKE_TAG() do { asm volatile("" : "+v"(ep)); tag = ((u64)__builtin_amdgcn_readfirstlane(ep[1]) << 32 | (unsigned)__builtin_amdgcn_readfirstlane(ep[0])) + 1; } while (0)
    const int q4 = (lane & 15) >> 2;
    const int trl = ((lane >> 4) & 1) * 32 + (lane & 3) * 8;
    asm volatile("" :: "s"(x), "s"(WA), "s"(WB), "s"(wphi), "s"(gatew), "s"(ws));
    if (__builtin_expect(w >= 8, 1)) {
        const int j = w - 8;
        const unsigned vsw = (unsigned)(((lane & 7) ^ (((lane >> 4) & 1) << 2)) << 4);
        unsigned vw[4];
#pragma unroll
        for (int u = 0; u < 4; ++u) vw[u] = (unsigned)((lane >> 3) * 3072) + vsw + u * (8 * 3072 - 1024);
        const unsigned vx0 = (unsigned)((lane >> 3) * 1024 + (((lane & 7) ^ (lane >> 4)) << 4)), vx1 = (vx0 ^ 64u) + 8192 - 1024;
        const char* xb = (const char*)x + (size_t)(64 * tl + 16 * j) * 1024;
        const size_t wo_ = (size_t)((j & 1) * 32) * 3072 + qd * 128;
        const char* wb[3] = {(const char*)WA + wo_ + 1024 + (j >> 1) * 1024, (const char*)WB + wo_ + (j >> 1) * 1024, (j >> 1) ? (const char*)WB + wo_ + 2048 : (const char*)WA + wo_};
        auto issue_x = [&](int i) { const int kc = (s0 + i) & 7; glds2(vx0, vx1, xb + kc * 128, lds0 + L_X + i * 8192 + j * 2048); };
        auto issue_w = [&](int i) { glds4(vw, wb[i >> 3] + ((s0 + i) & 7) * (64 * 3072), lds0 + L_RING + ((i + 4) % 6) * 16384 + j * 4096); };
        __builtin_amdgcn_s_setprio(3);
        issue_w(0); issue_x(0); issue_x(1); issue_x(2); issue_x(3);
        bf16x8 gw; unsigned warm;
        asm volatile("global_load_dwordx4 %0, %1, %2" : "=v"(gw) : "v"((lane & 15) * 64 + (lane >> 4) * 16), "s"(gatew) : "memory");
        glds2(lane * 16, lane * 16, wphi + qd * 1024, lds0 + L_WPHI);
        issue_w(1);
        int laneh = lane; asm volatile("" : "+v"(laneh));
        const int r32h = laneh & 31, hh = laneh >> 5, q4h = (laneh & 15) >> 2, trlh = ((laneh >> 4) & 1) * 32 + (laneh & 3) * 8;
        const unsigned vswh = (unsigned)(((laneh & 7) ^ (((laneh >> 4) & 1) << 2)) << 4);
        const int head = 4 * qd + j;
        const bool keep = (r32h >> 4) == (j & 1);
        constexpr unsigned OOB = 0x40000000u;
        unsigned a_ve[4], a_pf[2], a_kf[4], vo[4];
#pragma unroll
        for (int u = 0; u < 4; ++u) {
            a_ve[u] = keep ? L_VB + (j >> 1) * 4096 + u * 1024 + laneh * 16 : L_ONES;
            const unsigned n = u * 136 + ((r32h < 16 ? r32h : 16) * 2 + hh) * 4;
            a_kf[u] = (n >> 5) * 128 + (n & 31) * 2;
            vo[u] = (unsigned)((laneh >> 3) * 1024) + vswh + u * (8192 - 1024);
            asm volatile("" : "+v"(a_ve[u]), "+v"(a_kf[u]), "+v"(vo[u]));
        }
        a_pf[0] = L_PHK + j * 4096 + (4 * hh + q4h) * 64 + (trlh ^ ((2 * hh + (q4h >> 1)) * 8)); a_pf[1] = (a_pf[0] ^ 32) + 512;
        unsigned a_l16 = laneh * 16, a_q = L_PHQ + j * 4096 + laneh * 16, a_g2 = L_G2 + r32h * 4;
        unsigned v_pk = (keep || (r32h & 15) == 0) ? (unsigned)(((keep ? (r32h & 15) : 16) * 2 + hh) * 16) : OOB;
        unsigned v_one = laneh == 0 ? 0u : OOB, v_p32 = r32h * 8, v_p17 = (laneh < 17 ? laneh : 0) * 8, v_p8 = (laneh & 7) * 8;
        unsigned v_h1 = hh * 8704 + r32h * 4, v_h1s = (laneh < 32 && !(laneh & 1)) ? laneh * 2 : OOB, v_tch = ((laneh >> 3) & 1) * 32768 + (laneh & 7) * 128;
        bf16x8 none = {-1, -1, -1, -1, -1, -1, -1, -1};
        asm volatile("" : "+v"(a_pf[0]), "+v"(a_pf[1]), "+v"(a_l16), "+v"(a_q), "+v"(a_g2), "+v"(v_pk), "+v"(v_one), "+v"(v_p32), "+v"(v_p17), "+v"(v_p8));
        asm volatile("" : "+v"(v_h1), "+v"(v_h1s), "+v"(v_tch), "+v"(none));
        VM_BAR(13); issue_w(2); issue_w(3);
        VM_BAR(8); issue_x(4); issue_w(4);
        VM_BAR(10); issue_x(5); issue_w(5);
        VM_BAR(12); issue_x(6); issue_w(6);
        VM_BAR(12); issue_x(7); issue_w(7);
        VM_BAR(12);
        asm volatile("global_load_dword %0, %1, %2" : "=v"(warm) : "v"(((tl >> 1) * 32 + (lane & 15)) * 3072), "s"(wb[2] - (j & 1) * 16 * 3072) : "memory");
        LOAD_EPOCH();
        issue_w(8);
        VM_BAR(12);
        VM_BAR(6); issue_w(9); issue_w(10); issue_w(11);
        u32x2 tagv;
        f32x4 gl = {};
        const int grow = 16 * j + (lane & 15);
        int gxa = L_X + grow * 128 + (((lane >> 4) ^ ((grow >> 1) & 7)) << 4);
        u64 f1early = 0;
        unsigned a_gt = ((lane & 16) ? L_G2 : L_G) + grow * 4;
        asm volatile("" : "+v"(a_gt));
#pragma unroll
        for (int i = 8; i < 24; i += 2) {
            if (i < 22) VM_BAR(8); else VM_BAR(0);
            if (i == 8) asm volatile("" : "+v"(gw), "+v"(warm));
            bf16x8 xg[4];
            if (i < 16) {
#pragma unroll
                for (int u = 0; u < 4; ++u) xg[u] = ldsfrag(L + (gxa ^ ((u & 1) << 6)) + (i - 8 + (u >> 1)) * 8192);
            }
            SBAR();
            if (i < 20) { issue_w(i + 4); issue_w(i + 5); }
            SBAR();
            if (i < 16) {
                asm volatile("" : "+v"(gw));
#pragma unroll
                for (int u = 0; u < 4; ++u)
                    gl = __builtin_amdgcn_mfma_f32_16x16x32_bf16((lane & 15) == 2 * ((s0 + i + (u >> 1)) & 7) + (u & 1) ? gw : bf16x8{}, xg[u], gl, 0, 0, 0);
                asm volatile("" : "+v"(gl));
            }
            if (j == 0 && i == 22) {
                if (!__all(f1early == tag)) wait_flags<0, 2>(ws, WS_F1 + qd * 256, 32, tag, lane);
                touch(a_l16 * 8, ws + WS_KV + (qd * 32 + ((tl + 1) & 31)) * BLK, lds0 + L_DUMMY);
                touch(a_l16 * 8, ws + WS_KV + (qd * 32 + ((tl + 2) & 31)) * BLK, lds0 + L_DUMMY);
            }
            if (i == 22) {
                float z = (gl[0] + gl[1]) + (gl[2] + gl[3]);
                z += __shfl_xor(z, 16); z += __shfl_xor(z, 32);
                const float g = __builtin_amdgcn_rcpf(1.f + EXP2(-z * LOG2E));
                *(LAS float*)(L + a_gt) = (lane & 16) ? 1.f - g : g;
            }
            if (i == 16) { MAKE_TAG(); tagv = (u32x2){(unsigned)tag, (unsigned)(tag >> 32)}; asm volatile("" : "+v"(tagv)); }
            if (j == 0 && i == 20) {
                f1early = __hip_atomic_load((GAS u64*)(ws + WS_F1 + qd * 256) + (lane & 31), RLX_AGENT);
                if (lane < 36) *(LAS unsigned*)(L + L_SYNC + lane * 4) = lane >= 32 ? 0x3F803F80u : 0u;
            }
            SBAR();
        }
        auto publish_flag = [&](unsigned off) { DRAIN(); __builtin_amdgcn_raw_buffer_store_b64(tagv, rs, v_one, off, 16); };
        BAR();
        const u64 tp2 = __builtin_amdgcn_s_memtime();
        __builtin_amdgcn_s_sleep(6);
        bf16x8 phq[2][2]; float g2[2];
        {
            bf16x8 pf[4], ve[4];
#pragma unroll
            for (int u = 0; u < 4; ++u) {
                pf[u] = __builtin_shufflevector(trread(L + a_pf[0] + u * 1024), trread(L + a_pf[1] + u * 1024), 0, 1, 2, 3, 4, 5, 6, 7);
                ve[u] = ldsfrag(L + a_ve[u]);
                phq[u >> 1][u & 1] = ldsfrag(L + a_q + u * 1024);
            }
            g2[0] = *(LAS const float*)(L + a_g2); g2[1] = *(LAS const float*)(L + a_g2 + 128);
            lds_arrive(L + L_HCNT, lane);
            __builtin_amdgcn_s_sleep(PK_DELAY);
            touch(v_p8 * 16, (const char*)(j & 2 ? woA : woB) + blockIdx.x * 2048 + (j & 1) * 1024, lds0 + L_DUMMY);
            f32x16 kv = MFMA(pf[0], ve[0], f32x16{});
#pragma unroll
            for (int u = 1; u < 4; ++u) kv = MFMA(pf[u], ve[u], kv);
#pragma unroll
            for (int g = 0; g < 4; ++g) st16(rs, __builtin_bit_cast(bf16x8, (f32x4){kv[4 * g], kv[4 * g + 1], kv[4 * g + 2], kv[4 * g + 3]}), v_pk, WS_PK + ((qd * 32 + tl) * 4 + j) * 2176 + g * 544);
            publish_flag(WS_F2 + (head * 32 + tl) * 8);
        }
        lds_wait(L + L_HCNT, 4);
        int wo_done = 0;
        auto wo_service = [&]() {
            if (wo_done < 8 && __builtin_amdgcn_s_memtime() - tp2 >= (u64)(WO_T0 + WO_DT * (j + 4 * wo_done))) {
                const int mtx = wo_done >> 2, pc = wo_done & 3;
                glds4(vo, (const char*)(mtx ? woA : woB) + (size_t)(128 * j) * 1024 + ocg * 128 + pc * 32768, lds0 + (mtx ? L_WOA : L_WOB) + j * 16384 + pc * 4096);
                ++wo_done;
            }
        };
        auto wait_slow = [&](unsigned off, unsigned v, int first) {
#pragma unroll 1
            for (unsigned spins = 0; spins < SPIN_CAP; ++spins) {
#pragma unroll 1
                for (int z = 0; z < (spins ? POLL_GAP : first); ++z) { wo_service(); __builtin_amdgcn_s_sleep(6); }
                asm volatile("" ::: "memory");
                if (__all(__builtin_bit_cast(u64, __builtin_amdgcn_raw_buffer_load_b64(rs, v, off, 16)) == tag)) break;
            }
            __builtin_amdgcn_fence(__ATOMIC_ACQUIRE, "wavefront");
            asm volatile("" ::: "memory");
        };
        if (tl < 17) {
            wait_slow(WS_F2 + head * 256, v_p32, POLL_FIRST);
            float t[16];
#pragma unroll
            for (int i = 0; i < 16; ++i) t[i] = __uint_as_float(__builtin_amdgcn_raw_buffer_load_b32(rs, v_h1, WS_PK + (qd * 128 + j) * 2176 + tl * 128 + i * 17408, 16));
#pragma unroll
            for (int n = 8; n >= 1; n >>= 1)
#pragma unroll
                for (int i = 0; i < n; ++i) { t[i] += t[i + n]; KEEP1(t[i]); }
            auto r = __builtin_amdgcn_permlane32_swap(__float_as_uint(t[0]), __float_as_uint(t[0]), false, false);
            const float x0 = __uint_as_float(r[0]) + __uint_as_float(r[1]);
            const float x1 = __uint_as_float(__builtin_amdgcn_update_dpp(0, __float_as_uint(x0), 0xF5, 0xF, 0xF, true));
            __builtin_amdgcn_raw_buffer_store_b32(pk2(x0, x1), rs, v_h1s, WS_KVF + head * 2176 + tl * 128, 16);
            publish_flag(WS_F3 + (head * 32 + tl) * 8);
        }
        wait_slow(WS_F3 + head * 256, v_p17, tl < 17 ? POLL_FIRST : 2 * POLL_FIRST + 4);
        {
            u32x2 kq_[4];
#pragma unroll
            for (int g = 0; g < 4; ++g) kq_[g] = __builtin_amdgcn_raw_buffer_load_b64(rs, a_kf[g], WS_KVF + head * 2176, 16);
            const bf16x8 k0 = __builtin_bit_cast(bf16x8, __builtin_shufflevector(kq_[0], kq_[1], 0, 1, 2, 3)), k1 = __builtin_bit_cast(bf16x8, __builtin_shufflevector(kq_[2], kq_[3], 0, 1, 2, 3));
#pragma unroll
            for (int tt = 0; tt < 2; ++tt) {
                f32x16 ob = MFMA(k0, phq[tt][0], f32x16{});
                ob = MFMA(k1, phq[tt][1], ob);
                const float rd = g2[tt] * __builtin_amdgcn_rcpf(ob[8] + 1e-6f);
#pragma unroll
                for (int i = 0; i < 8; ++i) { float z = ob[i] * rd; KEEP1(z); ob[i] = z; }
                st16(rs, pack8(ob, 0), a_l16, WS_O + (2u << 20) + ((2 * tl + tt) * 32 + head) * 1024);
                st16(rs, none, a_l16, WS_O + ((2 * tl + tt) * 32 + head) * 1024);
            }
            publish_flag(WS_F5 + (tl * 32 + head) * 8);
        }
        wait_slow(WS_F5 + (otl * 32 + 8 * j) * 8, v_p8, POLL_FIRST);
        touch(v_tch, ws + WS_O + (2u << 20) + (2 * otl * 32 + 8 * j + ocg) * 1024, lds0 + L_DUMMY);
        while (wo_done < 8) { wo_service(); __builtin_amdgcn_s_sleep(6); }
        DRAIN();
        lds_arrive(L + L_OBS, lane);
        return;
    }

    if (w == 0) {
        const char* a = (const char*)(pc0 & ~127ull) + (idx * 9 + lane < CODE_LINES ? idx * 9 + lane : 0) * 128;
        if (lane == 9) {
            a = ws + (size_t)(idx < 21 ? idx : 0) * 524288;
            if (idx >= 22) a = idx < 25 ? (const char*)WB + (idx - 22) * 786368 : idx < 28 ? (const char*)out + (idx - 25) * 1048512 : idx < 30 ? (const char*)woA + (idx - 28) * 524224 : (const char*)woB + (idx - 30) * 524224;
        }
        unsigned keep;
        if (lane < 10) asm volatile("s_mov_b32 %0, m0\n\ts_mov_b32 m0, %2\n\ts_nop 0\n\tglobal_load_lds_dword %1, off sc1\n\ts_mov_b32 m0, %0" : "=&s"(keep) : "v"(a), "s"(lds0 + L_WPHI + 4096) : "memory");
    }
    const int p = w & 1;
    const int hq = w >> 1, kh = w & 1, head = 4 * qd + hq;
    const unsigned vK = w * 2048 + lane * 16;
    const unsigned vV = (unsigned)(w * 2048 + 1024 + h * 256 + (r32 & 15) * 16);
    const unsigned kvb = WS_KV + qd * 32 * BLK;
#define KVOFF(t) (kvb + (((t) + tl) & 31) * BLK)
    bf16x8 kr[4], v0r[4], v1r[4];
    const int kpos = ((lane & 0x33) | ((lane & 4) << 1) | ((lane & 8) >> 1)) * 16;
    bf16x8 kref; unsigned ownw = 0;
    const int cst = (r32 & 11) ? L_ZEROS : L_ONES;
    {
        const int xa0 = L_X + r32 * 128 + ((h ^ ((r32 >> 1) & 7)) << 4);
        const int wa0 = L_RING + ((w >> 1) & 1) * 8192 + (8 * h + q4) * 128 + ((p ^ (q4 >> 1)) * 64) + trl;
        bf16x8 xf[2][4][2], w0[2][4];
        f32x16 acc[2];
        int wsb[3], xsb[4];
#pragma unroll
        for (int i = 0; i < 3; ++i) { wsb[i] = wa0 + i * 32768; asm volatile("" : "+v"(wsb[i])); }
#pragma unroll
        for (int s = 0; s < 4; ++s) { xsb[s] = xa0 ^ (s << 5); asm volatile("" : "+v"(xsb[s])); }
        auto gemm = [&](int TEAM, int TYPE) __attribute__((always_inline)) {
            auto mine = [&](int i) __attribute__((always_inline)) { return i >= 0 && i < 24 && ((i >> 3) & 1) == TEAM; };
            auto rx = [&](int i, int s, int t) __attribute__((always_inline)) { if (mine(i)) xf[i & 1][s][t] = ldsfrag(L + xsb[s] + (i & 7) * 8192 + t * 4096); };
            auto rw = [&](int i, int s) __attribute__((always_inline)) { if (mine(i)) w0[i & 1][s] = trfrag(L + wsb[((i + 4) % 6) >> 1] + (i & 1) * 16384 + s * 2048, 512); };
            auto mm = [&](int i, int s, int t, bool now = false) __attribute__((always_inline)) {
                SBAR();
                if (mine(i) && (i != 7 || now)) {
                    const f32x16 c = ((i & 7) == 0 && s == 0) ? f32x16{} : acc[t];
                    acc[t] = (TEAM == 0 && TYPE) ? MFMA(xf[i & 1][s][t], w0[i & 1][s], c) : MFMA(w0[i & 1][s], xf[i & 1][s][t], c);
                }
                SBAR();
            };
            bf16x8 own[2][2], wf[2], ph[4][2];
            f32x16 f[4];
            static_for<0, 25>([&](auto ic) __attribute__((always_inline)) {
                constexpr int i = ic.value;
                if (i == F1_STEP) { VM_BAR(0); if (TEAM == 0 && TYPE == 0) { MAKE_TAG(); if (tid == 0) set_flag(ws, WS_F1 + (qd * 32 + tl) * 8, tag); } }
                else if (i == 2) { __builtin_amdgcn_s_waitcnt(0x0F70); BAR(); }
                else if (i < 8 || (i < 24 && !(i & 1))) BAR();
                mm(i - 1, 0, 0); rw(i, 0); rx(i, 0, 0);
                mm(i - 1, 0, 1); rx(i, 0, 1); rw(i, 1);
                mm(i - 1, 1, 0); rx(i, 1, 0); rx(i, 1, 1);
                mm(i - 1, 1, 1); rw(i, 2); rx(i, 2, 0);
                mm(i - 1, 2, 0); rx(i, 2, 1); rw(i, 3);
                mm(i - 1, 2, 1); rx(i, 3, 0); rx(i, 3, 1);
                mm(i - 1, 3, 0);
                mm(i - 1, 3, 1);
                if (TEAM == 0 && TYPE == 0 && i == 7) LOAD_EPOCH();
                if (TEAM == 0 && i == 7) {
#pragma unroll
                    for (int z = 0; z < 8; ++z) mm(7, z >> 1, z & 1, true);
#pragma unroll
                    for (int t = 0; t < 2; ++t) {
                        if (TYPE == 0) {
#pragma unroll
                            for (int e = 0; e < 16; ++e) acc[t][e] *= 0.25f * LOG2E;
                        }
                        own[t][0] = pack8(acc[t], 0); own[t][1] = pack8(acc[t], 1);
                        asm volatile("" : "+v"(own[t][0]), "+v"(own[t][1]));
                    }
                }
                if (TEAM == 0 && i == 8) {
                    __builtin_amdgcn_s_sleep(PUB_DELAY);
#pragma unroll
                    for (int t = 0; t < 2; ++t) {
                        const unsigned so = kvb + tl * BLK + (4 * p + t) * 2048;
#pragma unroll
                        for (int a = 0; a < 2; ++a) {
                            if (TYPE == 0) st16(rs, own[t][a], kpos, so + a * 4096);
                            else st16(rs, own[t][a], (unsigned)((r32 >> 4) * 4096 + 1024 + h * 256 + (r32 & 15) * 16) + a * 512, so);
                        }
                    }
                }
                if (TEAM == 1) {
                    if (i == 1) {
                        const short sg = (lane & 4) ? (short)0x8000 : (short)0;
#pragma unroll
                        for (int a = 0; a < 2; ++a) {
                            wf[a] = trfrag(L + L_WPHI + (2 * p + a) * 512 + (4 * h + q4) * 32 + (((lane >> 4) & 1) * 2 + ((lane >> 1) & 1)) * 8, 256);
#pragma unroll
                            for (int e = 0; e < 8; ++e) wf[a][e] ^= sg;
                            asm volatile("" : "+v"(wf[a]));
                        }
                    }
                    if (i == 16) {
                        __builtin_amdgcn_s_setprio(1);
#pragma unroll
                        for (int m = 0; m < 4; ++m) f[m] = MFMA(wf[m >> 1], pack8(acc[m & 1], m >> 1), f32x16{});
                    }
                    if (i == 19) asm volatile("global_load_dword %0, %1, %2 sc1" : "=v"(ownw) : "v"(r32 * 128), "s"(ws + kvb + tl * BLK + (w - 4) * 4096) : "memory");
                    if (i >= 16 && i < 24 && !(i & 1)) { softmax16(f[(i - 16) / 2], ph[(i - 16) / 2]); asm volatile("" : "+v"(ph[(i - 16) / 2][0]), "+v"(ph[(i - 16) / 2][1])); }
                }
                SBAR();
            });
            if (TEAM == 0) {
#pragma unroll
                for (int t = 0; t < 2; ++t)
#pragma unroll
                    for (int a = 0; a < 2; ++a)
                        *(LAS bf16x8*)(L + (TYPE ? L_VB + ((p * 2 + t) * 2 + a) * 1024 : L_QA + ((2 * p + a) * 2 + t) * 1024) + lane * 16) = pack8(acc[t], a);
            } else if (w < 6) {
#pragma unroll
                for (int m = 0; m < 8; ++m) *(LAS bf16x8*)(L + L_PHQ + (8 * p + m) * 1024 + lane * 16) = ph[m >> 1][m & 1];
            } else {
#pragma unroll
                for (int m = 0; m < 8; ++m) {
                    const u32x4 u = __builtin_bit_cast(u32x4, ph[m >> 1][m & 1]);
#pragma unroll
                    for (int g = 0; g < 2; ++g)
                        *(LAS u32x2*)(L + L_PHK + (4 * p + (m >> 1)) * 2048 + r32 * 64 + 8 * ((2 * (2 * (m & 1) + g) + h) ^ ((r32 >> 1) & 7))) = (u32x2){u[2 * g], u[2 * g + 1]};
                }
            }
        };
        if (w < 2) gemm(0, 0); else if (w < 4) gemm(0, 1); else gemm(1, 0);
    }
    const u32x4 rs4 = {(unsigned)(uintptr_t)ws, (unsigned)((uintptr_t)ws >> 32) & 0xFFFFu, WS_END, 0x00020000u};
#define LDK(R, t) asm volatile("buffer_load_dwordx4 %0, %1, %2, %3 offen sc1" : "=v"(R) : "v"(vK), "s"(rs4), "s"(KVOFF(t)))
#define LDV(R, t, S) asm volatile("s_mov_b64 exec, %4\n\tbuffer_load_dwordx4 %0, %1, %2, %3 offen offset:" #S " sc1\n\ts_mov_b64 exec, -1" : "+v"(R) : "v"(vV), "s"(rs4), "s"(KVOFF(t)), "s"(0x0000FFFF0000FFFFull))
#define LD2(a, b, t) asm volatile("buffer_load_dwordx4 %0, %6, %8, %9 offen sc1\n\tbuffer_load_dwordx4 %1, %6, %8, %10 offen sc1\n\ts_mov_b64 exec, %11\n\t" \
    "buffer_load_dwordx4 %2, %7, %8, %9 offen sc1\n\tbuffer_load_dwordx4 %3, %7, %8, %9 offen offset:512 sc1\n\t" \
    "buffer_load_dwordx4 %4, %7, %8, %10 offen sc1\n\tbuffer_load_dwordx4 %5, %7, %8, %10 offen offset:512 sc1\n\ts_mov_b64 exec, -1" \
    : "=&v"(kr[a]), "=&v"(kr[b]), "+v"(v0r[a]), "+v"(v1r[a]), "+v"(v0r[b]), "+v"(v1r[b]) : "v"(vK), "v"(vV), "s"(rs4), "s"(KVOFF(t)), "s"(KVOFF((t) + 1)), "s"(0x0000FFFF0000FFFFull))
#define WAITR2(N, R, Q) asm volatile("s_waitcnt vmcnt(" #N ")" : "+v"(R), "+v"(Q))
#define WAITR(N, R) asm volatile("s_waitcnt vmcnt(" #N ")" : "+v"(R))
    asm volatile("buffer_load_dwordx4 %0, %1, %2, %3 offen sc1" : "=v"(kref) : "v"(vK & ~2048u), "s"(rs4), "s"(KVOFF(0)));
    LDK(kr[0], 0);
#pragma unroll
    for (int t = 0; t < 4; ++t) { v0r[t] = ldsfrag(L + cst); v1r[t] = ldsfrag(L + cst); asm volatile("" ::: "memory"); }
    LDV(v0r[0], 0, 0); LDV(v1r[0], 0, 512);
    int aqa = L_QA + (w ^ 1) * 1024 + lane * 16, aqb = L_QA + w * 1024 + lane * 16;
    asm volatile("" : "+v"(aqa), "+v"(aqb));
    f32x16 oa = {}, ob = {};
    bf16x8 ofb[8];
    BAR();
    const bf16x8 qa = ldsfrag(L + aqa), qb = ldsfrag(L + aqb);
    asm volatile("s_waitcnt vmcnt(2)" : "+v"(kref), "+v"(kr[0]), "+v"(ownw));
    const f32x16 r0_ = MFMA(kref, qa, f32x16{}), r1_ = MFMA(kref, qb, f32x16{});
    asm volatile("" : "+v"(oa), "+v"(ob));
    SBAR();
    const float m0 = lmax16(r0_), m1 = lmax16(r1_);
    const int ot = w & 1, kq = w >> 1;
    {
        f32x16 negm;
        const float nm = -max32(fmx(m0, m1));
#pragma unroll
        for (int i = 0; i < 16; ++i) negm[i] = nm;
        f32x16 sa = MFMA(kr[0], qa, negm), sb = {};
        SBAR();
        LDK(kr[1], 1); LDV(v0r[1], 1, 0); LDV(v1r[1], 1, 512);
#define E(S, i) S[i] = EXP2(S[i])
#define PHASE(S, M1, M2, M3, X1, X2, X3)                                                          \
    do {                                                                                          \
        E(S, 0); E(S, 1); SBAR();                                                                 \
        M1; X1; SBAR();                                                                           \
        E(S, 2); E(S, 3); E(S, 4); E(S, 5); SBAR();                                               \
        M2; X2; SBAR();                                                                           \
        E(S, 6); E(S, 7); E(S, 8); E(S, 9); SBAR();                                               \
        M3; X3; SBAR();                                                                           \
        E(S, 10); E(S, 11); E(S, 12); E(S, 13); E(S, 14); E(S, 15); SBAR();                       \
    } while (0)
        const unsigned obo = WS_O + (2u << 20) + ((2 * otl + ot) * 32 + 8 * kq) * 1024 - w * 2048;
        auto handoff = [&]() {
            LAS char* EXW = L + L_EX + w * 2304 + lane * 16;
            *(LAS f32x4*)EXW = (f32x4){oa[0], oa[1], oa[2], oa[3]};
            *(LAS f32x4*)(EXW + 1024) = (f32x4){oa[4], oa[5], oa[6], oa[7]};
            *(LAS float*)(L + L_EX + w * 2304 + 2048 + lane * 4) = oa[8];
            asm volatile("" ::: "memory");
            *(LAS volatile unsigned*)(L + L_PAIR + w * 4) = 1;
        };
        const int psw = w < 4 ? PRIO_SWITCH : -1;
        if (w < 4) __builtin_amdgcn_s_setprio(2); else __builtin_amdgcn_s_setprio(1);
#define PSW(t, u) if (u == (PRIO_SWITCH & 3)) asm volatile("s_cmp_lg_u32 %0, %1\n\ts_cbranch_scc1 1f\n\ts_setprio 0\n1:" :: "s"(t), "s"(psw) : "scc")
#define PAIR(t, u)                                  \
        PSW(t, u);                                                                                                                                                      \
        PHASE(sa, ob = PV(v1r[(u + 3) & 3], sb, 1, ob), sb = MFMA(kr[u], qb, negm), oa = PV(v0r[u], sa, 0, oa), SBAR(); LD2(u ^ 2, u ^ 3, (t) + 2), SBAR(); WAITR(9, v0r[u]), (void)0); \
        WAITR2(8, v1r[u], kr[u + 1]); SBAR();                                                                                                                           \
        PHASE(sb, oa = PV(v1r[u], sa, 1, oa), sa = MFMA(kr[u + 1], qa, negm), ob = PV(v0r[u], sb, 0, ob), (void)0, (void)0, (void)0);                                   \
        PSW((t) + 1, u + 1);                                                                                                                                            \
        PHASE(sa, ob = PV(v1r[u], sb, 1, ob), sb = MFMA(kr[u + 1], qb, negm), oa = PV(v0r[u + 1], sa, 0, oa), (void)0, SBAR(); WAITR(7, v0r[u + 1]), (void)0);          \
        WAITR(6, v1r[u + 1]); SBAR();                                                                                                                                   \
        PHASE(sb, oa = PV(v1r[u + 1], sa, 1, oa), sa = MFMA(kr[u ^ 2], qa, negm), ob = PV(v0r[u + 1], sb, 0, ob), SBAR(); WAITR(5, kr[u ^ 2]), (void)0, (void)0)
#pragma unroll 1
        for (int t0 = 0; t0 < 28; t0 += 4) {
            PAIR(t0, 0);
            PAIR(t0 + 2, 2);
        }
        PAIR(28, 0);
#define OFB(k) ofb[k] = ld16(rs, vK, obo + (k) * 1024)
        asm volatile("s_waitcnt vmcnt(0)" : "+v"(v0r[2]), "+v"(v1r[2]), "+v"(kr[3]), "+v"(v0r[3]), "+v"(v1r[3]));
        lds_wait(L + L_OBS, 4);
        PHASE(sa, ob = PV(v1r[1], sb, 1, ob), sb = MFMA(kr[2], qb, negm), oa = PV(v0r[2], sa, 0, oa), OFB(0), OFB(1), OFB(2));
        PHASE(sb, oa = PV(v1r[2], sa, 1, oa), sa = MFMA(kr[3], qa, negm), ob = PV(v0r[2], sb, 0, ob), OFB(3), (void)0, (void)0);
        PHASE(sa, ob = PV(v1r[2], sb, 1, ob), sb = MFMA(kr[3], qb, negm), oa = PV(v0r[3], sa, 0, oa), OFB(4), OFB(5), OFB(6));
        PHASE(sb, oa = PV(v1r[3], sa, 1, oa), (void)0, ob = PV(v0r[3], sb, 0, ob), OFB(7), (void)0, handoff());
        ob = PV(v1r[3], sb, 1, ob);
#undef PHASE
#undef E
    }
    __builtin_amdgcn_s_setprio(0);
    int lane_ = __builtin_amdgcn_mbcnt_hi(~0u, __builtin_amdgcn_mbcnt_lo(~0u, 0u));
    asm volatile("" : "+v"(lane_));
    const int r32_ = lane_ & 31, h_ = lane_ >> 5, q4_ = (lane_ & 15) >> 2, trl_ = ((lane_ >> 4) & 1) * 32 + (lane_ & 3) * 8;
    const unsigned oao = WS_O + ((2 * otl + ot) * 32 + 8 * kq) * 1024, vp = lane_ < 8 ? (unsigned)(lane_ * 1024 + 1020) : 0x40000000u;
    auto probe = [&]() { return (unsigned)__ballot(__builtin_amdgcn_raw_buffer_load_b32(rs, vp, oao, 16) != 0xFFFFFFFFu); };
    const unsigned pr0 = __builtin_amdgcn_raw_buffer_load_b32(rs, vp, oao, 16);
    SBAR();
    const int wpo = kq * 16384 + (4 * h_ + q4_) * 128 + trl_;
    bf16x8 wv[8][2];
    auto wreads = [&](int base, int k0, int k1) {
#pragma unroll
        for (int k = k0; k < k1; ++k)
#pragma unroll
            for (int nt = 0; nt < 2; ++nt) wv[k][nt] = trfrag(L + base + wpo + k * 2048 + ((nt ^ (q4_ >> 1)) * 64), 1024);
    };
    const float gate = *(LAS const float*)(L + L_G + (kh * 32 + r32_) * 4);
    lds_wait(L + L_PAIR + (w ^ 1) * 4, 1);
    {
        LAS const char* EXR = L + L_EX + (w ^ 1) * 2304 + lane_ * 16;
        const f32x4 e0 = *(LAS const f32x4*)EXR, e1 = *(LAS const f32x4*)(EXR + 1024);
        const float e2 = *(LAS const float*)(L + L_EX + (w ^ 1) * 2304 + 2048 + lane_ * 4);
        SBAR();
        wreads(L_WOB, 0, 4);
        SBAR();
        const float l = ob[8] + e2;
#pragma unroll
        for (int i = 0; i < 4; ++i) { ob[i] += e0[i]; ob[4 + i] += e1[i]; }
        float rl = __builtin_amdgcn_rcpf(l), t = fmx(l, rl) * 1.0e19f;
#pragma unroll
        for (int i = 0; i < 8; i += 2) t = fmx(fmx(t, __builtin_fabsf(ob[i])), __builtin_fabsf(ob[i + 1]));
        if (__builtin_expect(__any(!(t < 1.0e38f)), 0)) {
            ob = f32x16{};
            float m = -INFINITY;
            for (int t2 = 0; t2 < 64; ++t2) {
                const unsigned so = kvb + (t2 >> 1) * BLK, fl = (t2 & 1) * 2048;
                const bf16x8 ones = {0x3F80, 0x3F80, 0x3F80, 0x3F80, 0x3F80, 0x3F80, 0x3F80, 0x3F80};
                const bf16x8 kf = ld16(rs, vK ^ fl, so), v0l = ld16(rs, vV ^ fl, so), v1l = ld16(rs, vV ^ fl, so + 512);
                const bf16x8 v0 = r32_ < 16 ? v0l : ones, v1 = r32_ < 16 ? v1l : ones;
                f32x16 s = MFMA(kf, qb, f32x16{});
                const float mn = fmx(max16(s), m), al = EXP2(m - mn);
                m = mn;
#pragma unroll
                for (int i = 0; i < 16; ++i) { ob[i] *= al; s[i] = EXP2(s[i] - mn); }
                ob = PV(v0, s, 0, ob);
                ob = PV(v1, s, 1, ob);
            }
            rl = __builtin_amdgcn_rcpf(ob[8]);
        }
        rl *= gate;
#pragma unroll
        for (int i = 0; i < 8; ++i) ob[i] *= rl;
        st16(rs, __builtin_bit_cast(bf16x8, __builtin_elementwise_min(__builtin_bit_cast(u32x4, pack8(ob, 0)), (u32x4){0xFFFFFFFEu, 0xFFFFFFFEu, 0xFFFFFFFEu, 0xFFFFFFFEu})),
             lane_ * 16, WS_O + ((2 * tl + kh) * 32 + head) * 1024);
        SBAR();
    }
    if (lane_ == 0) __hip_atomic_fetch_add((LAS unsigned*)(L + L_STORED), 1u, __ATOMIC_RELAXED, __HIP_MEMORY_SCOPE_WORKGROUP);
    f32x16 acc[2];
    f32x4 keep0 = {}, keep1 = {};
    unsigned dst = (64 * otl + 32 * ot + r32_) * 1024 + 128 * ocg + 32 * kq + 16 * h_;
    int src[3];
#pragma unroll
    for (int z = 0; z < 3; ++z) src[z] = L_WOB + ((kq + 1 + z) & 3) * 16384 + ot * 8192 + kq * 2048 + lane_ * 16;
    {
        bf16x8 of[8];
        auto loads = [&](int g) {
#pragma unroll
            for (int k = 4 * g; k < 4 * g + 4; ++k) of[k] = ld16(rs, lane_ * 16 + (k & 3) * 1024, oao + (k & 4) * 1024);
        };
        const bf16x8 none = {-1, -1, -1, -1, -1, -1, -1, -1};
        unsigned rdy = (unsigned)__ballot(pr0 != 0xFFFFFFFFu);
#pragma unroll
        for (int g = 0; g < 2; ++g) { if (((rdy >> (4 * g)) & 15) == 15) loads(g); else of[4 * g] = none; }
        SBAR();
#pragma unroll
        for (int k = 0; k < 8; ++k) {
#pragma unroll
            for (int nt = 0; nt < 2; ++nt) acc[nt] = MFMA(wv[k][nt], ofb[k], k ? acc[nt] : f32x16{});
            SBAR();
            if (k < 4) wreads(L_WOB, k + 4, k + 5); else wreads(L_WOA, k - 4, k - 3);
            SBAR();
        }
        wreads(L_WOA, 4, 8);
        lds_arrive(L + L_PAIR + w * 4, lane_);
        SBAR();
        auto missing = [&](int g) {
            const u32x4 v = __builtin_elementwise_max(__builtin_elementwise_max(__builtin_bit_cast(u32x4, of[4 * g]), __builtin_bit_cast(u32x4, of[4 * g + 1])),
                                                      __builtin_elementwise_max(__builtin_bit_cast(u32x4, of[4 * g + 2]), __builtin_bit_cast(u32x4, of[4 * g + 3])));
            return (bool)__any(__builtin_elementwise_max(__builtin_elementwise_max(v[0], v[1]), __builtin_elementwise_max(v[2], v[3])) == 0xFFFFFFFFu);
        };
        asm volatile("" : "+v"(dst), "+v"(src[0]), "+v"(src[1]), "+v"(src[2]));
        bool have1 = ((rdy >> 4) & 15) == 15;
#pragma unroll
        for (int g = 0; g < 2; ++g) {
#pragma unroll 1
            for (unsigned spins = 0; spins < SPIN_CAP && missing(g); ++spins) {
                rdy = lds_word(L + L_STORED) >= 8 ? 0xFFu : probe();
                asm volatile("" ::: "memory");
                if (g == 0 && !have1 && ((rdy >> 4) & 15) == 15) { loads(1); have1 = true; }
                if (((rdy >> (4 * g)) & 15) == 15) loads(g); else __builtin_amdgcn_s_sleep(RETRY_SLEEP);
            }
            if (g == 0) {
#pragma unroll
                for (int k = 0; k < 4; ++k) { acc[0] = MFMA(wv[k][0], of[k], acc[0]); acc[1] = MFMA(wv[k][1], of[k], acc[1]); }
                lds_wait(L + L_PAIR + (w ^ 1) * 4, 2);
            }
        }
        LAS char* PR = L + L_WOB + kq * 16384 + ot * 8192 + lane_ * 16;
#pragma unroll
        for (int nt = 0; nt < 2; ++nt)
#pragma unroll
            for (int k = 4; k < 8; ++k) acc[nt] = MFMA(wv[k][nt], of[k], acc[nt]);
        SBAR();
#pragma unroll
        for (int z = 0; z < 4; ++z) {
            const int b = 8 * (z & 1);
            const f32x4 u0 = {acc[z >> 1][b], acc[z >> 1][b + 1], acc[z >> 1][b + 2], acc[z >> 1][b + 3]}, u1 = {acc[z >> 1][b + 4], acc[z >> 1][b + 5], acc[z >> 1][b + 6], acc[z >> 1][b + 7]};
            if (z != kq) { *(LAS f32x4*)(PR + z * 2048) = u0; *(LAS f32x4*)(PR + z * 2048 + 1024) = u1; } else { keep0 = u0; keep1 = u1; }
        }
    }
    BAR();
    {
        f32x4 u[3][2];
#pragma unroll
        for (int z = 0; z < 3; ++z) { u[z][0] = *(LAS const f32x4*)(L + src[z]); u[z][1] = *(LAS const f32x4*)(L + src[z] + 1024); }
        SBAR();
        const f32x4 r0 = ((keep0 + u[0][0]) + u[1][0]) + u[2][0], r1 = ((keep1 + u[0][1]) + u[1][1]) + u[2][1];
        unsigned a0 = pk2(r0[0], r0[1]), a1 = pk2(r0[2], r0[3]), b0 = pk2(r1[0], r1[1]), b1 = pk2(r1[2], r1[3]);
        auto s0_ = __builtin_amdgcn_permlane32_swap(a0, b0, false, false);
        auto s1_ = __builtin_amdgcn_permlane32_swap(a1, b1, false, false);
        *(u32x4*)((char*)out + dst) = (u32x4){s0_[0], s1_[0], s0_[1], s1_[1]};
    }
    if (blockIdx.x == 0 && w == 0 && lane_ == 0) set_flag(ws, WS_EP, tag);
}

extern "C" void kernel_launch(void* const* d_in, const int* in_sizes, int n_in, void* d_out, int out_size, void* d_ws, size_t ws_size, hipStream_t stream) {
    fused_kernel<<<256, 768, 0, stream>>>((const uint16_t*)d_in[0], (const uint16_t*)d_in[1], (const uint16_t*)d_in[4], (const uint16_t*)d_in[5], (const uint16_t*)d_in[7],
                                          (const uint16_t*)d_in[2], (const uint16_t*)d_in[6], (uint16_t*)d_out, (char*)d_ws);
}
```

```cpp
#include <hip/hip_runtime.h>
#include <hip/hip_bf16.h>
#include <stdint.h>

typedef short bf16x8 __attribute__((ext_vector_type(8)));
typedef short s16x4 __attribute__((ext_vector_type(4)));
typedef float f32x16 __attribute__((ext_vector_type(16)));
typedef float f32x4 __attribute__((ext_vector_type(4)));
typedef float f32x2 __attribute__((ext_vector_type(2)));
typedef unsigned u32x4 __attribute__((ext_vector_type(4)));
typedef unsigned u32x2 __attribute__((ext_vector_type(2)));
typedef __bf16 bf16x2_t __attribute__((ext_vector_type(2)));
typedef unsigned long long u64;
#define LAS __attribute__((address_space(3)))
#define GAS __attribute__((address_space(1)))
#define DI __device__ __forceinline__
#define MFMA(a, b, c) __builtin_amdgcn_mfma_f32_32x32x16_bf16((a), (b), (c), 0, 0, 0)
typedef __bf16 bfx8 __attribute__((ext_vector_type(8)));
typedef __bf16 bfx16 __attribute__((ext_vector_type(16)));
#define PV(a, S, half, c) __builtin_amdgcn_smfmac_f32_32x32x32_bf16(__builtin_bit_cast(bfx8, a), \
    __builtin_bit_cast(bfx16, __builtin_shufflevector(S, S, 8 * half, 8 * half + 1, 8 * half + 2, 8 * half + 3, 8 * half + 4, 8 * half + 5, 8 * half + 6, 8 * half + 7)), (c), 0xDDDD, 0, 0)
#define SBAR() __builtin_amdgcn_sched_barrier(0)
#define RLX_AGENT __ATOMIC_RELAXED, __HIP_MEMORY_SCOPE_AGENT
#define EXP2(x) __builtin_amdgcn_exp2f(x)

constexpr float LOG2E = 1.4426950408889634f;
constexpr unsigned BLK = 16384;
constexpr unsigned WS_KV = 0;
constexpr unsigned WS_O = WS_KV + 256 * BLK;
constexpr unsigned WS_PK = WS_O + (4u << 20);
constexpr unsigned WS_KVF = WS_PK + 32 * 32 * 2176;
constexpr unsigned WS_F1 = WS_KVF + 32 * 2176;
constexpr unsigned WS_F2 = WS_F1 + 2048;
constexpr unsigned WS_F3 = WS_F2 + 8192;
constexpr unsigned WS_F5 = WS_F3 + 8192;
constexpr unsigned WS_EP = WS_F5 + 8192;
constexpr unsigned WS_END = WS_EP + 128;
constexpr int L_X = 0, L_RING = 65536;
constexpr int L_PHK = 65536, L_PHQ = 81920, L_VB = 16384;
constexpr int L_WOA = 0, L_WOB = 65536;
constexpr int L_WPHI = L_RING + 3 * 16384;
constexpr int L_G = 131072, L_SYNC = L_G + 256, L_STORED = L_SYNC + 4, L_HCNT = L_SYNC + 8, L_OBS = L_SYNC + 12, L_PAIR = L_SYNC + 16, L_ZEROS = L_SYNC + 96, L_ONES = L_SYNC + 128, L_DUMMY = L_SYNC + 256, L_G2 = L_SYNC + 512;
constexpr int L_QA = L_G + 1024, L_EX = L_QA + 8192;
constexpr int LDS_BYTES = 163840;
constexpr int F1_STEP = 14, PRIO_SWITCH = 23, PK_DELAY = 24, WO_T0 = 6400, WO_DT = 270, POLL_GAP = 2, POLL_FIRST = 6, RETRY_SLEEP = 2, PUB_DELAY = 4;
constexpr int CODE_LINES = 256;
constexpr unsigned SPIN_CAP = 1u << 15;

template <int N> struct IC { static constexpr int value = N; };
template <int I, int N, class F> DI void static_for(F&& f) { if constexpr (I < N) { f(IC<I>{}); static_for<I + 1, N>(f); } }
DI unsigned pk2(float a, float b) { f32x2 v = {a, b}; return __builtin_bit_cast(unsigned, __builtin_convertvector(v, bf16x2_t)); }
DI bf16x8 pack8(const f32x16& x, int s) {
    u32x4 p = {pk2(x[8 * s], x[8 * s + 1]), pk2(x[8 * s + 2], x[8 * s + 3]), pk2(x[8 * s + 4], x[8 * s + 5]), pk2(x[8 * s + 6], x[8 * s + 7])};
    return __builtin_bit_cast(bf16x8, p);
}
DI float fmx(float a, float b) { return __builtin_elementwise_maximum(a, b); }
DI float max32(float v) { auto r = __builtin_amdgcn_permlane32_swap(__float_as_uint(v), __float_as_uint(v), false, false); return fmx(__uint_as_float(r[0]), __uint_as_float(r[1])); }
DI float lmax16(const f32x16& s) {
    const float a = fmx(fmx(s[0], s[1]), s[2]), b = fmx(fmx(s[3], s[4]), s[5]), c = fmx(fmx(s[6], s[7]), s[8]), d = fmx(fmx(s[9], s[10]), s[11]), e = fmx(fmx(s[12], s[13]), s[14]);
    return fmx(fmx(fmx(a, b), c), fmx(fmx(d, e), s[15]));
}
DI float max16(const f32x16& s) { return max32(lmax16(s)); }
DI void glds2(unsigned v0, unsigned v1, const void* sbase, unsigned ldsdst) {
    unsigned keep;
    asm volatile("s_mov_b32 %0, m0\n\ts_mov_b32 m0, %4\n\ts_nop 0\n\tglobal_load_lds_dwordx4 %1, %3\n\tglobal_load_lds_dwordx4 %2, %3 offset:1024\n\ts_mov_b32 m0, %0"
                 : "=&s"(keep) : "v"(v0), "v"(v1), "s"(sbase), "s"(ldsdst) : "memory");
}
DI void glds4(const unsigned (&v)[4], const void* sbase, unsigned ldsdst) {
    unsigned keep;
    asm volatile("s_mov_b32 %0, m0\n\ts_mov_b32 m0, %6\n\ts_nop 0\n\tglobal_load_lds_dwordx4 %1, %5\n\tglobal_load_lds_dwordx4 %2, %5 offset:1024\n\t"
                 "global_load_lds_dwordx4 %3, %5 offset:2048\n\tglobal_load_lds_dwordx4 %4, %5 offset:3072\n\ts_mov_b32 m0, %0"
                 : "=&s"(keep) : "v"(v[0]), "v"(v[1]), "v"(v[2]), "v"(v[3]), "s"(sbase), "s"(ldsdst) : "memory");
}
DI void touch(unsigned voff, const void* sbase, unsigned ldsdst) {
    unsigned keep;
    asm volatile("s_mov_b32 %0, m0\n\ts_mov_b32 m0, %3\n\ts_nop 0\n\tglobal_load_lds_dword %1, %2 sc1\n\ts_mov_b32 m0, %0"
                 : "=&s"(keep) : "v"(voff), "s"(sbase), "s"(ldsdst) : "memory");
}
typedef short v4i16_t __attribute__((ext_vector_type(4)));
DI s16x4 trread(LAS const char* p) { return __builtin_bit_cast(s16x4, __builtin_amdgcn_ds_read_tr16_b64_v4i16((LAS v4i16_t*)p)); }
DI bf16x8 trfrag(LAS const char* p, int d2) { s16x4 lo = trread(p), hi = trread(p + d2); return __builtin_shufflevector(lo, hi, 0, 1, 2, 3, 4, 5, 6, 7); }
DI bf16x8 ldsfrag(LAS const char* p) { return *(LAS const bf16x8*)p; }
#define BAR() asm volatile("s_waitcnt lgkmcnt(0)\n\ts_barrier" ::: "memory")
#define VM_BAR(N) asm volatile("s_waitcnt vmcnt(" #N ") lgkmcnt(0)\n\ts_barrier" ::: "memory")
#define DRAIN() asm volatile("s_waitcnt vmcnt(0)" ::: "memory")
typedef __amdgpu_buffer_rsrc_t rsrc_t;
DI bf16x8 ld16(rsrc_t r, unsigned voff, unsigned soff) { return __builtin_bit_cast(bf16x8, __builtin_amdgcn_raw_buffer_load_b128(r, voff, soff, 16)); }
DI void st16(rsrc_t r, bf16x8 v, unsigned voff, unsigned soff) { __builtin_amdgcn_raw_buffer_store_b128(__builtin_bit_cast(u32x4, v), r, voff, soff, 16); }
DI unsigned lds_word(LAS char* p) { return __builtin_amdgcn_readfirstlane(*(LAS volatile unsigned*)p); }
DI void lds_wait(LAS char* p, unsigned want) {
#pragma unroll 1
    for (unsigned spins = 0; spins < (SPIN_CAP << 6) && lds_word(p) < want; ++spins) __builtin_amdgcn_s_sleep(1);
    asm volatile("" ::: "memory");
}
DI void lds_arrive(LAS char* p, int lane) {
    asm volatile("s_waitcnt lgkmcnt(0)" ::: "memory");
    if (lane == 0) __hip_atomic_fetch_add((LAS unsigned*)p, 1u, __ATOMIC_RELAXED, __HIP_MEMORY_SCOPE_WORKGROUP);
}
DI void set_flag(char* ws, unsigned off, u64 tag) { __hip_atomic_store((GAS u64*)(ws + off), tag, RLX_AGENT); }
template <int FIRST, int GAP> DI void wait_flags(char* ws, unsigned off, int n, u64 tag, int lane) {
    GAS u64* p = (GAS u64*)(ws + off) + (lane < n ? lane : 0);
    if (FIRST) __builtin_amdgcn_s_sleep(FIRST);
#pragma unroll 1
    for (unsigned spins = 0; spins < SPIN_CAP; ++spins) {
        const u64 v = __hip_atomic_load(p, RLX_AGENT);
        if (__all(v == tag)) break;
        __builtin_amdgcn_s_sleep(GAP);
    }
    __builtin_amdgcn_fence(__ATOMIC_ACQUIRE, "wavefront");
    asm volatile("" ::: "memory");
}

DI void softmax16(const f32x16& p, bf16x8 (&out)[2]) {
#define KEEP1(x) asm("" : "+v"(x))
    const float nm = -lmax16(p) * LOG2E;
    float e[16], t[8];
#pragma unroll
    for (int i = 0; i < 16; ++i) { e[i] = __builtin_fmaf(p[i], LOG2E, nm); KEEP1(e[i]); e[i] = EXP2(e[i]); }
#pragma unroll
    for (int i = 0; i < 8; ++i) { t[i] = e[2 * i] + e[2 * i + 1]; KEEP1(t[i]); }
#pragma unroll
    for (int i = 0; i < 4; ++i) { t[i] = t[2 * i] + t[2 * i + 1]; KEEP1(t[i]); }
    t[0] += t[1]; KEEP1(t[0]); t[2] += t[3]; KEEP1(t[2]);
    const float rs = __builtin_amdgcn_rcpf(t[0] + t[2]);
    u32x4 u[2];
#pragma unroll
    for (int i = 0; i < 16; ++i) { e[i] *= rs; KEEP1(e[i]); }
#pragma unroll
    for (int i = 0; i < 8; ++i) u[i >> 2][i & 3] = pk2(e[2 * i], e[2 * i + 1]);
    out[0] = __builtin_bit_cast(bf16x8, u[0]); out[1] = __builtin_bit_cast(bf16x8, u[1]);
}

__global__ __launch_bounds__(768, 3) void fused_kernel(const uint16_t* __restrict__ x, const uint16_t* __restrict__ WA, const uint16_t* __restrict__ WB,
                                                    const uint16_t* __restrict__ wphi, const uint16_t* __restrict__ gatew,
                                                    const uint16_t* __restrict__ woA, const uint16_t* __restrict__ woB,
                                                    uint16_t* __restrict__ out, char* ws) {
    __shared__ __attribute__((aligned(16))) char lds[LDS_BYTES];
    LAS char* L = (LAS char*)lds;
    u64 pc0; asm volatile("s_getpc_b64 %0" : "=s"(pc0));
    const unsigned lds0 = (unsigned)(uintptr_t)lds;
    const int tid = threadIdx.x, lane = tid & 63, w = __builtin_amdgcn_readfirstlane(tid >> 6);
    const int r32 = lane & 31, h = lane >> 5;
    const int xcd = blockIdx.x & 7, idx = blockIdx.x >> 3;
    const int qd = 2 * (xcd & 3) + (idx & 1), tl = 2 * (idx >> 1) + (xcd >> 2);
    const int qx = qd ^ 1;
    const int s0 = ((tl >> 1) + 4 * (qd & 1)) & 7;
    const int otl = 4 * xcd + (idx >> 3), ocg = idx & 7;
    const rsrc_t rs = __builtin_amdgcn_make_buffer_rsrc(ws, 0, WS_END, 0x00020000);
    u64 tag = 0; u32x2 ep;
#define LOAD_EPOCH() asm volatile("global_load_dwordx2 %0, %1, %2 sc1" : "=v"(ep) : "v"(0), "s"(ws + WS_EP) : "memory")
#define MAKE_TAG() do { asm volatile("" : "+v"(ep)); tag = ((u64)__builtin_amdgcn_readfirstlane(ep[1]) << 32 | (unsigned)__builtin_amdgcn_readfirstlane(ep[0])) + 1; } while (0)
    const int q4 = (lane & 15) >> 2;
    const int trl = ((lane >> 4) & 1) * 32 + (lane & 3) * 8;
    asm volatile("" :: "s"(x), "s"(WA), "s"(WB), "s"(wphi), "s"(gatew), "s"(ws));
    if (__builtin_expect(w >= 8, 1)) {
        const int j = w - 8;
        const unsigned vsw = (unsigned)(((lane & 7) ^ (((lane >> 4) & 1) << 2)) << 4);
        unsigned vw[4];
#pragma unroll
        for (int u = 0; u < 4; ++u) vw[u] = (unsigned)((lane >> 3) * 3072) + vsw + u * (8 * 3072 - 1024);
        const unsigned vx0 = (unsigned)((lane >> 3) * 1024 + (((lane & 7) ^ (lane >> 4)) << 4)), vx1 = (vx0 ^ 64u) + 8192 - 1024;
        const char* xb = (const char*)x + (size_t)(64 * tl + 16 * j) * 1024;
        const size_t wo_ = (size_t)((j & 1) * 32) * 3072 + qd * 128;
        const char* wb[3] = {(const char*)WA + wo_ + 1024 + (j >> 1) * 1024, (const char*)WB + wo_ + (j >> 1) * (1024 + (qx - qd) * 128), (j >> 1) ? (const char*)WB + wo_ + 2048 + (qx - qd) * 128 : (const char*)WA + wo_};
        auto issue_x = [&](int i) { const int kc = (s0 + i) & 7; glds2(vx0, vx1, xb + kc * 128, lds0 + L_X + i * 8192 + j * 2048); };
        auto issue_w = [&](int i) { glds4(vw, wb[i >> 3] + ((s0 + i) & 7) * (64 * 3072), lds0 + L_RING + ((i + 4) % 6) * 16384 + j * 4096); };
        __builtin_amdgcn_s_setprio(3);
        issue_w(0); issue_x(0); issue_x(1); issue_x(2); issue_x(3);
        bf16x8 gw; unsigned warm;
        asm volatile("global_load_dwordx4 %0, %1, %2" : "=v"(gw) : "v"((lane & 15) * 64 + (lane >> 4) * 16), "s"(gatew) : "memory");
        glds2(lane * 16, lane * 16, wphi + (j >> 1 ? qx : qd) * 1024, lds0 + L_WPHI + (j >> 1) * 2048);
        issue_w(1);
        int laneh = lane; asm volatile("" : "+v"(laneh));
        const int r32h = laneh & 31, hh = laneh >> 5, q4h = (laneh & 15) >> 2, trlh = ((laneh >> 4) & 1) * 32 + (laneh & 3) * 8;
        const unsigned vswh = (unsigned)(((laneh & 7) ^ (((laneh >> 4) & 1) << 2)) << 4);
        const int head = 4 * qd + j;
        const bool keep = (r32h >> 4) == (j & 1);
        constexpr unsigned OOB = 0x40000000u;
        unsigned a_ve[4], a_pf[2], a_kf[4], vo[4];
#pragma unroll
        for (int u = 0; u < 4; ++u) {
            a_ve[u] = keep ? L_VB + (j >> 1) * 4096 + u * 1024 + laneh * 16 : L_ONES;
            const unsigned n = u * 136 + ((r32h < 16 ? r32h : 16) * 2 + hh) * 4;
            a_kf[u] = (n >> 5) * 128 + (n & 31) * 2;
            vo[u] = (unsigned)((laneh >> 3) * 1024) + vswh + u * (8192 - 1024);
            asm volatile("" : "+v"(a_ve[u]), "+v"(a_kf[u]), "+v"(vo[u]));
        }
        a_pf[0] = L_PHK + j * 4096 + (4 * hh + q4h) * 64 + (trlh ^ ((2 * hh + (q4h >> 1)) * 8)); a_pf[1] = (a_pf[0] ^ 32) + 512;
        unsigned a_l16 = laneh * 16, a_q = L_PHQ + j * 4096 + laneh * 16, a_g2 = L_G2 + r32h * 4;
        unsigned v_pk = (keep || (r32h & 15) == 0) ? (unsigned)(((keep ? (r32h & 15) : 16) * 2 + hh) * 16) : OOB;
        unsigned v_one = laneh == 0 ? 0u : OOB, v_p32 = r32h * 8, v_p17 = (laneh < 17 ? laneh : 0) * 8, v_p8 = (laneh & 7) * 8;
        unsigned v_h1 = hh * 8704 + r32h * 4, v_h1s = (laneh < 32 && !(laneh & 1)) ? laneh * 2 : OOB, v_tch = ((laneh >> 3) & 1) * 32768 + (laneh & 7) * 128;
        bf16x8 none = {-1, -1, -1, -1, -1, -1, -1, -1};
        asm volatile("" : "+v"(a_pf[0]), "+v"(a_pf[1]), "+v"(a_l16), "+v"(a_q), "+v"(a_g2), "+v"(v_pk), "+v"(v_one), "+v"(v_p32), "+v"(v_p17), "+v"(v_p8));
        asm volatile("" : "+v"(v_h1), "+v"(v_h1s), "+v"(v_tch), "+v"(none));
        VM_BAR(13); issue_w(2); issue_w(3);
        VM_BAR(8); issue_x(4); issue_w(4);
        VM_BAR(10); issue_x(5); issue_w(5);
        VM_BAR(12); issue_x(6); issue_w(6);
        VM_BAR(12); issue_x(7); issue_w(7);
        VM_BAR(12);
        asm volatile("global_load_dword %0, %1, %2" : "=v"(warm) : "v"(((tl >> 1) * 32 + (lane & 15)) * 3072), "s"(wb[2] - (j & 1) * 16 * 3072) : "memory");
        LOAD_EPOCH();
        issue_w(8);
        VM_BAR(12);
        VM_BAR(6); issue_w(9); issue_w(10); issue_w(11);
        u32x2 tagv;
        f32x4 gl = {};
        const int grow = 16 * j + (lane & 15);
        int gxa = L_X + grow * 128 + (((lane >> 4) ^ ((grow >> 1) & 7)) << 4);
        u64 f1early = 0;
        unsigned a_gt = ((lane & 16) ? L_G2 : L_G) + grow * 4;
        asm volatile("" : "+v"(a_gt));
#pragma unroll
        for (int i = 8; i < 24; i += 2) {
            if (i < 22) VM_BAR(8); else VM_BAR(0);
            if (i == 8) asm volatile("" : "+v"(gw), "+v"(warm));
            bf16x8 xg[4];
            if (i < 16) {
#pragma unroll
                for (int u = 0; u < 4; ++u) xg[u] = ldsfrag(L + (gxa ^ ((u & 1) << 6)) + (i - 8 + (u >> 1)) * 8192);
            }
            SBAR();
            if (i < 20) { issue_w(i + 4); issue_w(i + 5); }
            SBAR();
            if (i < 16) {
                asm volatile("" : "+v"(gw));
#pragma unroll
                for (int u = 0; u < 4; ++u)
                    gl = __builtin_amdgcn_mfma_f32_16x16x32_bf16((lane & 15) == 2 * ((s0 + i + (u >> 1)) & 7) + (u & 1) ? gw : bf16x8{}, xg[u], gl, 0, 0, 0);
                asm volatile("" : "+v"(gl));
            }
            if (j == 0 && i == 22) {
                if (!__all(f1early == tag)) wait_flags<0, 2>(ws, WS_F1 + qd * 256, 32, tag, lane);
                touch(a_l16 * 8, ws + WS_KV + (qd * 32 + ((tl + 1) & 31)) * BLK, lds0 + L_DUMMY);
                touch(a_l16 * 8, ws + WS_KV + (qd * 32 + ((tl + 2) & 31)) * BLK, lds0 + L_DUMMY);
            }
            if (i == 22) {
                float z = (gl[0] + gl[1]) + (gl[2] + gl[3]);
                z += __shfl_xor(z, 16); z += __shfl_xor(z, 32);
                const float g = __builtin_amdgcn_rcpf(1.f + EXP2(-z * LOG2E));
                *(LAS float*)(L + a_gt) = (lane & 16) ? 1.f - g : g;
            }
            if (i == 16) { MAKE_TAG(); tagv = (u32x2){(unsigned)tag, (unsigned)(tag >> 32)}; asm volatile("" : "+v"(tagv)); }
            if (j == 0 && i == 20) {
                f1early = __hip_atomic_load((GAS u64*)(ws + WS_F1 + qd * 256) + (lane & 31), RLX_AGENT);
                if (lane < 36) *(LAS unsigned*)(L + L_SYNC + lane * 4) = lane >= 32 ? 0x3F803F80u : 0u;
            }
            SBAR();
        }
        auto publish_flag = [&](unsigned off) { DRAIN(); __builtin_amdgcn_raw_buffer_store_b64(tagv, rs, v_one, off, 16); };
        BAR();
        const u64 tp2 = __builtin_amdgcn_s_memtime();
        __builtin_amdgcn_s_sleep(6);
        bf16x8 phq[2][2]; float g2[2];
        {
            bf16x8 pf[4], ve[4];
#pragma unroll
            for (int u = 0; u < 4; ++u) {
                pf[u] = __builtin_shufflevector(trread(L + a_pf[0] + u * 1024), trread(L + a_pf[1] + u * 1024), 0, 1, 2, 3, 4, 5, 6, 7);
                ve[u] = ldsfrag(L + a_ve[u]);
                phq[u >> 1][u & 1] = ldsfrag(L + a_q + u * 1024);
            }
            g2[0] = *(LAS const float*)(L + a_g2); g2[1] = *(LAS const float*)(L + a_g2 + 128);
            lds_arrive(L + L_HCNT, lane);
            __builtin_amdgcn_s_sleep(PK_DELAY);
            touch(v_p8 * 16, (const char*)(j & 2 ? woA : woB) + blockIdx.x * 2048 + (j & 1) * 1024, lds0 + L_DUMMY);
            f32x16 kv = MFMA(pf[0], ve[0], f32x16{});
#pragma unroll
            for (int u = 1; u < 4; ++u) kv = MFMA(pf[u], ve[u], kv);
#pragma unroll
            for (int g = 0; g < 4; ++g) st16(rs, __builtin_bit_cast(bf16x8, (f32x4){kv[4 * g], kv[4 * g + 1], kv[4 * g + 2], kv[4 * g + 3]}), v_pk, WS_PK + ((qx * 32 + tl) * 4 + j) * 2176 + g * 544);
            publish_flag(WS_F2 + ((4 * qx + j) * 32 + tl) * 8);
        }
        lds_wait(L + L_HCNT, 4);
        int wo_done = 0;
        auto wo_service = [&]() {
            if (wo_done < 8 && __builtin_amdgcn_s_memtime() - tp2 >= (u64)(WO_T0 + WO_DT * (j + 4 * wo_done))) {
                const int mtx = wo_done >> 2, pc = wo_done & 3;
                glds4(vo, (const char*)(mtx ? woA : woB) + (size_t)(128 * j) * 1024 + ocg * 128 + pc * 32768, lds0 + (mtx ? L_WOA : L_WOB) + j * 16384 + pc * 4096);
                ++wo_done;
            }
        };
        auto wait_slow = [&](unsigned off, unsigned v, int first) {
#pragma unroll 1
            for (unsigned spins = 0; spins < SPIN_CAP; ++spins) {
#pragma unroll 1
                for (int z = 0; z < (spins ? POLL_GAP : first); ++z) { wo_service(); __builtin_amdgcn_s_sleep(6); }
                asm volatile("" ::: "memory");
                if (__all(__builtin_bit_cast(u64, __builtin_amdgcn_raw_buffer_load_b64(rs, v, off, 16)) == tag)) break;
            }
            __builtin_amdgcn_fence(__ATOMIC_ACQUIRE, "wavefront");
            asm volatile("" ::: "memory");
        };
        if (tl < 17) {
            wait_slow(WS_F2 + head * 256, v_p32, POLL_FIRST);
            float t[16];
#pragma unroll
            for (int i = 0; i < 16; ++i) t[i] = __uint_as_float(__builtin_amdgcn_raw_buffer_load_b32(rs, v_h1, WS_PK + (qd * 128 + j) * 2176 + tl * 128 + i * 17408, 16));
#pragma unroll
            for (int n = 8; n >= 1; n >>= 1)
#pragma unroll
                for (int i = 0; i < n; ++i) { t[i] += t[i + n]; KEEP1(t[i]); }
            auto r = __builtin_amdgcn_permlane32_swap(__float_as_uint(t[0]), __float_as_uint(t[0]), false, false);
            const float x0 = __uint_as_float(r[0]) + __uint_as_float(r[1]);
            const float x1 = __uint_as_float(__builtin_amdgcn_update_dpp(0, __float_as_uint(x0), 0xF5, 0xF, 0xF, true));
            __builtin_amdgcn_raw_buffer_store_b32(pk2(x0, x1), rs, v_h1s, WS_KVF + head * 2176 + tl * 128, 16);
            publish_flag(WS_F3 + (head * 32 + tl) * 8);
        }
        wait_slow(WS_F3 + head * 256, v_p17, tl < 17 ? POLL_FIRST : 2 * POLL_FIRST + 4);
        {
            u32x2 kq_[4];
#pragma unroll
            for (int g = 0; g < 4; ++g) kq_[g] = __builtin_amdgcn_raw_buffer_load_b64(rs, a_kf[g], WS_KVF + head * 2176, 16);
            const bf16x8 k0 = __builtin_bit_cast(bf16x8, __builtin_shufflevector(kq_[0], kq_[1], 0, 1, 2, 3)), k1 = __builtin_bit_cast(bf16x8, __builtin_shufflevector(kq_[2], kq_[3], 0, 1, 2, 3));
#pragma unroll
            for (int tt = 0; tt < 2; ++tt) {
                f32x16 ob = MFMA(k0, phq[tt][0], f32x16{});
                ob = MFMA(k1, phq[tt][1], ob);
                const float rd = g2[tt] * __builtin_amdgcn_rcpf(ob[8] + 1e-6f);
#pragma unroll
                for (int i = 0; i < 8; ++i) { float z = ob[i] * rd; KEEP1(z); ob[i] = z; }
                st16(rs, pack8(ob, 0), a_l16, WS_O + (2u << 20) + ((2 * tl + tt) * 32 + head) * 1024);
                st16(rs, none, a_l16, WS_O + ((2 * tl + tt) * 32 + head) * 1024);
            }
            publish_flag(WS_F5 + (tl * 32 + head) * 8);
        }
        wait_slow(WS_F5 + (otl * 32 + 8 * j) * 8, v_p8, POLL_FIRST);
        touch(v_tch, ws + WS_O + (2u << 20) + (2 * otl * 32 + 8 * j + ocg) * 1024, lds0 + L_DUMMY);
        while (wo_done < 8) { wo_service(); __builtin_amdgcn_s_sleep(6); }
        DRAIN();
        lds_arrive(L + L_OBS, lane);
        return;
    }

    if (w == 0) {
        const char* a = (const char*)(pc0 & ~127ull) + (idx * 9 + lane < CODE_LINES ? idx * 9 + lane : 0) * 128;
        if (lane == 9) {
            a = ws + (size_t)(idx < 21 ? idx : 0) * 524288;
            if (idx >= 22) a = idx < 25 ? (const char*)WB + (idx - 22) * 786368 : idx < 28 ? (const char*)out + (idx - 25) * 1048512 : idx < 30 ? (const char*)woA + (idx - 28) * 524224 : (const char*)woB + (idx - 30) * 524224;
        }
        unsigned keep;
        if (lane < 10) asm volatile("s_mov_b32 %0, m0\n\ts_mov_b32 m0, %2\n\ts_nop 0\n\tglobal_load_lds_dword %1, off sc1\n\ts_mov_b32 m0, %0" : "=&s"(keep) : "v"(a), "s"(lds0 + L_WPHI + 4096) : "memory");
    }
    const int p = w & 1;
    const int hq = w >> 1, kh = w & 1, head = 4 * qd + hq;
    const unsigned vK = w * 2048 + lane * 16;
    const unsigned vV = (unsigned)(w * 2048 + 1024 + h * 256 + (r32 & 15) * 16);
    const unsigned kvb = WS_KV + qd * 32 * BLK;
#define KVOFF(t) (kvb + (((t) + tl) & 31) * BLK)
    bf16x8 kr[4], v0r[4], v1r[4];
    const int kpos = ((lane & 0x33) | ((lane & 4) << 1) | ((lane & 8) >> 1)) * 16;
    bf16x8 kref; unsigned ownw = 0;
    const int cst = (r32 & 11) ? L_ZEROS : L_ONES;
    {
        const int xa0 = L_X + r32 * 128 + ((h ^ ((r32 >> 1) & 7)) << 4);
        const int wa0 = L_RING + ((w >> 1) & 1) * 8192 + (8 * h + q4) * 128 + ((p ^ (q4 >> 1)) * 64) + trl;
        bf16x8 xf[2][4][2], w0[2][4];
        f32x16 acc[2];
        int wsb[3], xsb[4];
#pragma unroll
        for (int i = 0; i < 3; ++i) { wsb[i] = wa0 + i * 32768; asm volatile("" : "+v"(wsb[i])); }
#pragma unroll
        for (int s = 0; s < 4; ++s) { xsb[s] = xa0 ^ (s << 5); asm volatile("" : "+v"(xsb[s])); }
        auto gemm = [&](int TEAM, int TYPE) __attribute__((always_inline)) {
            auto mine = [&](int i) __attribute__((always_inline)) { return i >= 0 && i < 24 && ((i >> 3) & 1) == TEAM; };
            auto rx = [&](int i, int s, int t) __attribute__((always_inline)) { if (mine(i)) xf[i & 1][s][t] = ldsfrag(L + xsb[s] + (i & 7) * 8192 + t * 4096); };
            auto rw = [&](int i, int s) __attribute__((always_inline)) { if (mine(i)) w0[i & 1][s] = trfrag(L + wsb[((i + 4) % 6) >> 1] + (i & 1) * 16384 + s * 2048, 512); };
            auto mm = [&](int i, int s, int t, bool now = false) __attribute__((always_inline)) {
                SBAR();
                if (mine(i) && (i != 7 || now)) {
                    const f32x16 c = ((i & 7) == 0 && s == 0) ? f32x16{} : acc[t];
                    acc[t] = (TEAM == 0 && TYPE) ? MFMA(xf[i & 1][s][t], w0[i & 1][s], c) : MFMA(w0[i & 1][s], xf[i & 1][s][t], c);
                }
                SBAR();
            };
            bf16x8 own[2][2], wf[2], ph[4][2];
            f32x16 f[4];
            static_for<0, 25>([&](auto ic) __attribute__((always_inline)) {
                constexpr int i = ic.value;
                if (i == F1_STEP) { VM_BAR(0); if (TEAM == 0 && TYPE == 0) { MAKE_TAG(); if (tid == 0) set_flag(ws, WS_F1 + (qd * 32 + tl) * 8, tag); } }
                else if (i == 2) { __builtin_amdgcn_s_waitcnt(0x0F70); BAR(); }
                else if (i < 8 || (i < 24 && !(i & 1))) BAR();
                mm(i - 1, 0, 0); rw(i, 0); rx(i, 0, 0);
                mm(i - 1, 0, 1); rx(i, 0, 1); rw(i, 1);
                mm(i - 1, 1, 0); rx(i, 1, 0); rx(i, 1, 1);
                mm(i - 1, 1, 1); rw(i, 2); rx(i, 2, 0);
                mm(i - 1, 2, 0); rx(i, 2, 1); rw(i, 3);
                mm(i - 1, 2, 1); rx(i, 3, 0); rx(i, 3, 1);
                mm(i - 1, 3, 0);
                mm(i - 1, 3, 1);
                if (TEAM == 0 && TYPE == 0 && i == 7) LOAD_EPOCH();
                if (TEAM == 0 && i == 7) {
#pragma unroll
                    for (int z = 0; z < 8; ++z) mm(7, z >> 1, z & 1, true);
#pragma unroll
                    for (int t = 0; t < 2; ++t) {
                        if (TYPE == 0) {
#pragma unroll
                            for (int e = 0; e < 16; ++e) acc[t][e] *= 0.25f * LOG2E;
                        }
                        own[t][0] = pack8(acc[t], 0); own[t][1] = pack8(acc[t], 1);
                        asm volatile("" : "+v"(own[t][0]), "+v"(own[t][1]));
                    }
                }
                if (TEAM == 0 && i == 8) {
                    __builtin_amdgcn_s_sleep(PUB_DELAY);
#pragma unroll
                    for (int t = 0; t < 2; ++t) {
                        const unsigned so = kvb + tl * BLK + (4 * p + t) * 2048;
#pragma unroll
                        for (int a = 0; a < 2; ++a) {
                            if (TYPE == 0) st16(rs, own[t][a], kpos, so + a * 4096);
                            else st16(rs, own[t][a], (unsigned)((r32 >> 4) * 4096 + 1024 + h * 256 + (r32 & 15) * 16) + a * 512, so);
                        }
                    }
                }
                if (TEAM == 1) {
                    if (i == 1) {
                        const short sg = (lane & 4) ? (short)0x8000 : (short)0;
#pragma unroll
                        for (int a = 0; a < 2; ++a) {
                            wf[a] = trfrag(L + L_WPHI + ((w >> 1) & 1) * 2048 + (2 * p + a) * 512 + (4 * h + q4) * 32 + (((lane >> 4) & 1) * 2 + ((lane >> 1) & 1)) * 8, 256);
#pragma unroll
                            for (int e = 0; e < 8; ++e) wf[a][e] ^= sg;
                            asm volatile("" : "+v"(wf[a]));
                        }
                    }
                    if (i == 16) {
                        __builtin_amdgcn_s_setprio(1);
#pragma unroll
                        for (int m = 0; m < 4; ++m) f[m] = MFMA(wf[m >> 1], pack8(acc[m & 1], m >> 1), f32x16{});
                    }
                    if (i == 19) asm volatile("global_load_dword %0, %1, %2 sc1" : "=v"(ownw) : "v"(r32 * 128), "s"(ws + kvb + tl * BLK + (w - 4) * 4096) : "memory");
                    if (i >= 16 && i < 24 && !(i & 1)) { softmax16(f[(i - 16) / 2], ph[(i - 16) / 2]); asm volatile("" : "+v"(ph[(i - 16) / 2][0]), "+v"(ph[(i - 16) / 2][1])); }
                }
                SBAR();
            });
            if (TEAM == 0) {
#pragma unroll
                for (int t = 0; t < 2; ++t)
#pragma unroll
                    for (int a = 0; a < 2; ++a)
                        *(LAS bf16x8*)(L + (TYPE ? L_VB + ((p * 2 + t) * 2 + a) * 1024 : L_QA + ((2 * p + a) * 2 + t) * 1024) + lane * 16) = pack8(acc[t], a);
            } else if (w < 6) {
#pragma unroll
                for (int m = 0; m < 8; ++m) *(LAS bf16x8*)(L + L_PHQ + (8 * p + m) * 1024 + lane * 16) = ph[m >> 1][m & 1];
            } else {
#pragma unroll
                for (int m = 0; m < 8; ++m) {
                    const u32x4 u = __builtin_bit_cast(u32x4, ph[m >> 1][m & 1]);
#pragma unroll
                    for (int g = 0; g < 2; ++g)
                        *(LAS u32x2*)(L + L_PHK + (4 * p + (m >> 1)) * 2048 + r32 * 64 + 8 * ((2 * (2 * (m & 1) + g) + h) ^ ((r32 >> 1) & 7))) = (u32x2){u[2 * g], u[2 * g + 1]};
                }
            }
        };
        if (w < 2) gemm(0, 0); else if (w < 4) gemm(0, 1); else gemm(1, 0);
    }
    const u32x4 rs4 = {(unsigned)(uintptr_t)ws, (unsigned)((uintptr_t)ws >> 32) & 0xFFFFu, WS_END, 0x00020000u};
#define LDK(R, t) asm volatile("buffer_load_dwordx4 %0, %1, %2, %3 offen sc1" : "=v"(R) : "v"(vK), "s"(rs4), "s"(KVOFF(t)))
#define LDV(R, t, S) asm volatile("s_mov_b64 exec, %4\n\tbuffer_load_dwordx4 %0, %1, %2, %3 offen offset:" #S " sc1\n\ts_mov_b64 exec, -1" : "+v"(R) : "v"(vV), "s"(rs4), "s"(KVOFF(t)), "s"(0x0000FFFF0000FFFFull))
#define LD2(a, b, t) asm volatile("buffer_load_dwordx4 %0, %6, %8, %9 offen sc1\n\tbuffer_load_dwordx4 %1, %6, %8, %10 offen sc1\n\ts_mov_b64 exec, %11\n\t" \
    "buffer_load_dwordx4 %2, %7, %8, %9 offen sc1\n\tbuffer_load_dwordx4 %3, %7, %8, %9 offen offset:512 sc1\n\t" \
    "buffer_load_dwordx4 %4, %7, %8, %10 offen sc1\n\tbuffer_load_dwordx4 %5, %7, %8, %10 offen offset:512 sc1\n\ts_mov_b64 exec, -1" \
    : "=&v"(kr[a]), "=&v"(kr[b]), "+v"(v0r[a]), "+v"(v1r[a]), "+v"(v0r[b]), "+v"(v1r[b]) : "v"(vK), "v"(vV), "s"(rs4), "s"(KVOFF(t)), "s"(KVOFF((t) + 1)), "s"(0x0000FFFF0000FFFFull))
#define WAITR2(N, R, Q) asm volatile("s_waitcnt vmcnt(" #N ")" : "+v"(R), "+v"(Q))
#define WAITR(N, R) asm volatile("s_waitcnt vmcnt(" #N ")" : "+v"(R))
    asm volatile("buffer_load_dwordx4 %0, %1, %2, %3 offen sc1" : "=v"(kref) : "v"(vK & ~2048u), "s"(rs4), "s"(KVOFF(0)));
    LDK(kr[0], 0);
#pragma unroll
    for (int t = 0; t < 4; ++t) { v0r[t] = ldsfrag(L + cst); v1r[t] = ldsfrag(L + cst); asm volatile("" ::: "memory"); }
    LDV(v0r[0], 0, 0); LDV(v1r[0], 0, 512);
    int aqa = L_QA + (w ^ 1) * 1024 + lane * 16, aqb = L_QA + w * 1024 + lane * 16;
    asm volatile("" : "+v"(aqa), "+v"(aqb));
    f32x16 oa = {}, ob = {};
    bf16x8 ofb[8];
    BAR();
    const bf16x8 qa = ldsfrag(L + aqa), qb = ldsfrag(L + aqb);
    asm volatile("s_waitcnt vmcnt(2)" : "+v"(kref), "+v"(kr[0]), "+v"(ownw));
    const f32x16 r0_ = MFMA(kref, qa, f32x16{}), r1_ = MFMA(kref, qb, f32x16{});
    asm volatile("" : "+v"(oa), "+v"(ob));
    SBAR();
    const float m0 = lmax16(r0_), m1 = lmax16(r1_);
    const int ot = w & 1, kq = w >> 1;
    {
        f32x16 negm;
        const float nm = -max32(fmx(m0, m1));
#pragma unroll
        for (int i = 0; i < 16; ++i) negm[i] = nm;
        f32x16 sa = MFMA(kr[0], qa, negm), sb = {};
        SBAR();
        LDK(kr[1], 1); LDV(v0r[1], 1, 0); LDV(v1r[1], 1, 512);
#define E(S, i) S[i] = EXP2(S[i])
#define PHASE(S, M1, M2, M3, X1, X2, X3)                                                          \
    do {                                                                                          \
        E(S, 0); E(S, 1); SBAR();                                                                 \
        M1; X1; SBAR();                                                                           \
        E(S, 2); E(S, 3); E(S, 4); E(S, 5); SBAR();                                               \
        M2; X2; SBAR();                                                                           \
        E(S, 6); E(S, 7); E(S, 8); E(S, 9); SBAR();                                               \
        M3; X3; SBAR();                                                                           \
        E(S, 10); E(S, 11); E(S, 12); E(S, 13); E(S, 14); E(S, 15); SBAR();                       \
    } while (0)
        const unsigned obo = WS_O + (2u << 20) + ((2 * otl + ot) * 32 + 8 * kq) * 1024 - w * 2048;
        auto handoff = [&]() {
            LAS char* EXW = L + L_EX + w * 2304 + lane * 16;
            *(LAS f32x4*)EXW = (f32x4){oa[0], oa[1], oa[2], oa[3]};
            *(LAS f32x4*)(EXW + 1024) = (f32x4){oa[4], oa[5], oa[6], oa[7]};
            *(LAS float*)(L + L_EX + w * 2304 + 2048 + lane * 4) = oa[8];
            asm volatile("" ::: "memory");
            *(LAS volatile unsigned*)(L + L_PAIR + w * 4) = 1;
        };
        const int psw = w < 4 ? PRIO_SWITCH : -1;
        if (w < 4) __builtin_amdgcn_s_setprio(2); else __builtin_amdgcn_s_setprio(1);
#define PSW(t, u) if (u == (PRIO_SWITCH & 3)) asm volatile("s_cmp_lg_u32 %0, %1\n\ts_cbranch_scc1 1f\n\ts_setprio 0\n1:" :: "s"(t), "s"(psw) : "scc")
#define PAIR(t, u)                                  \
        PSW(t, u);                                                                                                                                                      \
        PHASE(sa, ob = PV(v1r[(u + 3) & 3], sb, 1, ob), sb = MFMA(kr[u], qb, negm), oa = PV(v0r[u], sa, 0, oa), SBAR(); LD2(u ^ 2, u ^ 3, (t) + 2), SBAR(); WAITR(9, v0r[u]), (void)0); \
        WAITR2(8, v1r[u], kr[u + 1]); SBAR();                                                                                                                           \
        PHASE(sb, oa = PV(v1r[u], sa, 1, oa), sa = MFMA(kr[u + 1], qa, negm), ob = PV(v0r[u], sb, 0, ob), (void)0, (void)0, (void)0);                                   \
        PSW((t) + 1, u + 1);                                                                                                                                            \
        PHASE(sa, ob = PV(v1r[u], sb, 1, ob), sb = MFMA(kr[u + 1], qb, negm), oa = PV(v0r[u + 1], sa, 0, oa), (void)0, SBAR(); WAITR(7, v0r[u + 1]), (void)0);          \
        WAITR(6, v1r[u + 1]); SBAR();                                                                                                                                   \
        PHASE(sb, oa = PV(v1r[u + 1], sa, 1, oa), sa = MFMA(kr[u ^ 2], qa, negm), ob = PV(v0r[u + 1], sb, 0, ob), SBAR(); WAITR(5, kr[u ^ 2]), (void)0, (void)0)
#pragma unroll 1
        for (int t0 = 0; t0 < 28; t0 += 4) {
            PAIR(t0, 0);
            PAIR(t0 + 2, 2);
        }
        PAIR(28, 0);
#define OFB(k) ofb[k] = ld16(rs, vK, obo + (k) * 1024)
        asm volatile("s_waitcnt vmcnt(0)" : "+v"(v0r[2]), "+v"(v1r[2]), "+v"(kr[3]), "+v"(v0r[3]), "+v"(v1r[3]));
        lds_wait(L + L_OBS, 4);
        PHASE(sa, ob = PV(v1r[1], sb, 1, ob), sb = MFMA(kr[2], qb, negm), oa = PV(v0r[2], sa, 0, oa), OFB(0), OFB(1), OFB(2));
        PHASE(sb, oa = PV(v1r[2], sa, 1, oa), sa = MFMA(kr[3], qa, negm), ob = PV(v0r[2], sb, 0, ob), OFB(3), (void)0, (void)0);
        PHASE(sa, ob = PV(v1r[2], sb, 1, ob), sb = MFMA(kr[3], qb, negm), oa = PV(v0r[3], sa, 0, oa), OFB(4), OFB(5), OFB(6));
        PHASE(sb, oa = PV(v1r[3], sa, 1, oa), (void)0, ob = PV(v0r[3], sb, 0, ob), OFB(7), (void)0, handoff());
        ob = PV(v1r[3], sb, 1, ob);
#undef PHASE
#undef E
    }
    __builtin_amdgcn_s_setprio(0);
    int lane_ = __builtin_amdgcn_mbcnt_hi(~0u, __builtin_amdgcn_mbcnt_lo(~0u, 0u));
    asm volatile("" : "+v"(lane_));
    const int r32_ = lane_ & 31, h_ = lane_ >> 5, q4_ = (lane_ & 15) >> 2, trl_ = ((lane_ >> 4) & 1) * 32 + (lane_ & 3) * 8;
    const unsigned oao = WS_O + ((2 * otl + ot) * 32 + 8 * kq) * 1024, vp = lane_ < 8 ? (unsigned)(lane_ * 1024 + 1020) : 0x40000000u;
    const unsigned pr0 = __builtin_amdgcn_raw_buffer_load_b32(rs, vp, oao, 16);
    SBAR();
    const int wpo = kq * 16384 + (4 * h_ + q4_) * 128 + trl_;
    bf16x8 wv[8][2];
    auto wreads = [&](int base, int k0, int k1) {
#pragma unroll
        for (int k = k0; k < k1; ++k)
#pragma unroll
            for (int nt = 0; nt < 2; ++nt) wv[k][nt] = trfrag(L + base + wpo + k * 2048 + ((nt ^ (q4_ >> 1)) * 64), 1024);
    };
    const float gate = *(LAS const float*)(L + L_G + (kh * 32 + r32_) * 4);
    lds_wait(L + L_PAIR + (w ^ 1) * 4, 1);
    {
        LAS const char* EXR = L + L_EX + (w ^ 1) * 2304 + lane_ * 16;
        const f32x4 e0 = *(LAS const f32x4*)EXR, e1 = *(LAS const f32x4*)(EXR + 1024);
        const float e2 = *(LAS const float*)(L + L_EX + (w ^ 1) * 2304 + 2048 + lane_ * 4);
        SBAR();
        wreads(L_WOB, 0, 4);
        SBAR();
        const float l = ob[8] + e2;
#pragma unroll
        for (int i = 0; i < 4; ++i) { ob[i] += e0[i]; ob[4 + i] += e1[i]; }
        float rl = __builtin_amdgcn_rcpf(l), t = fmx(l, rl) * 1.0e19f;
#pragma unroll
        for (int i = 0; i < 8; i += 2) t = fmx(fmx(t, __builtin_fabsf(ob[i])), __builtin_fabsf(ob[i + 1]));
        if (__builtin_expect(__any(!(t < 1.0e38f)), 0)) {
            ob = f32x16{};
            float m = -INFINITY;
            for (int t2 = 0; t2 < 64; ++t2) {
                const unsigned so = kvb + (t2 >> 1) * BLK, fl = (t2 & 1) * 2048;
                const bf16x8 ones = {0x3F80, 0x3F80, 0x3F80, 0x3F80, 0x3F80, 0x3F80, 0x3F80, 0x3F80};
                const bf16x8 kf = ld16(rs, vK ^ fl, so), v0l = ld16(rs, vV ^ fl, so), v1l = ld16(rs, vV ^ fl, so + 512);
                const bf16x8 v0 = r32_ < 16 ? v0l : ones, v1 = r32_ < 16 ? v1l : ones;
                f32x16 s = MFMA(kf, qb, f32x16{});
                const float mn = fmx(max16(s), m), al = EXP2(m - mn);
                m = mn;
#pragma unroll
                for (int i = 0; i < 16; ++i) { ob[i] *= al; s[i] = EXP2(s[i] - mn); }
                ob = PV(v0, s, 0, ob);
                ob = PV(v1, s, 1, ob);
            }
            rl = __builtin_amdgcn_rcpf(ob[8]);
        }
        rl *= gate;
#pragma unroll
        for (int i = 0; i < 8; ++i) ob[i] *= rl;
        st16(rs, __builtin_bit_cast(bf16x8, __builtin_elementwise_min(__builtin_bit_cast(u32x4, pack8(ob, 0)), (u32x4){0xFFFFFFFEu, 0xFFFFFFFEu, 0xFFFFFFFEu, 0xFFFFFFFEu})),
             lane_ * 16, WS_O + ((2 * tl + kh) * 32 + head) * 1024);
        SBAR();
    }
    if (lane_ == 0) __hip_atomic_fetch_add((LAS unsigned*)(L + L_STORED), 1u, __ATOMIC_RELAXED, __HIP_MEMORY_SCOPE_WORKGROUP);
    f32x16 acc[2];
    f32x4 keep0 = {}, keep1 = {};
    unsigned dst = (64 * otl + 32 * ot + r32_) * 1024 + 128 * ocg + 32 * kq + 16 * h_;
    int src[3];
#pragma unroll
    for (int z = 0; z < 3; ++z) src[z] = L_WOB + ((kq + 1 + z) & 3) * 16384 + ot * 8192 + kq * 2048 + lane_ * 16;
    {
        bf16x8 of[8];
        auto loads = [&](int g) {
#pragma unroll
            for (int k = 4 * g; k < 4 * g + 4; ++k) of[k] = ld16(rs, lane_ * 16 + (k & 3) * 1024, oao + (k & 4) * 1024);
        };
        const bf16x8 none = {-1, -1, -1, -1, -1, -1, -1, -1};
        unsigned rdy = (unsigned)__ballot(pr0 != 0xFFFFFFFFu);
#pragma unroll
        for (int g = 0; g < 2; ++g) { if (((rdy >> (4 * g)) & 15) == 15) loads(g); else of[4 * g] = none; }
        SBAR();
#pragma unroll
        for (int k = 0; k < 8; ++k) {
#pragma unroll
            for (int nt = 0; nt < 2; ++nt) acc[nt] = MFMA(wv[k][nt], ofb[k], k ? acc[nt] : f32x16{});
            SBAR();
            if (k < 4) wreads(L_WOB, k + 4, k + 5); else wreads(L_WOA, k - 4, k - 3);
            SBAR();
        }
        wreads(L_WOA, 4, 8);
        lds_arrive(L + L_PAIR + w * 4, lane_);
        SBAR();
        auto missing = [&](int g) {
            const u32x4 v = __builtin_elementwise_max(__builtin_elementwise_max(__builtin_bit_cast(u32x4, of[4 * g]), __builtin_bit_cast(u32x4, of[4 * g + 1])),
                                                      __builtin_elementwise_max(__builtin_bit_cast(u32x4, of[4 * g + 2]), __builtin_bit_cast(u32x4, of[4 * g + 3])));
            return (bool)__any(__builtin_elementwise_max(__builtin_elementwise_max(v[0], v[1]), __builtin_elementwise_max(v[2], v[3])) == 0xFFFFFFFFu);
        };
        asm volatile("" : "+v"(dst), "+v"(src[0]), "+v"(src[1]), "+v"(src[2]));
        bool have1 = ((rdy >> 4) & 15) == 15;
#pragma unroll
        for (int g = 0; g < 2; ++g) {
#pragma unroll 1
            for (unsigned spins = 0; spins < SPIN_CAP && missing(g); ++spins) {
                rdy = lds_word(L + L_STORED) >= 8 ? 0xFFu : 0u;
                asm volatile("" ::: "memory");
                if (g == 0 && !have1 && ((rdy >> 4) & 15) == 15) { loads(1); have1 = true; }
                if (((rdy >> (4 * g)) & 15) == 15) loads(g); else __builtin_amdgcn_s_sleep(RETRY_SLEEP);
            }
            if (g == 0) {
#pragma unroll
                for (int k = 0; k < 4; ++k) { acc[0] = MFMA(wv[k][0], of[k], acc[0]); acc[1] = MFMA(wv[k][1], of[k], acc[1]); }
                lds_wait(L + L_PAIR + (w ^ 1) * 4, 2);
            }
        }
        LAS char* PR = L + L_WOB + kq * 16384 + ot * 8192 + lane_ * 16;
#pragma unroll
        for (int nt = 0; nt < 2; ++nt)
#pragma unroll
            for (int k = 4; k < 8; ++k) acc[nt] = MFMA(wv[k][nt], of[k], acc[nt]);
        SBAR();
#pragma unroll
        for (int z = 0; z < 4; ++z) {
            const int b = 8 * (z & 1);
            const f32x4 u0 = {acc[z >> 1][b], acc[z >> 1][b + 1], acc[z >> 1][b + 2], acc[z >> 1][b + 3]}, u1 = {acc[z >> 1][b + 4], acc[z >> 1][b + 5], acc[z >> 1][b + 6], acc[z >> 1][b + 7]};
            if (z != kq) { *(LAS f32x4*)(PR + z * 2048) = u0; *(LAS f32x4*)(PR + z * 2048 + 1024) = u1; } else { keep0 = u0; keep1 = u1; }
        }
    }
    BAR();
    {
        f32x4 u[3][2];
#pragma unroll
        for (int z = 0; z < 3; ++z) { u[z][0] = *(LAS const f32x4*)(L + src[z]); u[z][1] = *(LAS const f32x4*)(L + src[z] + 1024); }
        SBAR();
        const f32x4 r0 = ((keep0 + u[0][0]) + u[1][0]) + u[2][0], r1 = ((keep1 + u[0][1]) + u[1][1]) + u[2][1];
        unsigned a0 = pk2(r0[0], r0[1]), a1 = pk2(r0[2], r0[3]), b0 = pk2(r1[0], r1[1]), b1 = pk2(r1[2], r1[3]);
        auto s0_ = __builtin_amdgcn_permlane32_swap(a0, b0, false, false);
        auto s1_ = __builtin_amdgcn_permlane32_swap(a1, b1, false, false);
        *(u32x4*)((char*)out + dst) = (u32x4){s0_[0], s1_[0], s0_[1], s1_[1]};
    }
    if (blockIdx.x == 0 && w == 0 && lane_ == 0) set_flag(ws, WS_EP, tag);
}

extern "C" void kernel_launch(void* const* d_in, const int* in_sizes, int n_in, void* d_out, int out_size, void* d_ws, size_t ws_size, hipStream_t stream) {
    fused_kernel<<<256, 768, 0, stream>>>((const uint16_t*)d_in[0], (const uint16_t*)d_in[1], (const uint16_t*)d_in[4], (const uint16_t*)d_in[5], (const uint16_t*)d_in[7],
                                          (const uint16_t*)d_in[2], (const uint16_t*)d_in[6], (uint16_t*)d_out, (char*)d_ws);
}
```
